```python
import torch

def _softmax(x, axis=-1):
    return torch.softmax(x, dim=axis)
def _gelu(x):
    return torch.nn.functional.gelu(x, approximate="tanh")
def _silu(x):
    return torch.nn.functional.silu(x)
def _sigmoid(x):
    return torch.sigmoid(x)
def _layernorm(x, axis=-1, eps=1e-5):
    m = x.mean(dim=axis, keepdim=True)
    v = x.var(dim=axis, keepdim=True, unbiased=False)
    return (x - m) / torch.sqrt(v + eps)
def _scatter_add(tgt, idx, src, axis=0):
    return tgt.clone().index_add_(axis, idx, src)
def _segment_sum(src, seg, n):
    out = torch.zeros((n,) + src.shape[1:], dtype=src.dtype)
    return out.index_add_(0, seg, src)
def _pad(x, pw):
    flat = tuple(v for pair in reversed(pw) for v in pair)
    return torch.nn.functional.pad(x, flat)

kernel_name = 'standalone__mixer_token_mlp__prompt'

def setup_inputs():

    _w = (lambda a: (lambda t: t.to(torch.bfloat16) if t.is_floating_point() else t)(torch.as_tensor(a)))
    B, S, D, F = 1, 2048, 448, 1792
    return {
        "x": _w((lambda *s: torch.randn(*s, dtype=torch.bfloat16) * 0.05)(B, S, D)),
        "w1": _w((lambda *s: torch.randn(*s, dtype=torch.bfloat16) * 0.2)(S, F) * (S ** -0.5)),
        "b1": _w((lambda *s: torch.randn(*s, dtype=torch.bfloat16) * 3.2)(F) * 0.02),
        "w2": _w((lambda *s: torch.randn(*s, dtype=torch.bfloat16) * 0.05)(F, S) * (F ** -0.5)),
        "b2": _w((lambda *s: torch.randn(*s, dtype=torch.bfloat16) * 0.1)(S) * 0.02),
    }

def reference(x, w1, b1, w2, b2):
    B, S, D = x.shape
    h = _layernorm(x, axis=-1)
    u = torch.einsum("bsd,sf->bdf", h, w1) + b1
    u = _gelu(u)
    y = torch.einsum("bdf,fs->bds", u, w2) + b2
    return (lambda x, *d: x.permute(*d))(y, 0, 2, 1)
```

```cpp
#include <hip/hip_runtime.h>
#include <stdint.h>

#define GAS __attribute__((address_space(1)))
#define LAS __attribute__((address_space(3)))
typedef unsigned v4u __attribute__((ext_vector_type(4)));
typedef unsigned v2u __attribute__((ext_vector_type(2)));
typedef float f32x4 __attribute__((ext_vector_type(4)));
typedef short bf16x8 __attribute__((ext_vector_type(8)));
typedef unsigned short u16;

constexpr int S_ = 2048, D_ = 448, F_ = 1792;
constexpr int MT = D_ / 16;
constexpr int KS1 = S_ / 32;
constexpr int KS2 = F_ / 32;
constexpr size_t H_OFF = 0;
constexpr size_t H_BYTES = (size_t)KS1 * MT * 1024;
constexpr size_t U_OFF = H_OFF + H_BYTES;
constexpr size_t U_BYTES = (size_t)KS2 * MT * 1024;

__device__ __forceinline__ float bf2f(unsigned b) { return __builtin_bit_cast(float, b << 16); }
__device__ __forceinline__ float bflo(unsigned w) { return __builtin_bit_cast(float, w << 16); }
__device__ __forceinline__ float bfhi(unsigned w) { return __builtin_bit_cast(float, w & 0xffff0000u); }
__device__ __forceinline__ unsigned pack2(float lo, float hi) {
  typedef __bf16 bf2 __attribute__((ext_vector_type(2)));
  typedef float f2 __attribute__((ext_vector_type(2)));
  f2 v = {lo, hi};
  bf2 r = __builtin_convertvector(v, bf2);
  return __builtin_bit_cast(unsigned, r);
}
__device__ __forceinline__ f32x4 mfma(v4u a, v4u b, f32x4 c) {
  return __builtin_amdgcn_mfma_f32_16x16x32_bf16(__builtin_bit_cast(bf16x8, a), __builtin_bit_cast(bf16x8, b), c, 0, 0, 0);
}
__device__ __forceinline__ float wave_sum(float v) {
#pragma unroll
  for (int m = 32; m >= 1; m >>= 1) v += __shfl_xor(v, m);
  return v;
}

__global__ void __launch_bounds__(256) k_ln(const u16* __restrict__ x, unsigned char* __restrict__ ws) {
  __shared__ __attribute__((aligned(16))) u16 t[8][D_ + 8];
  const int tid = threadIdx.x, lane = tid & 63, w = tid >> 6;
  const int s0 = blockIdx.x * 8;
  v4u v[2];
#pragma unroll
  for (int r = 0; r < 2; ++r) {
    v[r] = v4u{0, 0, 0, 0};
    if (lane < 56) v[r] = *(const GAS v4u*)(x + (size_t)(s0 + 2 * w + r) * D_ + lane * 8);
  }
#pragma unroll
  for (int r = 0; r < 2; ++r) {
    float f[8];
#pragma unroll
    for (int i = 0; i < 4; ++i) { f[2 * i] = bflo(v[r][i]); f[2 * i + 1] = bfhi(v[r][i]); }
    float s = 0.f;
#pragma unroll
    for (int i = 0; i < 8; ++i) s += f[i];
    s = wave_sum(s);
    const float m = s * (1.0f / D_);
    float q = 0.f;
#pragma unroll
    for (int i = 0; i < 8; ++i) { f[i] -= m; q += f[i] * f[i]; }
    if (lane >= 56) q = 0.f;
    q = wave_sum(q);
    const float rs = __builtin_amdgcn_rsqf(q * (1.0f / D_) + 1e-5f);
    v4u o;
#pragma unroll
    for (int i = 0; i < 4; ++i) o[i] = pack2(f[2 * i] * rs, f[2 * i + 1] * rs);
    if (lane < 56) *(v4u*)&t[2 * w + r][lane * 8] = o;
  }
  __syncthreads();
  const int ks = s0 >> 5, g = (s0 >> 3) & 3;
  for (int d = tid; d < D_; d += 256) {
    v4u o;
#pragma unroll
    for (int i = 0; i < 4; ++i) o[i] = (unsigned)t[2 * i][d] | ((unsigned)t[2 * i + 1][d] << 16);
    const int mt = d >> 4, c = d & 15;
    *(GAS v4u*)(ws + H_OFF + ((size_t)(ks * MT + mt) * 64 + g * 16 + c) * 16) = o;
  }
}

template <int KSW, int LDW, int DEPTH>
__device__ __forceinline__ void gemm_core(const u16* __restrict__ W, const unsigned char* __restrict__ A, int mt0, int n0, int w, int lane,
                                          f32x4 (&acc)[4][4]) {
  const int g = lane >> 4, c = lane & 15;
  const GAS u16* wp = (const GAS u16*)W + (size_t)(w * KSW * 32 + 8 * g) * LDW + n0 + 4 * c;
  const GAS v4u* ap = (const GAS v4u*)A + ((size_t)(w * KSW) * MT + mt0) * 64 + lane;
  v2u wr[DEPTH][8];
  v4u ar[DEPTH][4];
#pragma unroll
  for (int k = 0; k < DEPTH; ++k) {
#pragma unroll
    for (int m = 0; m < 4; ++m) ar[k][m] = ap[(k * MT + m) * 64];
#pragma unroll
    for (int j = 0; j < 8; ++j) wr[k][j] = *(const GAS v2u*)(wp + (size_t)(k * 32 + j) * LDW);
  }
  __builtin_amdgcn_sched_barrier(0);
#pragma unroll
  for (int k = 0; k < KSW; ++k) {
    const int sl = k % DEPTH;
    v4u b[4];
#pragma unroll
    for (int i = 0; i < 4; ++i) {
      b[0][i] = __builtin_amdgcn_perm(wr[sl][2 * i + 1].x, wr[sl][2 * i].x, 0x05040100u);
      b[1][i] = __builtin_amdgcn_perm(wr[sl][2 * i + 1].x, wr[sl][2 * i].x, 0x07060302u);
      b[2][i] = __builtin_amdgcn_perm(wr[sl][2 * i + 1].y, wr[sl][2 * i].y, 0x05040100u);
      b[3][i] = __builtin_amdgcn_perm(wr[sl][2 * i + 1].y, wr[sl][2 * i].y, 0x07060302u);
    }
    v4u a[4];
#pragma unroll
    for (int m = 0; m < 4; ++m) a[m] = ar[sl][m];
    if (k + DEPTH < KSW) {
#pragma unroll
      for (int m = 0; m < 4; ++m) ar[sl][m] = ap[((k + DEPTH) * MT + m) * 64];
#pragma unroll
      for (int j = 0; j < 8; ++j) wr[sl][j] = *(const GAS v2u*)(wp + (size_t)((k + DEPTH) * 32 + j) * LDW);
    }
#pragma unroll
    for (int m = 0; m < 4; ++m)
#pragma unroll
      for (int t = 0; t < 4; ++t) acc[m][t] = mfma(a[m], b[t], acc[m][t]);
    __builtin_amdgcn_sched_barrier(0);
  }
}

__global__ void __launch_bounds__(256, 1) k_gemm1(const u16* __restrict__ w1, const u16* __restrict__ b1, unsigned char* __restrict__ ws) {
  __shared__ __attribute__((aligned(16))) float red[4][64][64];
  const int tid = threadIdx.x, lane = tid & 63, w = __builtin_amdgcn_readfirstlane(tid >> 6);
  const int nb = blockIdx.x % 28, mb = blockIdx.x / 28;
  const int n0 = nb * 64, mt0 = mb * 4;
  f32x4 acc[4][4];
#pragma unroll
  for (int m = 0; m < 4; ++m)
#pragma unroll
    for (int t = 0; t < 4; ++t) acc[m][t] = f32x4{0.f, 0.f, 0.f, 0.f};
  gemm_core<KS1 / 4, F_, 4>(w1, ws + H_OFF, mt0, n0, w, lane, acc);
  const int g = lane >> 4, c = lane & 15;
#pragma unroll
  for (int m = 0; m < 4; ++m)
#pragma unroll
    for (int r = 0; r < 4; ++r) {
      const int d = 16 * m + 4 * g + r;
      f32x4 v = {acc[m][0][r], acc[m][1][r], acc[m][2][r], acc[m][3][r]};
      *(f32x4*)&red[w][d][(c ^ (d & 15)) * 4] = v;
    }
  __syncthreads();
#pragma unroll
  for (int it = 0; it < 2; ++it) {
    const int fr = it * 4 + w;
    const int m = fr & 3, ks = fr >> 2;
    const int d = 16 * m + (lane & 15), ch = 8 * ks + 2 * (lane >> 4);
    f32x4 s0 = {0, 0, 0, 0}, s1 = {0, 0, 0, 0};
#pragma unroll
    for (int ww = 0; ww < 4; ++ww) {
      s0 += *(const f32x4*)&red[ww][d][((ch) ^ (d & 15)) * 4];
      s1 += *(const f32x4*)&red[ww][d][((ch + 1) ^ (d & 15)) * 4];
    }
    const int f = n0 + 32 * ks + 8 * (lane >> 4);
    const v4u bb = *(const GAS v4u*)(b1 + f);
    float z[8] = {s0[0] + bflo(bb[0]), s0[1] + bfhi(bb[0]), s0[2] + bflo(bb[1]), s0[3] + bfhi(bb[1]),
                  s1[0] + bflo(bb[2]), s1[1] + bfhi(bb[2]), s1[2] + bflo(bb[3]), s1[3] + bfhi(bb[3])};
#pragma unroll
    for (int i = 0; i < 8; ++i) {
      const float u = z[i];
      const float e = __builtin_amdgcn_exp2f(u * (-2.3022082f - 0.1029432f * u * u));
      z[i] = u * __builtin_amdgcn_rcpf(1.0f + e);
    }
    v4u o = {pack2(z[0], z[1]), pack2(z[2], z[3]), pack2(z[4], z[5]), pack2(z[6], z[7])};
    *(GAS v4u*)(ws + U_OFF + ((size_t)((f >> 5) * MT + mt0 + m) * 64 + lane) * 16) = o;
  }
}

__global__ void __launch_bounds__(256, 1) k_gemm2(const u16* __restrict__ w2, const u16* __restrict__ b2, const unsigned char* __restrict__ ws,
                                                  u16* __restrict__ out) {
  __shared__ __attribute__((aligned(16))) float red[4][64][64];
  const int tid = threadIdx.x, lane = tid & 63, w = __builtin_amdgcn_readfirstlane(tid >> 6);
  const int nb = blockIdx.x % 32, mb = blockIdx.x / 32;
  const int n0 = nb * 64, mt0 = mb * 4;
  f32x4 acc[4][4];
#pragma unroll
  for (int m = 0; m < 4; ++m)
#pragma unroll
    for (int t = 0; t < 4; ++t) acc[m][t] = f32x4{0.f, 0.f, 0.f, 0.f};
  gemm_core<KS2 / 4, S_, 4>(w2, ws + U_OFF, mt0, n0, w, lane, acc);
  const int g = lane >> 4, c = lane & 15;
#pragma unroll
  for (int m = 0; m < 4; ++m)
#pragma unroll
    for (int t = 0; t < 4; ++t) {
      const int s = 4 * c + t;
      *(f32x4*)&red[w][s][((4 * m + g) ^ (c & 7)) * 4] = acc[m][t];
    }
  __syncthreads();
#pragma unroll
  for (int it = 0; it < 2; ++it) {
    const int s = it * 32 + (tid >> 3), q = tid & 7;
    const int sw = (s >> 2) & 7;
    f32x4 s0 = {0, 0, 0, 0}, s1 = {0, 0, 0, 0};
#pragma unroll
    for (int ww = 0; ww < 4; ++ww) {
      s0 += *(const f32x4*)&red[ww][s][((2 * q) ^ sw) * 4];
      s1 += *(const f32x4*)&red[ww][s][((2 * q + 1) ^ sw) * 4];
    }
    const float bb = bf2f(b2[n0 + s]);
    v4u o = {pack2(s0[0] + bb, s0[1] + bb), pack2(s0[2] + bb, s0[3] + bb), pack2(s1[0] + bb, s1[1] + bb), pack2(s1[2] + bb, s1[3] + bb)};
    *(GAS v4u*)(out + (size_t)(n0 + s) * D_ + mt0 * 16 + q * 8) = o;
  }
}

extern "C" void kernel_launch(void* const* d_in, const int* in_sizes, int n_in, void* d_out, int out_size, void* d_ws, size_t ws_size,
                              hipStream_t stream) {
  const u16* x = (const u16*)d_in[0];
  const u16* w1 = (const u16*)d_in[1];
  const u16* b1 = (const u16*)d_in[2];
  const u16* w2 = (const u16*)d_in[3];
  const u16* b2 = (const u16*)d_in[4];
  unsigned char* ws = (unsigned char*)d_ws;
  k_ln<<<256, 256, 0, stream>>>(x, ws);
  k_gemm1<<<196, 256, 0, stream>>>(w1, b1, ws);
  k_gemm2<<<224, 256, 0, stream>>>(w2, b2, ws, (u16*)d_out);
}
```

```cpp
#include <hip/hip_runtime.h>
#include <stdint.h>
#include <utility>

typedef unsigned v4u __attribute__((ext_vector_type(4)));
typedef unsigned v2u __attribute__((ext_vector_type(2)));
typedef float f32x4 __attribute__((ext_vector_type(4)));
typedef unsigned short u16;
typedef __amdgpu_buffer_rsrc_t srd_t;
#define DI __device__ __forceinline__
#define LAS __attribute__((address_space(3)))
#define GAP() __builtin_amdgcn_sched_barrier(0)
template <int... Is> using iseq = std::integer_sequence<int, Is...>;
template <int N> using make_iseq = std::make_integer_sequence<int, N>;
#define ACLOB "a0","a1","a2","a3","a4","a5","a6","a7","a8","a9","a10","a11","a12","a13","a14","a15","a16","a17","a18","a19","a20","a21","a22","a23","a24","a25","a26","a27","a28","a29","a30","a31","a32","a33","a34","a35","a36","a37","a38","a39","a40","a41","a42","a43","a44","a45","a46","a47","a48","a49","a50","a51","a52","a53","a54","a55","a56","a57","a58","a59","a60","a61","a62","a63","a64","a65","a66","a67","a68","a69","a70","a71","a72","a73","a74","a75","a76","a77","a78","a79","a80","a81","a82","a83","a84","a85","a86","a87","a88","a89","a90","a91","a92","a93","a94","a95","a96","a97","a98","a99","a100","a101","a102","a103","a104","a105","a106","a107","a108","a109","a110","a111","a112","a113","a114","a115","a116","a117","a118","a119","a120","a121","a122","a123","a124","a125","a126","a127","a128","a129","a130","a131","a132","a133","a134","a135","a136","a137","a138","a139","a140","a141","a142","a143","a144","a145","a146","a147","a148","a149","a150","a151","a152","a153","a154","a155","a156","a157","a158","a159","a160","a161","a162","a163","a164","a165","a166","a167","a168","a169","a170","a171","a172","a173","a174","a175","a176","a177","a178","a179","a180","a181","a182","a183","a184","a185","a186","a187","a188","a189","a190","a191","a192","a193","a194","a195","a196","a197","a198","a199","a200","a201","a202","a203","a204","a205","a206","a207","a208","a209","a210","a211","a212","a213","a214","a215","a216","a217","a218","a219","a220","a221","a222","a223","a224","a225","a226","a227","a228","a229","a230","a231","a232","a233","a234","a235","a236","a237","a238","a239","a240","a241","a242","a243","a244","a245","a246","a247","a248","a249","a250","a251","a252","a253","a254","a255"

#define MAPPING 0
#define DEPTH1 5
#define DEPTH2 5
#define R1 16
#define R2 4

constexpr int S_ = 2048, D_ = 448, F_ = 1792;
constexpr int KW1 = 16, KW2 = 14;
constexpr int NSLOT = 5;
constexpr unsigned KSTRIDE = 28u * 1024u;
constexpr unsigned H_OFF = 0, U_OFF = 64u * KSTRIDE;
constexpr unsigned EP_OFF = 4u << 20, HF_OFF = EP_OFF + 4096u, UF_OFF = HF_OFF + 32u * 2048u;
constexpr unsigned WS_USED = UF_OFF + 16u * 28u * 128u;
constexpr unsigned RING_W = NSLOT * 4096u, RED = 4u * RING_W, LDS_BYTES = RED + 65536u;
constexpr unsigned LNP = 960;
constexpr unsigned OOB = 0x7ffffff0u;
constexpr int SPIN_MAX = 1 << 15;

DI unsigned rfl(unsigned x) { return __builtin_amdgcn_readfirstlane(x); }
DI float bflo(unsigned w) { return __builtin_bit_cast(float, w << 16); }
DI float bfhi(unsigned w) { return __builtin_bit_cast(float, w & 0xffff0000u); }
DI unsigned pack2(float lo, float hi) {
  typedef __bf16 bf2 __attribute__((ext_vector_type(2)));
  typedef float f2 __attribute__((ext_vector_type(2)));
  f2 v = {lo, hi};
  return __builtin_bit_cast(unsigned, __builtin_convertvector(v, bf2));
}
DI srd_t mk_srd(const void* p, unsigned bytes) { return __builtin_amdgcn_make_buffer_rsrc((void*)p, 0, bytes, 0x00020000); }
template <int C> DI float dpp_add(float v) {
  return v + __builtin_bit_cast(float, __builtin_amdgcn_update_dpp(0, __builtin_bit_cast(int, v), C, 0xf, 0xf, true));
}
DI float wave_sum(float v) {
  v = dpp_add<0xB1>(v); v = dpp_add<0x4E>(v); v = dpp_add<0x141>(v); v = dpp_add<0x140>(v);
  auto a = __builtin_amdgcn_permlane16_swap(__builtin_bit_cast(unsigned, v), __builtin_bit_cast(unsigned, v), false, false);
  const unsigned a0 = a[0], a1 = a[1];
  v = __builtin_bit_cast(float, a0) + __builtin_bit_cast(float, a1);
  auto c = __builtin_amdgcn_permlane32_swap(__builtin_bit_cast(unsigned, v), __builtin_bit_cast(unsigned, v), false, false);
  const unsigned c0 = c[0], c1 = c[1];
  return __builtin_bit_cast(float, c0) + __builtin_bit_cast(float, c1);
}
template <int N> DI void waitv() { asm volatile("s_waitcnt vmcnt(%0)" ::"i"(N) : "memory"); }
DI void wait_lds() { asm volatile("s_waitcnt lgkmcnt(0)" ::: "memory"); }
DI void wg_barrier() { asm volatile("s_waitcnt lgkmcnt(0)\n\ts_barrier" ::: "memory"); }

struct WStream {
  srd_t srd;
  unsigned sbase;
  unsigned voe, voo;
  unsigned ring;
  unsigned ta[4];
};
template <int LDW, int KSTEP, int I> DI void glds_piece(const WStream& s) {
  unsigned t;
  asm volatile("s_add_u32 m0, %1, %2\n\ts_add_u32 %0, %3, %4\n\tbuffer_load_dwordx4 %5, %6, %0 offen lds"
               : "=&s"(t)
               : "s"(s.ring), "i"((KSTEP % NSLOT) * 4096 + I * 1024), "s"(s.sbase), "i"((KSTEP * 32 + I * 8) * LDW * 2), "v"((I & 1) ? s.voo : s.voe), "s"(s.srd)
               : "scc", "memory");
}
template <int LDW, int KSTEP> DI void glds_slot(const WStream& s) {
  glds_piece<LDW, KSTEP, 0>(s); glds_piece<LDW, KSTEP, 1>(s); glds_piece<LDW, KSTEP, 2>(s); glds_piece<LDW, KSTEP, 3>(s);
}
template <int KSTEP, int T, int H> DI void tr_piece(const WStream& s) {
  asm volatile("ds_read_b64_tr_b16 a[%c1:%c2], %0 offset:%c3" ::"v"(s.ta[T]), "i"(16 * KSTEP + 4 * T + 2 * H), "i"(16 * KSTEP + 4 * T + 2 * H + 1),
               "i"((KSTEP % NSLOT) * 4096 + H * 512) : "memory", ACLOB);
}
template <int KSTEP> DI void tr_slot(const WStream& s) {
  tr_piece<KSTEP, 0, 0>(s); tr_piece<KSTEP, 0, 1>(s); tr_piece<KSTEP, 1, 0>(s); tr_piece<KSTEP, 1, 1>(s);
  tr_piece<KSTEP, 2, 0>(s); tr_piece<KSTEP, 2, 1>(s); tr_piece<KSTEP, 3, 0>(s); tr_piece<KSTEP, 3, 1>(s);
}
template <int LDW, int KW, int J, int EXTRA> DI void wpipe_step(const WStream& s) {
  constexpr int rem = KW - 1 - J;
  waitv<4 * (rem < NSLOT - 1 ? rem : NSLOT - 1) + EXTRA>();
  tr_slot<J>(s);
  wait_lds();
  if constexpr (J + NSLOT < KW) glds_slot<LDW, J + NSLOT>(s);
  GAP();
}
template <int LDW, int KW, int J0, int EXTRA, int... Js> DI void wpipe_steps(const WStream& s, iseq<Js...>) { (wpipe_step<LDW, KW, J0 + Js, EXTRA>(s), ...); }
template <int LDW, int... Js> DI void glds_slots(const WStream& s, iseq<Js...>) { (glds_slot<LDW, Js>(s), ...); }

template <int IMM> DI void act_load(v4u& d, unsigned voff, srd_t srd, unsigned soff) {
  asm volatile("buffer_load_dwordx4 %0, %1, %2, %3 offen offset:%c4 sc1" : "=v"(d) : "v"(voff), "s"(srd), "s"(soff), "i"(IMM) : "memory");
}
DI void sadd(unsigned& s, unsigned inc) { asm volatile("s_add_u32 %0, %0, %1" : "+s"(s) : "s"(inc) : "scc"); }
template <bool WA, bool FIRST, int AR> DI void mfma(f32x4& acc, const v4u& act) {
  if constexpr (WA) {
    if constexpr (FIRST) asm volatile("v_mfma_f32_16x16x32_bf16 %0, a[%c2:%c3], %1, 0" : "=&v"(acc) : "v"(act), "i"(AR), "i"(AR + 3) : ACLOB);
    else asm volatile("v_mfma_f32_16x16x32_bf16 %0, a[%c2:%c3], %1, %0" : "+v"(acc) : "v"(act), "i"(AR), "i"(AR + 3) : ACLOB);
  } else {
    if constexpr (FIRST) asm volatile("v_mfma_f32_16x16x32_bf16 %0, %1, a[%c2:%c3], 0" : "=&v"(acc) : "v"(act), "i"(AR), "i"(AR + 3) : ACLOB);
    else asm volatile("v_mfma_f32_16x16x32_bf16 %0, %1, a[%c2:%c3], %0" : "+v"(acc) : "v"(act), "i"(AR), "i"(AR + 3) : ACLOB);
  }
}
template <int N> DI void wait_tie(v4u& r) { asm volatile("s_waitcnt vmcnt(%1)" : "+v"(r) : "i"(N) : "memory"); }

template <bool G1, int K, int M> DI void gemm_group(f32x4 (&acc)[4][4], v4u (&ring)[G1 ? DEPTH1 : DEPTH2][4], unsigned voff, srd_t srd, unsigned& soff, const WStream& w2) {
  constexpr int KW = G1 ? KW1 : KW2, DEPTH = G1 ? DEPTH1 : DEPTH2, sl = K % DEPTH;
  constexpr bool more = K + DEPTH < KW;
  constexpr int tail = K + DEPTH - KW;
  constexpr bool pf = G1 && !more && tail < NSLOT;
  constexpr int later_act = (KW - 1 - K < DEPTH - 1 ? KW - 1 - K : DEPTH - 1) * 4;
  constexpr int tails_before = tail > 0 ? (G1 ? (tail < NSLOT ? tail : NSLOT) * 4 : 0) : 0;
  constexpr int mine = (more || pf) ? (M >= 1 ? M - 1 : 0) : 0;
  wait_tie<(3 - M) + later_act + tails_before + mine>(ring[sl][M]);
  mfma<G1, K == 0, 16 * K + 0>(acc[M][0], ring[sl][M]);
  if constexpr (M >= 1) {
    if constexpr (more) act_load<(M - 1) * 1024>(ring[sl][M - 1], voff, srd, soff);
    else if constexpr (pf) glds_piece<S_, tail, M - 1>(w2);
  }
  mfma<G1, K == 0, 16 * K + 4>(acc[M][1], ring[sl][M]);
  mfma<G1, K == 0, 16 * K + 8>(acc[M][2], ring[sl][M]);
  mfma<G1, K == 0, 16 * K + 12>(acc[M][3], ring[sl][M]);
  if constexpr (M == 3) {
    if constexpr (more) { act_load<3072>(ring[sl][3], voff, srd, soff); sadd(soff, KSTRIDE); }
    else if constexpr (pf) glds_piece<S_, tail, 3>(w2);
  }
  GAP();
}
template <bool G1, int K> DI void gemm_step(f32x4 (&acc)[4][4], v4u (&ring)[G1 ? DEPTH1 : DEPTH2][4], unsigned voff, srd_t srd, unsigned& soff, const WStream& w2) {
  gemm_group<G1, K, 0>(acc, ring, voff, srd, soff, w2); gemm_group<G1, K, 1>(acc, ring, voff, srd, soff, w2);
  gemm_group<G1, K, 2>(acc, ring, voff, srd, soff, w2); gemm_group<G1, K, 3>(acc, ring, voff, srd, soff, w2);
}
template <bool G1, int... Ks> DI void gemm_steps(f32x4 (&acc)[4][4], v4u (&ring)[G1 ? DEPTH1 : DEPTH2][4], unsigned voff, srd_t srd, unsigned& soff, const WStream& w2, iseq<Ks...>) {
  (gemm_step<G1, Ks>(acc, ring, voff, srd, soff, w2), ...);
}
template <bool G1> DI void gemm_run(f32x4 (&acc)[4][4], unsigned voff, srd_t srd, unsigned soff, const WStream& w2) {
  constexpr int DEPTH = G1 ? DEPTH1 : DEPTH2;
  v4u ring[DEPTH][4];
#pragma unroll
  for (int k = 0; k < DEPTH; ++k) {
    act_load<0>(ring[k][0], voff, srd, soff); act_load<1024>(ring[k][1], voff, srd, soff);
    act_load<2048>(ring[k][2], voff, srd, soff); act_load<3072>(ring[k][3], voff, srd, soff);
    sadd(soff, KSTRIDE);
  }
  GAP();
  gemm_steps<G1>(acc, ring, voff, srd, soff, w2, make_iseq<G1 ? KW1 : KW2>{});
  asm volatile("s_nop 15\n\ts_nop 7" : "+v"(acc[3][0]), "+v"(acc[3][1]), "+v"(acc[3][2]), "+v"(acc[3][3]));
  GAP();
}
DI void poll_flags(unsigned voff, srd_t srd, unsigned soff, unsigned tlo, unsigned thi) {
  for (int spin = 0; spin < SPIN_MAX; ++spin) {
    v2u f;
    asm volatile("buffer_load_dwordx2 %0, %1, %2, %3 offen sc1\n\ts_waitcnt vmcnt(0)" : "=v"(f) : "v"(voff), "s"(srd), "s"(soff) : "memory");
    if (__builtin_amdgcn_ballot_w64(f[0] != tlo || f[1] != thi) == 0) break;
    __builtin_amdgcn_s_sleep(2);
  }
}
DI void store16_sc1(v4u d, unsigned voff, srd_t srd, unsigned soff) {
  asm volatile("buffer_store_dwordx4 %0, %1, %2, %3 offen sc1\n\ts_nop 1" ::"v"(d), "v"(voff), "s"(srd), "s"(soff) : "memory");
}
DI void store8_sc1(v2u d, unsigned voff, srd_t srd, unsigned soff) {
  asm volatile("buffer_store_dwordx2 %0, %1, %2, %3 offen sc1\n\ts_nop 1" ::"v"(d), "v"(voff), "s"(srd), "s"(soff) : "memory");
}
DI float gelu(float u) {
  const float e = __builtin_amdgcn_exp2f(u * (-2.3022082f - 0.1029432f * u * u));
  return u * __builtin_amdgcn_rcpf(1.0f + e);
}
#define STAMP(i)

__global__ void __launch_bounds__(256, 1)
mixer_kernel(const u16* __restrict__ x, const u16* __restrict__ w1, const u16* __restrict__ b1, const u16* __restrict__ w2, const u16* __restrict__ b2,
             u16* __restrict__ out, unsigned char* __restrict__ ws) {
  __shared__ __attribute__((aligned(1024))) unsigned char smem[LDS_BYTES];
  STAMP(0);
  asm volatile("" ::: ACLOB);
  const unsigned tid = threadIdx.x, lane = tid & 63, w = rfl(tid >> 6), b = blockIdx.x;
  const unsigned lds0 = (unsigned)(uintptr_t)(LAS unsigned char*)smem;
  const unsigned xcd = b & 7, li = b >> 3;
  bool g1, g2;
  unsigned mb1, nb1, mb2, nb2;
  {
    const unsigned n1 = xcd < 4 ? 25 : 24, start = 24 * xcd + (xcd < 4 ? xcd : 4), i0 = 32 - n1;
    g1 = li >= i0;
    const unsigned T = g1 ? start + li - i0 : 0;
    nb1 = T / 7; mb1 = T % 7;
    g2 = li < 28; nb2 = 4 * xcd + (li & 3); mb2 = g2 ? li >> 2 : 0;
  }
  const srd_t srdWS = mk_srd(ws, WS_USED);
  const unsigned g = lane >> 4, c = lane & 15;

  v2u ep; v4u xv[2], bv1[2]; unsigned bv2[2];
  {
    const srd_t srdX = mk_srd(x, S_ * D_ * 2), srdB1 = mk_srd(b1, F_ * 2), srdB2 = mk_srd(b2, S_ * 2);
    const unsigned zero = 0, vx = lane < 56 ? lane * 16 : OOB, row = 8 * b + 2 * w;
    asm volatile("buffer_load_dwordx2 %0, %1, %2, %3 offen sc1" : "=v"(ep) : "v"(zero), "s"(srdWS), "s"(EP_OFF) : "memory");
    asm volatile("buffer_load_dwordx4 %0, %1, %2, %3 offen" : "=v"(xv[0]) : "v"(vx), "s"(srdX), "s"(row * 896u) : "memory");
    asm volatile("buffer_load_dwordx4 %0, %1, %2, %3 offen offset:896" : "=v"(xv[1]) : "v"(vx), "s"(srdX), "s"(row * 896u) : "memory");
    asm volatile("buffer_load_dwordx4 %0, %1, %2, %3 offen" : "=v"(bv1[0]) : "v"(g * 16), "s"(srdB1), "s"(nb1 * 128u) : "memory");
    asm volatile("buffer_load_dwordx4 %0, %1, %2, %3 offen offset:64" : "=v"(bv1[1]) : "v"(g * 16), "s"(srdB1), "s"(nb1 * 128u) : "memory");
    const unsigned vb2 = (16 * w + (lane >> 3)) * 2;
    asm volatile("buffer_load_ushort %0, %1, %2, %3 offen" : "=v"(bv2[0]) : "v"(vb2), "s"(srdB2), "s"(nb2 * 128u) : "memory");
    asm volatile("buffer_load_ushort %0, %1, %2, %3 offen offset:16" : "=v"(bv2[1]) : "v"(vb2), "s"(srdB2), "s"(nb2 * 128u) : "memory");
  }
  WStream W1, W2;
  {
    const unsigned r8 = lane >> 3, pc = lane & 7, s0 = (r8 >> 1) & 1;
    const unsigned q = (lane >> 2) & 3, p = lane & 3, sr = ((q >> 1) & 1) | ((g & 1) << 1);
    const unsigned ring = lds0 + w * RING_W;
    const unsigned t0 = ring + (8 * g + q) * 128 + sr * 32 + p * 8;
    W1.srd = mk_srd(w1, S_ * F_ * 2); W2.srd = mk_srd(w2, g2 ? F_ * S_ * 2 : 0);
    W1.sbase = w * (KW1 * 32) * F_ * 2 + nb1 * 128; W2.sbase = w * (KW2 * 32) * S_ * 2 + nb2 * 128;
    W1.voe = r8 * F_ * 2 + ((pc ^ (2 * s0)) << 4); W1.voo = r8 * F_ * 2 + ((pc ^ (2 * (s0 | 2))) << 4);
    W2.voe = r8 * S_ * 2 + ((pc ^ (2 * s0)) << 4); W2.voo = r8 * S_ * 2 + ((pc ^ (2 * (s0 | 2))) << 4);
    W1.ring = W2.ring = ring;
#pragma unroll
    for (int t = 0; t < 4; ++t) W1.ta[t] = W2.ta[t] = t0 ^ (t << 5);
  }
  if (w != 0) {
    if (g1) glds_slots<F_>(W1, make_iseq<NSLOT>{}); else glds_slots<S_>(W2, make_iseq<NSLOT>{});
  } else {
    waitv<0>();
  }
  asm volatile("s_waitcnt vmcnt(%7)" : "+v"(ep), "+v"(xv[0]), "+v"(xv[1]), "+v"(bv1[0]), "+v"(bv1[1]), "+v"(bv2[0]), "+v"(bv2[1]) : "i"(4 * NSLOT) : "memory");
  GAP();
  STAMP(1);
  unsigned tlo, thi;
  {
    const unsigned long long e = (((unsigned long long)ep[1] << 32) | ep[0]) + 1ull;
    tlo = rfl((unsigned)e); thi = rfl((unsigned)(e >> 32));
  }
#pragma unroll
  for (int r = 0; r < 2; ++r) {
    float f[8];
#pragma unroll
    for (int i = 0; i < 4; ++i) { f[2 * i] = bflo(xv[r][i]); f[2 * i + 1] = bfhi(xv[r][i]); }
    const float m = wave_sum(((f[0] + f[1]) + (f[2] + f[3])) + ((f[4] + f[5]) + (f[6] + f[7]))) * (1.0f / D_);
    float qs = 0.f;
#pragma unroll
    for (int i = 0; i < 8; ++i) { f[i] -= m; qs += f[i] * f[i]; }
    qs = wave_sum(lane < 56 ? qs : 0.f);
    const float rs = __builtin_amdgcn_rsqf(qs * (1.0f / D_) + 1e-5f);
    const v4u o = {pack2(f[0] * rs, f[1] * rs), pack2(f[2] * rs, f[3] * rs), pack2(f[4] * rs, f[5] * rs), pack2(f[6] * rs, f[7] * rs)};
    if (lane < 56) *(LAS v4u*)(smem + RED + (2 * w + r) * LNP + lane * 16) = o;
  }
  wg_barrier();
  if (w == 0) {
    v2u lo[7], hi[7];
    const unsigned ta = lds0 + RED + ((lane >> 2) & 3) * LNP + g * 32 + (lane & 3) * 8;
#pragma unroll
    for (int it = 0; it < 7; ++it) {
      asm volatile("ds_read_b64_tr_b16 %0, %1 offset:%c2" : "=v"(lo[it]) : "v"(ta), "i"(it * 128) : "memory");
      asm volatile("ds_read_b64_tr_b16 %0, %1 offset:%c2" : "=v"(hi[it]) : "v"(ta), "i"(it * 128 + 4 * LNP) : "memory");
    }
    asm volatile("s_waitcnt lgkmcnt(0)" : "+v"(lo[0]), "+v"(lo[1]), "+v"(lo[2]), "+v"(lo[3]), "+v"(lo[4]), "+v"(lo[5]), "+v"(lo[6]),
                 "+v"(hi[0]), "+v"(hi[1]), "+v"(hi[2]), "+v"(hi[3]), "+v"(hi[4]), "+v"(hi[5]), "+v"(hi[6])::"memory");
    const unsigned vo = g * 1024 + c * 16 + (b & 3) * 256;
    unsigned so = H_OFF + (b >> 2) * KSTRIDE;
#pragma unroll
    for (int it = 0; it < 7; ++it) { store16_sc1(v4u{lo[it][0], lo[it][1], hi[it][0], hi[it][1]}, vo, srdWS, so); sadd(so, 4096); }
    if (g1) glds_slots<F_>(W1, make_iseq<NSLOT>{}); else glds_slots<S_>(W2, make_iseq<NSLOT>{});
    waitv<4 * NSLOT>();
    store8_sc1(v2u{tlo, thi}, lane < R1 ? lane * 2048 + b * 8 : OOB, srdWS, HF_OFF);
  }
  STAMP(2);
  f32x4 acc[4][4];
  if (g1) {
    wpipe_steps<F_, KW1, 0, 0>(W1, make_iseq<KW1>{});
    STAMP(3);
    poll_flags((64 * w + lane) * 8, srdWS, HF_OFF + (b % R1) * 2048, tlo, thi);
    STAMP(4);
    gemm_run<true>(acc, lane * 16, srdWS, H_OFF + (w * KW1 * 28 + 4 * mb1) * 1024, W2);
    STAMP(5);
#pragma unroll
    for (int m = 0; m < 4; ++m)
#pragma unroll
      for (int t = 0; t < 4; ++t) *(LAS f32x4*)(smem + RED + w * 16384 + (16 * m + c) * 256 + (((4 * t + g) ^ c) << 4)) = acc[m][t];
    wg_barrier();
    if (b == (MAPPING == 0 ? 56u : 32u) && w == 1) store8_sc1(v2u{tlo, thi}, lane == 0 ? 0 : OOB, srdWS, EP_OFF);
#pragma unroll
    for (int kk = 0; kk < 2; ++kk) {
      f32x4 s0 = {0, 0, 0, 0}, s1 = {0, 0, 0, 0};
#pragma unroll
      for (int ww = 0; ww < 4; ++ww) {
        s0 += *(const LAS f32x4*)(smem + RED + ww * 16384 + (16 * w + c) * 256 + (((8 * kk + 2 * g) ^ c) << 4));
        s1 += *(const LAS f32x4*)(smem + RED + ww * 16384 + (16 * w + c) * 256 + (((8 * kk + 2 * g + 1) ^ c) << 4));
      }
      const v4u bb = bv1[kk];
      const v4u o = {pack2(gelu(s0[0] + bflo(bb[0])), gelu(s0[1] + bfhi(bb[0]))), pack2(gelu(s0[2] + bflo(bb[1])), gelu(s0[3] + bfhi(bb[1]))),
                     pack2(gelu(s1[0] + bflo(bb[2])), gelu(s1[1] + bfhi(bb[2]))), pack2(gelu(s1[2] + bflo(bb[3])), gelu(s1[3] + bfhi(bb[3])))};
      store16_sc1(o, lane * 16, srdWS, U_OFF + ((2 * nb1 + kk) * 28 + 4 * mb1 + w) * 1024);
    }
    GAP();
    wpipe_steps<S_, KW2, 0, 2>(W2, make_iseq<NSLOT>{});
    waitv<4 * NSLOT>();
    wg_barrier();
    if (w == 0) store8_sc1(v2u{tlo, thi}, lane < R2 ? lane * (28 * 128) + (mb1 * 4 + nb1 / 7) * 128 + (nb1 % 7) * 8 : OOB, srdWS, UF_OFF);
    STAMP(6);
    if (!g2) goto done;
    wpipe_steps<S_, KW2, NSLOT, 0>(W2, make_iseq<KW2 - NSLOT>{});
  } else {
    if (!g2) goto done;
    wpipe_steps<S_, KW2, 0, 0>(W2, make_iseq<KW2>{});
  }
  {
    poll_flags((c < 7 ? c : 6) * 8, srdWS, UF_OFF + (nb2 % R2) * (28 * 128) + (mb2 * 4 + w) * 128, tlo, thi);
    gemm_run<false>(acc, lane * 16, srdWS, U_OFF + (w * KW2 * 28 + 4 * mb2) * 1024, W2);
#pragma unroll
    for (int m = 0; m < 4; ++m)
#pragma unroll
      for (int t = 0; t < 4; ++t) *(LAS f32x4*)(smem + RED + w * 16384 + (16 * t + c) * 256 + (((4 * m + g) ^ c) << 4)) = acc[m][t];
    wg_barrier();
    const srd_t srdO = mk_srd(out, S_ * D_ * 2);
#pragma unroll
    for (int it = 0; it < 2; ++it) {
      const unsigned s = 16 * w + 8 * it + (lane >> 3), q = lane & 7;
      f32x4 s0 = {0, 0, 0, 0}, s1 = {0, 0, 0, 0};
#pragma unroll
      for (int ww = 0; ww < 4; ++ww) {
        s0 += *(const LAS f32x4*)(smem + RED + ww * 16384 + s * 256 + (((2 * q) ^ (s & 15)) << 4));
        s1 += *(const LAS f32x4*)(smem + RED + ww * 16384 + s * 256 + (((2 * q + 1) ^ (s & 15)) << 4));
      }
      const float bb = bflo(bv2[it]);
      const v4u o = {pack2(s0[0] + bb, s0[1] + bb), pack2(s0[2] + bb, s0[3] + bb), pack2(s1[0] + bb, s1[1] + bb), pack2(s1[2] + bb, s1[3] + bb)};
      asm volatile("buffer_store_dwordx4 %0, %1, %2, %3 offen\n\ts_nop 1" ::"v"(o), "v"(s * (D_ * 2) + q * 16), "s"(srdO), "s"(nb2 * 64 * (D_ * 2) + mb2 * 128) : "memory");
    }
  }
done:
  STAMP(7);
}

extern "C" void kernel_launch(void* const* d_in, const int* in_sizes, int n_in, void* d_out, int out_size, void* d_ws, size_t ws_size, hipStream_t stream) {
  mixer_kernel<<<256, 256, 0, stream>>>((const u16*)d_in[0], (const u16*)d_in[1], (const u16*)d_in[2], (const u16*)d_in[3], (const u16*)d_in[4],
                                       (u16*)d_out, (unsigned char*)d_ws);
}
```

```cpp
#include <hip/hip_runtime.h>
#include <stdint.h>
#include <utility>

typedef unsigned v4u __attribute__((ext_vector_type(4)));
typedef unsigned v2u __attribute__((ext_vector_type(2)));
typedef float f32x4 __attribute__((ext_vector_type(4)));
typedef unsigned short u16;
typedef __amdgpu_buffer_rsrc_t srd_t;
#define DI __device__ __forceinline__
#define LAS __attribute__((address_space(3)))
#define GAP() __builtin_amdgcn_sched_barrier(0)
template <int... Is> using iseq = std::integer_sequence<int, Is...>;
template <int N> using make_iseq = std::make_integer_sequence<int, N>;
#define ACLOB "a0","a1","a2","a3","a4","a5","a6","a7","a8","a9","a10","a11","a12","a13","a14","a15","a16","a17","a18","a19","a20","a21","a22","a23","a24","a25","a26","a27","a28","a29","a30","a31","a32","a33","a34","a35","a36","a37","a38","a39","a40","a41","a42","a43","a44","a45","a46","a47","a48","a49","a50","a51","a52","a53","a54","a55","a56","a57","a58","a59","a60","a61","a62","a63","a64","a65","a66","a67","a68","a69","a70","a71","a72","a73","a74","a75","a76","a77","a78","a79","a80","a81","a82","a83","a84","a85","a86","a87","a88","a89","a90","a91","a92","a93","a94","a95","a96","a97","a98","a99","a100","a101","a102","a103","a104","a105","a106","a107","a108","a109","a110","a111","a112","a113","a114","a115","a116","a117","a118","a119","a120","a121","a122","a123","a124","a125","a126","a127","a128","a129","a130","a131","a132","a133","a134","a135","a136","a137","a138","a139","a140","a141","a142","a143","a144","a145","a146","a147","a148","a149","a150","a151","a152","a153","a154","a155","a156","a157","a158","a159","a160","a161","a162","a163","a164","a165","a166","a167","a168","a169","a170","a171","a172","a173","a174","a175","a176","a177","a178","a179","a180","a181","a182","a183","a184","a185","a186","a187","a188","a189","a190","a191","a192","a193","a194","a195","a196","a197","a198","a199","a200","a201","a202","a203","a204","a205","a206","a207","a208","a209","a210","a211","a212","a213","a214","a215","a216","a217","a218","a219","a220","a221","a222","a223","a224","a225","a226","a227","a228","a229","a230","a231","a232","a233","a234","a235","a236","a237","a238","a239","a240","a241","a242","a243","a244","a245","a246","a247","a248","a249","a250","a251","a252","a253","a254","a255"

#define MAPPING 0
#define DEPTH1 7
#define DEPTH2 7
#define PRE 3
#define MID 0
#define NA 3
#define POST 0
#define POST2 7
#define ROT1 1
#define ROT2 1
#define IDLE_SLEEP 0
#define PSLEEP 2
#define QUIET 1
#define R1 4
#define R2 4

constexpr int S_ = 2048, D_ = 448, F_ = 1792;
constexpr int KW1 = 16, KW2 = 14;
constexpr int NS1 = 9, NS2 = 7;
constexpr int TPF = DEPTH1 < 6 ? DEPTH1 : 6;
constexpr unsigned KSTRIDE = 28u * 1024u;
constexpr unsigned H_OFF = 0, U_OFF = 64u * KSTRIDE;
constexpr unsigned EP_OFF = 4u << 20, HF_OFF = EP_OFF + 4096u, UF_OFF = HF_OFF + 16u * 8192u;
constexpr unsigned UF_REP = 28u * 256u;
constexpr unsigned WS_USED = UF_OFF + 16u * UF_REP;
constexpr unsigned REG_W = 40960, LN_OFF = 36864, LNP = 960, W2_OFF = 12288, LDS_BYTES = 4 * REG_W;
constexpr unsigned OOB = 0x7ffffff0u;
constexpr int SPIN_MAX = 1 << 15;
constexpr unsigned EPOCH_WG = 225;

DI unsigned rfl(unsigned x) { return __builtin_amdgcn_readfirstlane(x); }
DI float bflo(unsigned w) { return __builtin_bit_cast(float, w << 16); }
DI float bfhi(unsigned w) { return __builtin_bit_cast(float, w & 0xffff0000u); }
DI unsigned pack2(float lo, float hi) {
  typedef __bf16 bf2 __attribute__((ext_vector_type(2)));
  typedef float f2 __attribute__((ext_vector_type(2)));
  f2 v = {lo, hi};
  return __builtin_bit_cast(unsigned, __builtin_convertvector(v, bf2));
}
DI srd_t mk_srd(const void* p, unsigned bytes) { return __builtin_amdgcn_make_buffer_rsrc((void*)p, 0, bytes, 0x00020000); }
template <int C> DI float dpp_add(float v) {
  return v + __builtin_bit_cast(float, __builtin_amdgcn_update_dpp(0, __builtin_bit_cast(int, v), C, 0xf, 0xf, true));
}
DI void wave_sum2(float& a, float& b) {
  a = dpp_add<0xB1>(a); b = dpp_add<0xB1>(b); a = dpp_add<0x4E>(a); b = dpp_add<0x4E>(b);
  a = dpp_add<0x141>(a); b = dpp_add<0x141>(b); a = dpp_add<0x140>(a); b = dpp_add<0x140>(b);
  auto p = __builtin_amdgcn_permlane16_swap(__builtin_bit_cast(unsigned, a), __builtin_bit_cast(unsigned, b), false, false);
  const unsigned p0 = p[0], p1 = p[1];
  float s = __builtin_bit_cast(float, p0) + __builtin_bit_cast(float, p1);
  auto q = __builtin_amdgcn_permlane32_swap(__builtin_bit_cast(unsigned, s), __builtin_bit_cast(unsigned, s), false, false);
  const unsigned q0 = q[0], q1 = q[1];
  s = __builtin_bit_cast(float, q0) + __builtin_bit_cast(float, q1);
  a = __builtin_bit_cast(float, __builtin_amdgcn_readlane(__builtin_bit_cast(int, s), 0));
  b = __builtin_bit_cast(float, __builtin_amdgcn_readlane(__builtin_bit_cast(int, s), 16));
}
template <int N> DI void waitv() { static_assert(N >= 0 && N < 64, "vmcnt"); asm volatile("s_waitcnt vmcnt(%0)" ::"i"(N) : "memory"); }
DI void wait_lds() { asm volatile("s_waitcnt lgkmcnt(0)" ::: "memory"); }
DI void wg_barrier() { asm volatile("s_waitcnt lgkmcnt(0)\n\ts_barrier" ::: "memory"); }

struct WStream {
  srd_t srd;
  unsigned cur, beg, end;
  unsigned vo;
  unsigned ring;
  unsigned ta[4];
};
DI void cyc_add(unsigned& cur, unsigned inc, unsigned beg, unsigned end) {
  asm volatile("s_add_u32 %0, %0, %1\n\ts_cmp_eq_u32 %0, %3\n\ts_cselect_b32 %0, %2, %0" : "+s"(cur) : "s"(inc), "s"(beg), "s"(end) : "scc");
}
template <bool W1> struct WP { static constexpr int LDW = W1 ? F_ : S_, KW = W1 ? KW1 : KW2, NS = W1 ? NS1 : NS2, OFF = W1 ? 0 : (int)W2_OFF; };
template <bool W1, int KSTEP, int I> DI void glds_piece(WStream& s) {
  using P = WP<W1>;
  unsigned t;
  asm volatile("s_add_u32 m0, %1, %2\n\ts_add_u32 %0, %3, %4\n\tbuffer_load_dwordx4 %5, %6, %0 offen lds"
               : "=&s"(t)
               : "s"(s.ring), "i"(P::OFF + (KSTEP % P::NS) * 4096 + I * 1024), "s"(s.cur), "i"(I * 8 * P::LDW * 2), "v"(s.vo), "s"(s.srd)
               : "scc", "memory");
  if constexpr (I == 3) cyc_add(s.cur, 32 * P::LDW * 2, s.beg, s.end);
}
template <bool W1, int KSTEP> DI void glds_slot(WStream& s) {
  glds_piece<W1, KSTEP, 0>(s); glds_piece<W1, KSTEP, 1>(s); glds_piece<W1, KSTEP, 2>(s); glds_piece<W1, KSTEP, 3>(s);
}
template <bool W1, int KSTEP, int T, int H> DI void tr_piece(const WStream& s) {
  using P = WP<W1>;
  asm volatile("ds_read_b64_tr_b16 a[%c1:%c2], %0 offset:%c3" ::"v"(s.ta[T]), "i"(16 * KSTEP + 4 * T + 2 * H), "i"(16 * KSTEP + 4 * T + 2 * H + 1),
               "i"(P::OFF + (KSTEP % P::NS) * 4096 + H * 2048) : "memory", ACLOB);
}
template <bool W1, int KSTEP> DI void tr_slot(const WStream& s) {
  tr_piece<W1, KSTEP, 0, 0>(s); tr_piece<W1, KSTEP, 0, 1>(s); tr_piece<W1, KSTEP, 1, 0>(s); tr_piece<W1, KSTEP, 1, 1>(s);
  tr_piece<W1, KSTEP, 2, 0>(s); tr_piece<W1, KSTEP, 2, 1>(s); tr_piece<W1, KSTEP, 3, 0>(s); tr_piece<W1, KSTEP, 3, 1>(s);
}
constexpr int NB1 = PRE + MID;
static_assert(NB1 + NA <= NS1 && (POST == 0 || NB1 + NA == NS1) && POST <= NB1 && POST <= KW1 - NS1 && POST2 <= NS2, "schedule");
template <bool W1> constexpr int younger(int j, bool flagged) {
  constexpr int KW = W1 ? KW1 : KW2, NS = W1 ? NS1 : NS2;
  const int before_stores = W1 ? NB1 : NS2, before_flag = W1 ? (NB1 + NA == NS1 ? NS1 + POST : NB1 + NA) : NS2 + POST2;
  const int last = j + NS - 1 < KW - 1 ? j + NS - 1 : KW - 1;
  return 4 * (last - j) + (j < before_stores ? 2 : 0) + (flagged && j < before_flag ? 1 : 0);
}
template <bool W1, int J, int MODE> DI void wpipe_step(WStream& s) {
  using P = WP<W1>;
  constexpr int rem = P::KW - 1 - J;
  waitv<MODE == 0 ? 4 * (rem < P::NS - 1 ? rem : P::NS - 1) : younger<W1>(J, MODE == 2)>();
  tr_slot<W1, J>(s);
  if constexpr (J + P::NS < P::KW) { wait_lds(); glds_slot<W1, J + P::NS>(s); }
  GAP();
}
template <bool W1, int J0, int MODE, int... Js> DI void wpipe_steps(WStream& s, iseq<Js...>) { (wpipe_step<W1, J0 + Js, MODE>(s), ...); }
template <bool W1, int J0, int... Js> DI void glds_slots(WStream& s, iseq<Js...>) { (glds_slot<W1, J0 + Js>(s), ...); }

template <int IMM> DI void act_load(v4u& d, unsigned voff, srd_t srd, unsigned soff) {
  asm volatile("buffer_load_dwordx4 %0, %1, %2, %3 offen offset:%c4 sc1" : "=v"(d) : "v"(voff), "s"(srd), "s"(soff), "i"(IMM) : "memory");
}
struct AStream { srd_t srd; unsigned voff[4], cur, beg, end; };
template <bool G1, int M> DI void act_frag(v4u& d, const AStream& a) {
  if constexpr (G1) act_load<0>(d, a.voff[M], a.srd, a.cur); else act_load<M * 1024>(d, a.voff[0], a.srd, a.cur);
}
DI void act_next(AStream& a) { cyc_add(a.cur, KSTRIDE, a.beg, a.end); }
template <bool WA, bool ZERO, int AR> DI void mfma(f32x4& acc, const v4u& act) {
  if constexpr (WA) {
    if constexpr (ZERO) asm volatile("v_mfma_f32_16x16x32_bf16 %0, a[%c2:%c3], %1, 0" : "=&v"(acc) : "v"(act), "i"(AR), "i"(AR + 3) : ACLOB);
    else asm volatile("v_mfma_f32_16x16x32_bf16 %0, a[%c2:%c3], %1, %0" : "+v"(acc) : "v"(act), "i"(AR), "i"(AR + 3) : ACLOB);
  } else {
    if constexpr (ZERO) asm volatile("v_mfma_f32_16x16x32_bf16 %0, %1, a[%c2:%c3], 0" : "=&v"(acc) : "v"(act), "i"(AR), "i"(AR + 3) : ACLOB);
    else asm volatile("v_mfma_f32_16x16x32_bf16 %0, %1, a[%c2:%c3], %0" : "+v"(acc) : "v"(act), "i"(AR), "i"(AR + 3) : ACLOB);
  }
}
template <int N> DI void wait_tie(v4u& r) { static_assert(N >= 0 && N < 64, "vmcnt"); asm volatile("s_waitcnt vmcnt(%1)" : "+v"(r) : "i"(N) : "memory"); }

template <bool G1> constexpr int vops(int v) { return v < (G1 ? KW1 : KW2) ? 4 : (G1 && v - KW1 < TPF ? 4 : 0); }
template <bool G1> constexpr int vops_range(int a, int b) { int n = 0; for (int v = a; v <= b; ++v) n += vops<G1>(v); return n; }

template <bool G1, int K, int M> DI void gemm_group(f32x4 (&acc)[4][4], v4u (&ring)[G1 ? DEPTH1 : DEPTH2][4], AStream& a, WStream& w2) {
  constexpr int KW = G1 ? KW1 : KW2, DEPTH = G1 ? DEPTH1 : DEPTH2, sl = K % DEPTH, V = K + DEPTH;
  constexpr bool more = V < KW, pf = !more && vops<G1>(V) != 0;
  wait_tie<(3 - M) + vops_range<G1>(K + 1, V - 1) + ((more || pf) && M >= 1 ? M - 1 : 0)>(ring[sl][M]);
  mfma<G1, K == 0 && !(G1 && M == 0) && G1, 16 * K + 0>(acc[M][0], ring[sl][M]);
  if constexpr (M >= 1) {
    if constexpr (more) act_frag<G1, M - 1>(ring[sl][M - 1], a);
    else if constexpr (pf) glds_piece<false, V - KW, M - 1>(w2);
  }
  mfma<G1, K == 0 && !(G1 && M == 0), 16 * K + 4>(acc[M][1], ring[sl][M]);
  mfma<G1, K == 0 && !(G1 && M == 0), 16 * K + 8>(acc[M][2], ring[sl][M]);
  mfma<G1, K == 0 && !(G1 && M == 0), 16 * K + 12>(acc[M][3], ring[sl][M]);
  if constexpr (M == 3) {
    if constexpr (more) { act_frag<G1, 3>(ring[sl][3], a); act_next(a); }
    else if constexpr (pf) glds_piece<false, V - KW, 3>(w2);
  }
  GAP();
}
template <bool G1, int K> DI void gemm_step(f32x4 (&acc)[4][4], v4u (&ring)[G1 ? DEPTH1 : DEPTH2][4], AStream& a, WStream& w2) {
  gemm_group<G1, K, 0>(acc, ring, a, w2); gemm_group<G1, K, 1>(acc, ring, a, w2);
  gemm_group<G1, K, 2>(acc, ring, a, w2); gemm_group<G1, K, 3>(acc, ring, a, w2);
}
template <bool G1, int... Ks> DI void gemm_steps(f32x4 (&acc)[4][4], v4u (&ring)[G1 ? DEPTH1 : DEPTH2][4], AStream& a, WStream& w2, iseq<Ks...>) {
  (gemm_step<G1, Ks>(acc, ring, a, w2), ...);
}
template <bool G1> DI void gemm_run(f32x4 (&acc)[4][4], AStream& a, WStream& w2) {
  constexpr int DEPTH = G1 ? DEPTH1 : DEPTH2;
  v4u ring[DEPTH][4];
#pragma unroll
  for (int k = 0; k < DEPTH; ++k) {
    act_frag<G1, 0>(ring[k][0], a); act_frag<G1, 1>(ring[k][1], a); act_frag<G1, 2>(ring[k][2], a); act_frag<G1, 3>(ring[k][3], a);
    act_next(a);
  }
  GAP();
  gemm_steps<G1>(acc, ring, a, w2, make_iseq<G1 ? KW1 : KW2>{});
  asm volatile("s_nop 15\n\ts_nop 7" : "+v"(acc[3][0]), "+v"(acc[3][1]), "+v"(acc[3][2]), "+v"(acc[3][3]));
  GAP();
}
template <int SLEEP> DI void poll_flags(unsigned voff, srd_t srd, unsigned soff, unsigned tlo, unsigned thi) {
#pragma unroll 1
  for (int spin = 0; spin < SPIN_MAX; ++spin) {
    v2u f;
    asm volatile("buffer_load_dwordx2 %0, %1, %2, %3 offen sc1\n\ts_waitcnt vmcnt(0)" : "=v"(f) : "v"(voff), "s"(srd), "s"(soff) : "memory");
    if (__builtin_amdgcn_ballot_w64(f[0] != tlo || f[1] != thi) == 0) break;
    __builtin_amdgcn_s_sleep(SLEEP);
  }
}
DI void store16_sc1(v4u d, unsigned voff, srd_t srd, unsigned soff) {
  asm volatile("buffer_store_dwordx4 %0, %1, %2, %3 offen sc1\n\ts_nop 1" ::"v"(d), "v"(voff), "s"(srd), "s"(soff) : "memory");
}
DI void store8_sc1(v2u d, unsigned voff, srd_t srd, unsigned soff) {
  asm volatile("buffer_store_dwordx2 %0, %1, %2, %3 offen sc1\n\ts_nop 1" ::"v"(d), "v"(voff), "s"(srd), "s"(soff) : "memory");
}
DI float gelu(float u) {
  const float e = __builtin_amdgcn_exp2f(u * (-2.3022082f - 0.1029432f * u * u));
  return u * __builtin_amdgcn_rcpf(1.0f + e);
}
#define PA(i)
#define PB(i)
#define STAMP(i)
#define STAMPA(i)
#define STAMPB(i)

__global__ void __launch_bounds__(256, 1)
mixer_kernel(const u16* __restrict__ x, const u16* __restrict__ w1, const u16* __restrict__ b1, const u16* __restrict__ w2, const u16* __restrict__ b2,
             u16* __restrict__ out, unsigned char* __restrict__ ws) {
  __shared__ __attribute__((aligned(1024))) unsigned char smem[LDS_BYTES];
  STAMP(0);
  asm volatile("" ::: ACLOB);
  const unsigned tid = threadIdx.x, lane = tid & 63, w = rfl(tid >> 6), b = blockIdx.x;
  const unsigned lds0 = (unsigned)(uintptr_t)(LAS unsigned char*)smem;
  const srd_t srdWS = mk_srd(ws, WS_USED);
  const unsigned g = lane >> 4, c = lane & 15;

  v2u ep; v4u xv[2];
  {
    const srd_t srdX = mk_srd(x, S_ * D_ * 2);
    const unsigned zero = 0, vx = lane < 56 ? lane * 16 : OOB, row = 32 * (b >> 2) + 4 * (b & 3) + 2 * (w & 1) + 16 * (w >> 1);
    asm volatile("buffer_load_dwordx4 %0, %1, %2, %3 offen" : "=v"(xv[0]) : "v"(vx), "s"(srdX), "s"(row * 896u) : "memory");
    asm volatile("buffer_load_dwordx4 %0, %1, %2, %3 offen offset:896" : "=v"(xv[1]) : "v"(vx), "s"(srdX), "s"(row * 896u) : "memory");
    asm volatile("buffer_load_dwordx2 %0, %1, %2, %3 offen sc1" : "=v"(ep) : "v"(zero), "s"(srdWS), "s"(EP_OFF) : "memory");
  }
  GAP();
  const unsigned xcd = b & 7, li = b >> 3;
  bool g1, g2;
  unsigned mb1, nb1, mb2, nb2;
  {
    const unsigned n1 = xcd < 4 ? 25 : 24, start = 24 * xcd + (xcd < 4 ? xcd : 4), i0 = 32 - n1;
    g1 = li >= i0;
    const unsigned T = g1 ? start + li - i0 : 0;
    nb1 = T / 7; mb1 = T % 7;
    g2 = li < 28; nb2 = 4 * xcd + (li & 3); mb2 = g2 ? li >> 2 : 0;
  }
  const unsigned rot1 = ROT1 ? (mb1 * 16) / 7 : 0, rot2 = ROT2 ? (mb2 * 8) % 14 : 0;
  WStream W1, W2;
  const unsigned region = lds0 + w * REG_W;
  {
    const unsigned r8 = lane >> 3, pc = (lane & 7) ^ (r8 & 6);
    const unsigned q = (lane >> 2) & 3, p = lane & 3, row = 4 * g + q;
    const unsigned t0 = region + row * 128 + ((row >> 1) & 3) * 32 + p * 8;
    W1.srd = mk_srd(w1, S_ * F_ * 2);
    W2.srd = mk_srd(w2, g2 ? F_ * S_ * 2 : 0);
    W1.beg = w * (KW1 * 32) * F_ * 2 + nb1 * 128; W1.end = W1.beg + KW1 * 32 * F_ * 2; W1.cur = W1.beg + rot1 * (32 * F_ * 2);
    W2.beg = w * (KW2 * 32) * S_ * 2 + nb2 * 128; W2.end = W2.beg + KW2 * 32 * S_ * 2; W2.cur = W2.beg + rot2 * (32 * S_ * 2);
    W1.vo = r8 * F_ * 2 + pc * 16;
    W2.vo = r8 * S_ * 2 + pc * 16;
    W1.ring = W2.ring = region;
#pragma unroll
    for (int t = 0; t < 4; ++t) { W1.ta[t] = t0 ^ (t << 5); W2.ta[t] = t0 ^ (((t + w) & 3) << 5); }
  }
  if (g1) glds_slots<true, 0>(W1, make_iseq<PRE>{}); else waitv<0>();
  asm volatile("s_waitcnt vmcnt(%3)" : "+v"(ep), "+v"(xv[0]), "+v"(xv[1]) : "i"(4 * PRE) : "memory");
  GAP();
  STAMP(1);
  if (g1) glds_slots<true, PRE>(W1, make_iseq<MID>{});
  GAP();
  unsigned tlo, thi;
  {
    const unsigned long long e = (((unsigned long long)ep[1] << 32) | ep[0]) + 1ull;
    tlo = rfl((unsigned)e); thi = rfl((unsigned)(e >> 32));
  }
  {
    float f[2][8], m[2], qs[2];
#pragma unroll
    for (int r = 0; r < 2; ++r) {
#pragma unroll
      for (int i = 0; i < 4; ++i) { f[r][2 * i] = bflo(xv[r][i]); f[r][2 * i + 1] = bfhi(xv[r][i]); }
      m[r] = ((f[r][0] + f[r][1]) + (f[r][2] + f[r][3])) + ((f[r][4] + f[r][5]) + (f[r][6] + f[r][7]));
    }
    wave_sum2(m[0], m[1]);
#pragma unroll
    for (int r = 0; r < 2; ++r) {
      const float mu = m[r] * (1.0f / D_);
#pragma unroll
      for (int i = 0; i < 8; ++i) f[r][i] -= mu;
      qs[r] = ((f[r][0] * f[r][0] + f[r][1] * f[r][1]) + (f[r][2] * f[r][2] + f[r][3] * f[r][3])) + ((f[r][4] * f[r][4] + f[r][5] * f[r][5]) + (f[r][6] * f[r][6] + f[r][7] * f[r][7]));
      qs[r] = lane < 56 ? qs[r] : 0.f;
    }
    wave_sum2(qs[0], qs[1]);
    v4u o[2];
#pragma unroll
    for (int r = 0; r < 2; ++r) {
      const float rs = __builtin_amdgcn_rsqf(qs[r] * (1.0f / D_) + 1e-5f);
      o[r] = v4u{pack2(f[r][0] * rs, f[r][1] * rs), pack2(f[r][2] * rs, f[r][3] * rs), pack2(f[r][4] * rs, f[r][5] * rs), pack2(f[r][6] * rs, f[r][7] * rs)};
    }
    if (lane < 56) {
      const unsigned ro = w * REG_W + LN_OFF + (w & 1) * 128 + lane * 16;
      *(LAS v4u*)(smem + ro) = o[0];
      *(LAS v4u*)(smem + ro + LNP) = o[1];
    }
  }
  wg_barrier();
  PA(2);
  {
    v2u lo[2], hi[2];
    const unsigned q = (lane >> 2) & 3;
    const unsigned tlo_a = lds0 + (q >> 1) * (REG_W + 128) + LN_OFF + (q & 1) * LNP + g * 32 + (lane & 3) * 8 + w * 128, thi_a = tlo_a + 2 * REG_W;
    asm volatile("ds_read_b64_tr_b16 %0, %1 offset:0" : "=v"(lo[0]) : "v"(tlo_a) : "memory");
    asm volatile("ds_read_b64_tr_b16 %0, %1 offset:0" : "=v"(hi[0]) : "v"(thi_a) : "memory");
    asm volatile("ds_read_b64_tr_b16 %0, %1 offset:512" : "=v"(lo[1]) : "v"(tlo_a) : "memory");
    asm volatile("ds_read_b64_tr_b16 %0, %1 offset:512" : "=v"(hi[1]) : "v"(thi_a) : "memory");
    asm volatile("s_waitcnt lgkmcnt(0)" : "+v"(lo[0]), "+v"(lo[1]), "+v"(hi[0]), "+v"(hi[1])::"memory");
    const unsigned vo = g * 1024 + c * 16 + (b & 3) * 256;
    const unsigned so = H_OFF + (b >> 2) * KSTRIDE + w * 4096;
    store16_sc1(v4u{lo[0][0], lo[0][1], hi[0][0], hi[0][1]}, vo, srdWS, so);
    store16_sc1(v4u{lo[1][0], lo[1][1], hi[1][0], hi[1][1]}, w < 3 ? vo : OOB, srdWS, so + 16384);
    if (g1) {
      glds_slots<true, NB1>(W1, make_iseq<NA>{});
      PA(3);
      wpipe_steps<true, 0, 1>(W1, make_iseq<POST>{});
      PA(4);
      waitv<4 * (NA + POST)>();
      PA(5);
    } else {
      PA(3);
      waitv<0>();
      PA(5);
    }
    store8_sc1(v2u{tlo, thi}, lane < R1 ? lane * 8192 + w * 2048 + b * 8 : OOB, srdWS, HF_OFF);
  }
  STAMP(2);
  f32x4 acc[4][4];
  if (g1) {
    glds_slots<true, NB1 + NA>(W1, make_iseq<NS1 - NB1 - NA>{});
    wpipe_steps<true, POST, 2>(W1, make_iseq<7 - POST>{});
    PA(6);
    wpipe_steps<true, 7, 2>(W1, make_iseq<KW1 - 7>{});
    PA(7);
    v2u bv[4];
    {
      const srd_t srdB1 = mk_srd(b1, F_ * 2);
      asm volatile("buffer_load_dwordx2 %0, %1, %2, %3 offen" : "=v"(bv[0]) : "v"(g * 8), "s"(srdB1), "s"(nb1 * 128u) : "memory");
      asm volatile("buffer_load_dwordx2 %0, %1, %2, %3 offen offset:32" : "=v"(bv[1]) : "v"(g * 8), "s"(srdB1), "s"(nb1 * 128u) : "memory");
      asm volatile("buffer_load_dwordx2 %0, %1, %2, %3 offen offset:64" : "=v"(bv[2]) : "v"(g * 8), "s"(srdB1), "s"(nb1 * 128u) : "memory");
      asm volatile("buffer_load_dwordx2 %0, %1, %2, %3 offen offset:96" : "=v"(bv[3]) : "v"(g * 8), "s"(srdB1), "s"(nb1 * 128u) : "memory");
    }
    STAMPA(3);
    poll_flags<PSLEEP>(w * 512 + lane * 8, srdWS, HF_OFF + (b % R1) * 8192 + (mb1 & 3) * 2048, tlo, thi);
    asm volatile("s_waitcnt lgkmcnt(0)" : "+v"(bv[0]), "+v"(bv[1]), "+v"(bv[2]), "+v"(bv[3])::"memory");
    STAMPA(4);
#pragma unroll
    for (int t = 0; t < 4; ++t) { const v2u q = bv[t]; const unsigned q0 = q[0], q1 = q[1]; acc[0][t] = f32x4{bflo(q0), bfhi(q0), bflo(q1), bfhi(q1)}; }
    asm volatile("" : "+v"(acc[0][0]), "+v"(acc[0][1]), "+v"(acc[0][2]), "+v"(acc[0][3]));
    {
      AStream A;
      A.srd = srdWS; A.beg = H_OFF + (w * KW1 * 28 + 4 * mb1) * 1024; A.end = A.beg + KW1 * KSTRIDE; A.cur = A.beg + rot1 * KSTRIDE;
#pragma unroll
      for (int m = 0; m < 4; ++m) A.voff[m] = lane * 16 + ((m + w) & 3) * 1024;
      gemm_run<true>(acc, A, W2);
    }
    STAMPA(5);
#pragma unroll
    for (int m = 1; m < 4; ++m)
#pragma unroll
      for (int t = 0; t < 4; ++t) *(LAS f32x4*)(smem + w * REG_W + (m - 1) * 4096 + t * 1024 + lane * 16) = acc[m][t];
    wg_barrier();
    {
      f32x4 r[3][4];
#pragma unroll
      for (int j = 1; j < 4; ++j)
#pragma unroll
        for (int t = 0; t < 4; ++t) r[j - 1][t] = *(const LAS f32x4*)(smem + ((w - j) & 3) * REG_W + (j - 1) * 4096 + t * 1024 + lane * 16);
      GAP();
      glds_slots<false, TPF>(W2, make_iseq<NS2 - TPF>{});
      GAP();
#pragma unroll
      for (int kk = 0; kk < 2; ++kk) {
        const f32x4 s0 = (acc[0][2 * kk] + r[0][2 * kk]) + (r[1][2 * kk] + r[2][2 * kk]);
        const f32x4 s1 = (acc[0][2 * kk + 1] + r[0][2 * kk + 1]) + (r[1][2 * kk + 1] + r[2][2 * kk + 1]);
        const v4u o = {pack2(gelu(s0[0]), gelu(s0[1])), pack2(gelu(s0[2]), gelu(s0[3])), pack2(gelu(s1[0]), gelu(s1[1])), pack2(gelu(s1[2]), gelu(s1[3]))};
        store16_sc1(o, lane * 16, srdWS, U_OFF + ((2 * nb1 + kk) * 28 + 4 * mb1 + w) * 1024);
      }
    }
    GAP();
    wpipe_steps<false, 0, 1>(W2, make_iseq<POST2>{});
    waitv<4 * POST2>();
    store8_sc1(v2u{tlo, thi}, lane < R2 ? lane * UF_REP + (mb1 * 4 + nb1 / 7) * 256 + ((nb1 % 7) * 4 + w) * 8 : OOB, srdWS, UF_OFF);
    STAMP(6);
    if (!g2) {
      if (b == EPOCH_WG && w == 1) store8_sc1(v2u{tlo, thi}, lane == 0 ? 0 : OOB, srdWS, EP_OFF);
      waitv<0>();
      goto done;
    }
    wpipe_steps<false, POST2, 2>(W2, make_iseq<KW2 - POST2>{});
  } else {
    wg_barrier();
    if (!g2) goto done;
#pragma unroll 1
    for (int i = 0; i < IDLE_SLEEP; ++i) __builtin_amdgcn_s_sleep(16);
    glds_slots<false, 0>(W2, make_iseq<NS2>{});
    wpipe_steps<false, 0, 0>(W2, make_iseq<KW2>{});
    if (w == 0) poll_flags<8>(0, srdWS, EP_OFF, tlo, thi);
    wg_barrier();
  }
  {
    unsigned bv;
    {
      const srd_t srdB2 = mk_srd(b2, S_ * 2);
      asm volatile("buffer_load_ushort %0, %1, %2, %3 offen" : "=v"(bv) : "v"((16 * w + c) * 2), "s"(srdB2), "s"(nb2 * 128u) : "memory");
    }
    STAMPB(3);
    poll_flags<PSLEEP>((lane < 28 ? lane : 27) * 8, srdWS, UF_OFF + (nb2 % R2) * UF_REP + (mb2 * 4 + w) * 256, tlo, thi);
    asm volatile("s_waitcnt lgkmcnt(0)" : "+v"(bv)::"memory");
    STAMPB(4);
    {
      const float bb = bflo(bv);
#pragma unroll
      for (int m = 0; m < 4; ++m) acc[m][0] = f32x4{bb, bb, bb, bb};
      asm volatile("" : "+v"(acc[0][0]), "+v"(acc[1][0]), "+v"(acc[2][0]), "+v"(acc[3][0]));
      AStream A;
      A.srd = srdWS; A.beg = U_OFF + (w * KW2 * 28 + 4 * mb2) * 1024; A.end = A.beg + KW2 * KSTRIDE; A.cur = A.beg + rot2 * KSTRIDE;
      A.voff[0] = A.voff[1] = A.voff[2] = A.voff[3] = lane * 16;
      gemm_run<false>(acc, A, W2);
    }
    STAMPB(5);
#pragma unroll
    for (int t = 1; t < 4; ++t)
#pragma unroll
      for (int m = 0; m < 4; ++m) *(LAS f32x4*)(smem + w * REG_W + W2_OFF + (t - 1) * 4096 + m * 1024 + lane * 16) = acc[m][t];
    wg_barrier();
    const srd_t srdO = mk_srd(out, S_ * D_ * 2);
    unsigned pk[4][2];
#pragma unroll
    for (int m = 0; m < 4; ++m) {
      f32x4 r[3];
#pragma unroll
      for (int j = 1; j < 4; ++j) r[j - 1] = *(const LAS f32x4*)(smem + ((w - j) & 3) * REG_W + W2_OFF + (j - 1) * 4096 + m * 1024 + lane * 16);
      const f32x4 s = (acc[m][0] + r[0]) + (r[1] + r[2]);
      pk[m][0] = pack2(s[0], s[1]); pk[m][1] = pack2(s[2], s[3]);
    }
    const unsigned vo = (16 * w + c) * (D_ * 2) + (g & 1) * 32 + (g >> 1) * 16;
#pragma unroll
    for (int mp = 0; mp < 2; ++mp) {
      auto px = __builtin_amdgcn_permlane16_swap(pk[2 * mp][0], pk[2 * mp + 1][0], false, false);
      auto py = __builtin_amdgcn_permlane16_swap(pk[2 * mp][1], pk[2 * mp + 1][1], false, false);
      const unsigned x0 = px[0], x1 = px[1], y0 = py[0], y1 = py[1];
      const v4u o = {x0, y0, x1, y1};
      asm volatile("buffer_store_dwordx4 %0, %1, %2, %3 offen offset:%c4\n\ts_nop 1" ::"v"(o), "v"(vo), "s"(srdO), "s"(nb2 * 64 * (D_ * 2) + mb2 * 128), "i"(mp * 64) : "memory");
    }
  }
done:
  STAMP(7);
}

extern "C" void kernel_launch(void* const* d_in, const int* in_sizes, int n_in, void* d_out, int out_size, void* d_ws, size_t ws_size, hipStream_t stream) {
  mixer_kernel<<<256, 256, 0, stream>>>((const u16*)d_in[0], (const u16*)d_in[1], (const u16*)d_in[2], (const u16*)d_in[3], (const u16*)d_in[4],
                                       (u16*)d_out, (unsigned char*)d_ws);
}
```

```cpp
#include <hip/hip_runtime.h>
#include <stdint.h>
#include <utility>

typedef unsigned v4u __attribute__((ext_vector_type(4)));
typedef unsigned v2u __attribute__((ext_vector_type(2)));
typedef float f32x4 __attribute__((ext_vector_type(4)));
typedef unsigned short u16;
typedef __amdgpu_buffer_rsrc_t srd_t;
#define DI __device__ __forceinline__
#define LAS __attribute__((address_space(3)))
#define GAP() __builtin_amdgcn_sched_barrier(0)
template <int... Is> using iseq = std::integer_sequence<int, Is...>;
template <int N> using make_iseq = std::make_integer_sequence<int, N>;
#define ACLOB "a0","a1","a2","a3","a4","a5","a6","a7","a8","a9","a10","a11","a12","a13","a14","a15","a16","a17","a18","a19","a20","a21","a22","a23","a24","a25","a26","a27","a28","a29","a30","a31","a32","a33","a34","a35","a36","a37","a38","a39","a40","a41","a42","a43","a44","a45","a46","a47","a48","a49","a50","a51","a52","a53","a54","a55","a56","a57","a58","a59","a60","a61","a62","a63","a64","a65","a66","a67","a68","a69","a70","a71","a72","a73","a74","a75","a76","a77","a78","a79","a80","a81","a82","a83","a84","a85","a86","a87","a88","a89","a90","a91","a92","a93","a94","a95","a96","a97","a98","a99","a100","a101","a102","a103","a104","a105","a106","a107","a108","a109","a110","a111","a112","a113","a114","a115","a116","a117","a118","a119","a120","a121","a122","a123","a124","a125","a126","a127","a128","a129","a130","a131","a132","a133","a134","a135","a136","a137","a138","a139","a140","a141","a142","a143","a144","a145","a146","a147","a148","a149","a150","a151","a152","a153","a154","a155","a156","a157","a158","a159","a160","a161","a162","a163","a164","a165","a166","a167","a168","a169","a170","a171","a172","a173","a174","a175","a176","a177","a178","a179","a180","a181","a182","a183","a184","a185","a186","a187","a188","a189","a190","a191","a192","a193","a194","a195","a196","a197","a198","a199","a200","a201","a202","a203","a204","a205","a206","a207","a208","a209","a210","a211","a212","a213","a214","a215","a216","a217","a218","a219","a220","a221","a222","a223","a224","a225","a226","a227","a228","a229","a230","a231","a232","a233","a234","a235","a236","a237","a238","a239","a240","a241","a242","a243","a244","a245","a246","a247","a248","a249","a250","a251","a252","a253","a254","a255"

#define MAPPING 3
#define DEPTH1 4
#define DEPTH2 4
#define PRE 3
#define MID 0
#define NA 3
#define POST 0
#define POST2 2
#define ROT1 1
#define ROT2 1
#define IDLE_SLEEP 0
#define PSLEEP 2
#define QUIET 1
#define WSLEEP1 16
#define WSLEEP2 16
#define R1 4
#define R2 4

constexpr int S_ = 2048, D_ = 448, F_ = 1792;
constexpr int KW1 = 16, KW2 = 14;
constexpr int NS1 = 9, NS2 = 7;
#define TPFMAX 6
constexpr int TPF = DEPTH1 < TPFMAX ? DEPTH1 : TPFMAX;
constexpr unsigned KSTRIDE = 28u * 1024u;
constexpr unsigned H_OFF = 0, U_OFF = 64u * KSTRIDE;
constexpr unsigned EP_OFF = 4u << 20, HF_OFF = EP_OFF + 4096u, UF_OFF = HF_OFF + 16u * 8192u;
constexpr unsigned UF_REP = 28u * 256u;
constexpr unsigned WS_USED = UF_OFF + 16u * UF_REP;
constexpr unsigned REG_W = 40960, LN_OFF = 36864, LNP = 960, W2_OFF = 12288, LDS_BYTES = 4 * REG_W;
constexpr unsigned OOB = 0x7ffffff0u;
constexpr int SPIN_MAX = 1 << 15;
constexpr unsigned EPOCH_WG = 225;

DI unsigned rfl(unsigned x) { return __builtin_amdgcn_readfirstlane(x); }
DI float bflo(unsigned w) { return __builtin_bit_cast(float, w << 16); }
DI float bfhi(unsigned w) { return __builtin_bit_cast(float, w & 0xffff0000u); }
DI unsigned pack2(float lo, float hi) {
  typedef __bf16 bf2 __attribute__((ext_vector_type(2)));
  typedef float f2 __attribute__((ext_vector_type(2)));
  f2 v = {lo, hi};
  return __builtin_bit_cast(unsigned, __builtin_convertvector(v, bf2));
}
DI srd_t mk_srd(const void* p, unsigned bytes) { return __builtin_amdgcn_make_buffer_rsrc((void*)p, 0, bytes, 0x00020000); }
template <int C> DI float dpp_add(float v) {
  return v + __builtin_bit_cast(float, __builtin_amdgcn_update_dpp(0, __builtin_bit_cast(int, v), C, 0xf, 0xf, true));
}
DI void wave_sum2(float& a, float& b) {
  a = dpp_add<0xB1>(a); b = dpp_add<0xB1>(b); a = dpp_add<0x4E>(a); b = dpp_add<0x4E>(b);
  a = dpp_add<0x141>(a); b = dpp_add<0x141>(b); a = dpp_add<0x140>(a); b = dpp_add<0x140>(b);
  auto p = __builtin_amdgcn_permlane16_swap(__builtin_bit_cast(unsigned, a), __builtin_bit_cast(unsigned, b), false, false);
  const unsigned p0 = p[0], p1 = p[1];
  float s = __builtin_bit_cast(float, p0) + __builtin_bit_cast(float, p1);
  auto q = __builtin_amdgcn_permlane32_swap(__builtin_bit_cast(unsigned, s), __builtin_bit_cast(unsigned, s), false, false);
  const unsigned q0 = q[0], q1 = q[1];
  s = __builtin_bit_cast(float, q0) + __builtin_bit_cast(float, q1);
  a = __builtin_bit_cast(float, __builtin_amdgcn_readlane(__builtin_bit_cast(int, s), 0));
  b = __builtin_bit_cast(float, __builtin_amdgcn_readlane(__builtin_bit_cast(int, s), 16));
}
template <int N> DI void waitv() { static_assert(N >= 0 && N < 64, "vmcnt"); asm volatile("s_waitcnt vmcnt(%0)" ::"i"(N) : "memory"); }
DI void wait_lds() { asm volatile("s_waitcnt lgkmcnt(0)" ::: "memory"); }
DI void wg_barrier() { asm volatile("s_waitcnt lgkmcnt(0)\n\ts_barrier" ::: "memory"); }

struct WStream {
  srd_t srd;
  unsigned cur, beg, end;
  unsigned vo;
  unsigned ring;
  unsigned ta[4];
};
DI void cyc_add(unsigned& cur, unsigned inc, unsigned beg, unsigned end) {
  asm volatile("s_add_u32 %0, %0, %1\n\ts_cmp_eq_u32 %0, %3\n\ts_cselect_b32 %0, %2, %0" : "+s"(cur) : "s"(inc), "s"(beg), "s"(end) : "scc");
}
template <bool W1> struct WP { static constexpr int LDW = W1 ? F_ : S_, KW = W1 ? KW1 : KW2, NS = W1 ? NS1 : NS2, OFF = W1 ? 0 : (int)W2_OFF; };
template <bool W1, int KSTEP, int I> DI void glds_piece(WStream& s) {
  using P = WP<W1>;
  unsigned t;
  asm volatile("s_add_u32 m0, %1, %2\n\ts_add_u32 %0, %3, %4\n\tbuffer_load_dwordx4 %5, %6, %0 offen lds"
               : "=&s"(t)
               : "s"(s.ring), "i"(P::OFF + (KSTEP % P::NS) * 4096 + I * 1024), "s"(s.cur), "i"(I * 8 * P::LDW * 2), "v"(s.vo), "s"(s.srd)
               : "scc", "memory");
  if constexpr (I == 3) cyc_add(s.cur, 32 * P::LDW * 2, s.beg, s.end);
}
template <bool W1, int KSTEP> DI void glds_slot(WStream& s) {
  glds_piece<W1, KSTEP, 0>(s); glds_piece<W1, KSTEP, 1>(s); glds_piece<W1, KSTEP, 2>(s); glds_piece<W1, KSTEP, 3>(s);
}
template <bool W1, int KSTEP, int T, int H> DI void tr_piece(const WStream& s) {
  using P = WP<W1>;
  asm volatile("ds_read_b64_tr_b16 a[%c1:%c2], %0 offset:%c3" ::"v"(s.ta[T]), "i"(16 * KSTEP + 4 * T + 2 * H), "i"(16 * KSTEP + 4 * T + 2 * H + 1),
               "i"(P::OFF + (KSTEP % P::NS) * 4096 + H * 2048) : "memory", ACLOB);
}
template <bool W1, int KSTEP> DI void tr_slot(const WStream& s) {
  tr_piece<W1, KSTEP, 0, 0>(s); tr_piece<W1, KSTEP, 0, 1>(s); tr_piece<W1, KSTEP, 1, 0>(s); tr_piece<W1, KSTEP, 1, 1>(s);
  tr_piece<W1, KSTEP, 2, 0>(s); tr_piece<W1, KSTEP, 2, 1>(s); tr_piece<W1, KSTEP, 3, 0>(s); tr_piece<W1, KSTEP, 3, 1>(s);
}
constexpr int NB1 = PRE + MID;
static_assert(NB1 + NA <= NS1 && (POST == 0 || NB1 + NA == NS1) && POST <= NB1 && POST <= KW1 - NS1 && POST2 <= NS2, "schedule");
template <bool W1> constexpr int younger(int j, bool flagged) {
  constexpr int KW = W1 ? KW1 : KW2, NS = W1 ? NS1 : NS2;
  const int before_stores = W1 ? NB1 : NS2, before_flag = W1 ? (NB1 + NA == NS1 ? NS1 + POST : NB1 + NA) : NS2 + POST2;
  const int last = j + NS - 1 < KW - 1 ? j + NS - 1 : KW - 1;
  return 4 * (last - j) + (j < before_stores ? 2 : 0) + (flagged && j < before_flag ? 1 : 0);
}
template <bool W1, int J, int MODE, int EXTRA = 0> DI void wpipe_step(WStream& s) {
  using P = WP<W1>;
  constexpr int rem = P::KW - 1 - J;
  waitv<(MODE == 0 ? 4 * (rem < P::NS - 1 ? rem : P::NS - 1) : younger<W1>(J, MODE == 2)) + EXTRA>();
  tr_slot<W1, J>(s);
  if constexpr (J + P::NS < P::KW) { wait_lds(); glds_slot<W1, J + P::NS>(s); }
  GAP();
}
template <bool W1, int J0, int MODE, int... Js> DI void wpipe_steps(WStream& s, iseq<Js...>) { (wpipe_step<W1, J0 + Js, MODE>(s), ...); }
template <bool W1, int J0, int EXTRA, int... Js> DI void wtail_steps(WStream& s, iseq<Js...>) { (wpipe_step<W1, J0 + Js, 2, EXTRA>(s), ...); }
template <bool W1, int J0, int... Js> DI void glds_slots(WStream& s, iseq<Js...>) { (glds_slot<W1, J0 + Js>(s), ...); }

template <int IMM> DI void act_load(v4u& d, unsigned voff, srd_t srd, unsigned soff) {
  asm volatile("buffer_load_dwordx4 %0, %1, %2, %3 offen offset:%c4 sc1" : "=v"(d) : "v"(voff), "s"(srd), "s"(soff), "i"(IMM) : "memory");
}
struct AStream { srd_t srd; unsigned voff[4], cur, beg, end; };
template <bool G1, int M> DI void act_frag(v4u& d, const AStream& a) {
  if constexpr (G1) act_load<0>(d, a.voff[M], a.srd, a.cur); else act_load<M * 1024>(d, a.voff[0], a.srd, a.cur);
}
DI void act_next(AStream& a) { cyc_add(a.cur, KSTRIDE, a.beg, a.end); }
template <bool WA, bool ZERO, int AR> DI void mfma(f32x4& acc, const v4u& act) {
  if constexpr (WA) {
    if constexpr (ZERO) asm volatile("v_mfma_f32_16x16x32_bf16 %0, a[%c2:%c3], %1, 0" : "=&v"(acc) : "v"(act), "i"(AR), "i"(AR + 3) : ACLOB);
    else asm volatile("v_mfma_f32_16x16x32_bf16 %0, a[%c2:%c3], %1, %0" : "+v"(acc) : "v"(act), "i"(AR), "i"(AR + 3) : ACLOB);
  } else {
    if constexpr (ZERO) asm volatile("v_mfma_f32_16x16x32_bf16 %0, %1, a[%c2:%c3], 0" : "=&v"(acc) : "v"(act), "i"(AR), "i"(AR + 3) : ACLOB);
    else asm volatile("v_mfma_f32_16x16x32_bf16 %0, %1, a[%c2:%c3], %0" : "+v"(acc) : "v"(act), "i"(AR), "i"(AR + 3) : ACLOB);
  }
}
template <int N> DI void wait_tie(v4u& r) { static_assert(N >= 0 && N < 64, "vmcnt"); asm volatile("s_waitcnt vmcnt(%1)" : "+v"(r) : "i"(N) : "memory"); }

template <bool G1> constexpr int vops(int v) { return v < (G1 ? KW1 : KW2) ? 4 : (G1 && v - KW1 < TPF ? 4 : 0); }
template <bool G1> constexpr int vops_range(int a, int b) { int n = 0; for (int v = a; v <= b; ++v) n += vops<G1>(v); return n; }

template <bool G1, int K, int M> DI void gemm_group(f32x4 (&acc)[4][4], v4u (&ring)[G1 ? DEPTH1 : DEPTH2][4], AStream& a, WStream& w2) {
  constexpr int KW = G1 ? KW1 : KW2, DEPTH = G1 ? DEPTH1 : DEPTH2, sl = K % DEPTH, V = K + DEPTH;
  constexpr bool more = V < KW, pf = !more && vops<G1>(V) != 0;
  wait_tie<(3 - M) + vops_range<G1>(K + 1, V - 1) + ((more || pf) && M >= 1 ? M - 1 : 0)>(ring[sl][M]);
  mfma<G1, K == 0 && !(G1 && M == 0) && G1, 16 * K + 0>(acc[M][0], ring[sl][M]);
  if constexpr (M >= 1) {
    if constexpr (more) act_frag<G1, M - 1>(ring[sl][M - 1], a);
    else if constexpr (pf) glds_piece<false, V - KW, M - 1>(w2);
  }
  mfma<G1, K == 0 && !(G1 && M == 0), 16 * K + 4>(acc[M][1], ring[sl][M]);
  mfma<G1, K == 0 && !(G1 && M == 0), 16 * K + 8>(acc[M][2], ring[sl][M]);
  mfma<G1, K == 0 && !(G1 && M == 0), 16 * K + 12>(acc[M][3], ring[sl][M]);
  if constexpr (M == 3) {
    if constexpr (more) { act_frag<G1, 3>(ring[sl][3], a); act_next(a); }
    else if constexpr (pf) glds_piece<false, V - KW, 3>(w2);
  }
  GAP();
}
template <bool G1, int K> DI void gemm_step(f32x4 (&acc)[4][4], v4u (&ring)[G1 ? DEPTH1 : DEPTH2][4], AStream& a, WStream& w2) {
  gemm_group<G1, K, 0>(acc, ring, a, w2); gemm_group<G1, K, 1>(acc, ring, a, w2);
  gemm_group<G1, K, 2>(acc, ring, a, w2); gemm_group<G1, K, 3>(acc, ring, a, w2);
}
template <bool G1, int... Ks> DI void gemm_steps(f32x4 (&acc)[4][4], v4u (&ring)[G1 ? DEPTH1 : DEPTH2][4], AStream& a, WStream& w2, iseq<Ks...>) {
  (gemm_step<G1, Ks>(acc, ring, a, w2), ...);
}
template <bool G1> DI void gemm_prologue(v4u (&ring)[G1 ? DEPTH1 : DEPTH2][4], AStream& a) {
  constexpr int DEPTH = G1 ? DEPTH1 : DEPTH2;
#pragma unroll
  for (int k = 0; k < DEPTH; ++k) {
    act_frag<G1, 0>(ring[k][0], a); act_frag<G1, 1>(ring[k][1], a); act_frag<G1, 2>(ring[k][2], a); act_frag<G1, 3>(ring[k][3], a);
    act_next(a);
  }
  GAP();
}
template <bool G1> DI void gemm_body(f32x4 (&acc)[4][4], v4u (&ring)[G1 ? DEPTH1 : DEPTH2][4], AStream& a, WStream& w2) {
  gemm_steps<G1>(acc, ring, a, w2, make_iseq<G1 ? KW1 : KW2>{});
  asm volatile("s_nop 15\n\ts_nop 7" : "+v"(acc[3][0]), "+v"(acc[3][1]), "+v"(acc[3][2]), "+v"(acc[3][3]));
  GAP();
}
template <int SLEEP> DI void poll_flags(unsigned voff, srd_t srd, unsigned soff, unsigned tlo, unsigned thi) {
#pragma unroll 1
  for (int spin = 0; spin < SPIN_MAX; ++spin) {
    v2u f;
    asm volatile("buffer_load_dwordx2 %0, %1, %2, %3 offen sc1\n\ts_waitcnt vmcnt(0)" : "=v"(f) : "v"(voff), "s"(srd), "s"(soff) : "memory");
    if (__builtin_amdgcn_ballot_w64(f[0] != tlo || f[1] != thi) == 0) break;
    __builtin_amdgcn_s_sleep(SLEEP);
  }
}
template <int SLEEP, int N> DI void poll_flags2(unsigned voff, bool use, srd_t srd, unsigned soff, unsigned tlo, unsigned thi) {
#pragma unroll 1
  for (int spin = 0; spin < SPIN_MAX; ++spin) {
    v4u f[2];
    asm volatile("buffer_load_dwordx4 %0, %1, %2, %3 offen sc1" : "=v"(f[0]) : "v"(voff), "s"(srd), "s"(soff) : "memory");
    if constexpr (N == 2) asm volatile("buffer_load_dwordx4 %0, %1, %2, %3 offen offset:1024 sc1" : "=v"(f[1]) : "v"(voff), "s"(srd), "s"(soff) : "memory");
    if constexpr (N == 2) asm volatile("s_waitcnt vmcnt(0)" : "+v"(f[0]), "+v"(f[1])::"memory");
    else asm volatile("s_waitcnt vmcnt(0)" : "+v"(f[0])::"memory");
    const v4u a = f[0];
    bool bad = a[0] != tlo || a[1] != thi || a[2] != tlo || a[3] != thi;
    if constexpr (N == 2) { const v4u c = f[1]; bad = bad || c[0] != tlo || c[1] != thi || c[2] != tlo || c[3] != thi; }
    if (__builtin_amdgcn_ballot_w64(bad && use) == 0) break;
    __builtin_amdgcn_s_sleep(SLEEP);
  }
}
DI void store16_sc1(v4u d, unsigned voff, srd_t srd, unsigned soff) {
  asm volatile("buffer_store_dwordx4 %0, %1, %2, %3 offen sc1\n\ts_nop 1" ::"v"(d), "v"(voff), "s"(srd), "s"(soff) : "memory");
}
DI void store8_sc1(v2u d, unsigned voff, srd_t srd, unsigned soff) {
  asm volatile("buffer_store_dwordx2 %0, %1, %2, %3 offen sc1\n\ts_nop 1" ::"v"(d), "v"(voff), "s"(srd), "s"(soff) : "memory");
}
DI float gelu(float u) {
  const float e = __builtin_amdgcn_exp2f(u * (-2.3022082f - 0.1029432f * u * u));
  return u * __builtin_amdgcn_rcpf(1.0f + e);
}
#define PA(i)
#define PB(i)
#define STAMP(i)
#define STAMPA(i)
#define STAMPB(i)
#define STAMPX0(i)
#define STAMPX1(i)

__global__ void __launch_bounds__(256, 1)
mixer_kernel(const u16* __restrict__ x, const u16* __restrict__ w1, const u16* __restrict__ b1, const u16* __restrict__ w2, const u16* __restrict__ b2,
             u16* __restrict__ out, unsigned char* __restrict__ ws) {
  __shared__ __attribute__((aligned(1024))) unsigned char smem[LDS_BYTES];
  STAMP(0);
  asm volatile("" ::: ACLOB);
  const unsigned tid = threadIdx.x, lane = tid & 63, w = rfl(tid >> 6), b = blockIdx.x;
  const unsigned lds0 = (unsigned)(uintptr_t)(LAS unsigned char*)smem;
  const srd_t srdWS = mk_srd(ws, WS_USED);
  const unsigned g = lane >> 4, c = lane & 15;

  v2u ep; v4u xv[2];
  {
    const srd_t srdX = mk_srd(x, S_ * D_ * 2);
    const unsigned zero = 0, vx = lane < 56 ? lane * 16 : OOB, row = 32 * (b >> 2) + 4 * (b & 3) + 2 * (w & 1) + 16 * (w >> 1);
    asm volatile("buffer_load_dwordx4 %0, %1, %2, %3 offen" : "=v"(xv[0]) : "v"(vx), "s"(srdX), "s"(row * 896u) : "memory");
    asm volatile("buffer_load_dwordx4 %0, %1, %2, %3 offen offset:896" : "=v"(xv[1]) : "v"(vx), "s"(srdX), "s"(row * 896u) : "memory");
    asm volatile("buffer_load_dwordx2 %0, %1, %2, %3 offen sc1" : "=v"(ep) : "v"(zero), "s"(srdWS), "s"(EP_OFF) : "memory");
  }
  GAP();
  const unsigned xcd = b & 7, li = b >> 3;
  bool g1, g2;
  unsigned mb1, nb1, mb2, nb2;
  {
    const unsigned j = xcd >> 1, odd = xcd & 1, i0 = 7 + odd;
    g1 = li >= i0;
    const unsigned idx = g1 ? li - i0 + (odd ? 25 : 0) : 0;
    nb1 = 7 * j + idx / 7; mb1 = idx % 7;
    g2 = li < 28; nb2 = 4 * xcd + (li & 3); mb2 = g2 ? li >> 2 : 0;
  }
  const unsigned rot1 = ROT1 == 0 ? 0 : ROT1 == 1 ? (mb1 * 16) / 7 : ROT1 == 2 ? ((mb1 * 16) / 7 + 4 * (nb1 & 3)) & 15 : (4 * (nb1 & 3)) & 15;
  const unsigned rot2 = ROT2 == 0 ? 0 : ROT2 == 1 ? (mb2 * 8) % 14 : ROT2 == 2 ? (mb2 * 2 + ((nb2 & 3) * 7) / 2) % 14 : ((nb2 & 3) * 7) / 2;
  WStream W1, W2;
  const unsigned region = lds0 + w * REG_W;
  {
    const unsigned r8 = lane >> 3, pc = (lane & 7) ^ (r8 & 6);
    const unsigned q = (lane >> 2) & 3, p = lane & 3, row = 4 * g + q;
    const unsigned t0 = region + row * 128 + ((row >> 1) & 3) * 32 + p * 8;
    W1.srd = mk_srd(w1, S_ * F_ * 2);
    W2.srd = mk_srd(w2, g2 ? F_ * S_ * 2 : 0);
    W1.beg = w * (KW1 * 32) * F_ * 2 + nb1 * 128; W1.end = W1.beg + KW1 * 32 * F_ * 2; W1.cur = W1.beg + rot1 * (32 * F_ * 2);
    W2.beg = w * (KW2 * 32) * S_ * 2 + nb2 * 128; W2.end = W2.beg + KW2 * 32 * S_ * 2; W2.cur = W2.beg + rot2 * (32 * S_ * 2);
    W1.vo = r8 * F_ * 2 + pc * 16;
    W2.vo = r8 * S_ * 2 + pc * 16;
    W1.ring = W2.ring = region;
#pragma unroll
    for (int t = 0; t < 4; ++t) { W1.ta[t] = t0 ^ (t << 5); W2.ta[t] = t0 ^ (((t + w) & 3) << 5); }
  }
  if (g1) glds_slots<true, 0>(W1, make_iseq<PRE>{}); else waitv<0>();
  asm volatile("s_waitcnt vmcnt(%3)" : "+v"(ep), "+v"(xv[0]), "+v"(xv[1]) : "i"(4 * PRE) : "memory");
  GAP();
  STAMP(1);
  if (g1) glds_slots<true, PRE>(W1, make_iseq<MID>{});
  GAP();
  unsigned tlo, thi;
  {
    const unsigned long long e = (((unsigned long long)ep[1] << 32) | ep[0]) + 1ull;
    tlo = rfl((unsigned)e); thi = rfl((unsigned)(e >> 32));
  }
  {
    float f[2][8], m[2], qs[2];
#pragma unroll
    for (int r = 0; r < 2; ++r) {
#pragma unroll
      for (int i = 0; i < 4; ++i) { f[r][2 * i] = bflo(xv[r][i]); f[r][2 * i + 1] = bfhi(xv[r][i]); }
      m[r] = ((f[r][0] + f[r][1]) + (f[r][2] + f[r][3])) + ((f[r][4] + f[r][5]) + (f[r][6] + f[r][7]));
    }
    wave_sum2(m[0], m[1]);
#pragma unroll
    for (int r = 0; r < 2; ++r) {
      const float mu = m[r] * (1.0f / D_);
#pragma unroll
      for (int i = 0; i < 8; ++i) f[r][i] -= mu;
      qs[r] = ((f[r][0] * f[r][0] + f[r][1] * f[r][1]) + (f[r][2] * f[r][2] + f[r][3] * f[r][3])) + ((f[r][4] * f[r][4] + f[r][5] * f[r][5]) + (f[r][6] * f[r][6] + f[r][7] * f[r][7]));
      qs[r] = lane < 56 ? qs[r] : 0.f;
    }
    wave_sum2(qs[0], qs[1]);
    v4u o[2];
#pragma unroll
    for (int r = 0; r < 2; ++r) {
      const float rs = __builtin_amdgcn_rsqf(qs[r] * (1.0f / D_) + 1e-5f);
      o[r] = v4u{pack2(f[r][0] * rs, f[r][1] * rs), pack2(f[r][2] * rs, f[r][3] * rs), pack2(f[r][4] * rs, f[r][5] * rs), pack2(f[r][6] * rs, f[r][7] * rs)};
    }
    if (lane < 56) {
      const unsigned ro = w * REG_W + LN_OFF + (w & 1) * 128 + lane * 16;
      *(LAS v4u*)(smem + ro) = o[0];
      *(LAS v4u*)(smem + ro + LNP) = o[1];
    }
  }
  wg_barrier();
  PA(2);
  {
    v2u lo[2], hi[2];
    const unsigned q = (lane >> 2) & 3;
    const unsigned tlo_a = lds0 + (q >> 1) * (REG_W + 128) + LN_OFF + (q & 1) * LNP + g * 32 + (lane & 3) * 8 + w * 128, thi_a = tlo_a + 2 * REG_W;
    asm volatile("ds_read_b64_tr_b16 %0, %1 offset:0" : "=v"(lo[0]) : "v"(tlo_a) : "memory");
    asm volatile("ds_read_b64_tr_b16 %0, %1 offset:0" : "=v"(hi[0]) : "v"(thi_a) : "memory");
    asm volatile("ds_read_b64_tr_b16 %0, %1 offset:512" : "=v"(lo[1]) : "v"(tlo_a) : "memory");
    asm volatile("ds_read_b64_tr_b16 %0, %1 offset:512" : "=v"(hi[1]) : "v"(thi_a) : "memory");
    asm volatile("s_waitcnt lgkmcnt(0)" : "+v"(lo[0]), "+v"(lo[1]), "+v"(hi[0]), "+v"(hi[1])::"memory");
    const unsigned vo = g * 1024 + c * 16 + (b & 3) * 256;
    const unsigned so = H_OFF + (b >> 2) * KSTRIDE + w * 4096;
    store16_sc1(v4u{lo[0][0], lo[0][1], hi[0][0], hi[0][1]}, vo, srdWS, so);
    store16_sc1(v4u{lo[1][0], lo[1][1], hi[1][0], hi[1][1]}, w < 3 ? vo : OOB, srdWS, so + 16384);
    if (g1) {
      glds_slots<true, NB1>(W1, make_iseq<NA>{});
      PA(3);
      wpipe_steps<true, 0, 1>(W1, make_iseq<POST>{});
      PA(4);
      waitv<4 * (NA + POST)>();
      PA(5);
    } else {
      PA(3);
      waitv<0>();
      PA(5);
    }
    store8_sc1(v2u{tlo, thi}, lane < R1 ? lane * 8192 + w * 2048 + b * 8 : OOB, srdWS, HF_OFF);
  }
  STAMP(2);
  f32x4 acc[4][4];
  if (g1) {
    if (w != 0) __builtin_amdgcn_s_sleep(WSLEEP1);
    glds_slots<true, NB1 + NA>(W1, make_iseq<NS1 - NB1 - NA>{});
    wpipe_steps<true, POST, 2>(W1, make_iseq<7 - POST>{});
    v2u bv[4];
    {
      const srd_t srdB1 = mk_srd(b1, F_ * 2);
      asm volatile("buffer_load_dwordx2 %0, %1, %2, %3 offen" : "=v"(bv[0]) : "v"(g * 8), "s"(srdB1), "s"(nb1 * 128u) : "memory");
      asm volatile("buffer_load_dwordx2 %0, %1, %2, %3 offen offset:32" : "=v"(bv[1]) : "v"(g * 8), "s"(srdB1), "s"(nb1 * 128u) : "memory");
      asm volatile("buffer_load_dwordx2 %0, %1, %2, %3 offen offset:64" : "=v"(bv[2]) : "v"(g * 8), "s"(srdB1), "s"(nb1 * 128u) : "memory");
      asm volatile("buffer_load_dwordx2 %0, %1, %2, %3 offen offset:96" : "=v"(bv[3]) : "v"(g * 8), "s"(srdB1), "s"(nb1 * 128u) : "memory");
    }
    GAP();
    if (w == 0) {
      wtail_steps<true, 7, 4>(W1, make_iseq<KW1 - 7>{});
      STAMPX0(3);
      poll_flags2<PSLEEP, 2>(lane * 16, true, srdWS, HF_OFF + (b % R1) * 8192 + (mb1 & 3) * 2048, tlo, thi);
    }
    asm volatile("s_barrier" ::: "memory");
    STAMPA(4);
    AStream A;
    A.srd = srdWS; A.beg = H_OFF + (w * KW1 * 28 + 4 * mb1) * 1024; A.end = A.beg + KW1 * KSTRIDE; A.cur = A.beg + rot1 * KSTRIDE;
#pragma unroll
    for (int m = 0; m < 4; ++m) A.voff[m] = lane * 16 + ((m + w) & 3) * 1024;
    v4u ring[DEPTH1][4];
    gemm_prologue<true>(ring, A);
    if (w != 0) { wtail_steps<true, 7, 4 + 4 * DEPTH1>(W1, make_iseq<KW1 - 7>{}); STAMPX1(3); }
    GAP();
    asm volatile("s_waitcnt vmcnt(%4) lgkmcnt(0)" : "+v"(bv[0]), "+v"(bv[1]), "+v"(bv[2]), "+v"(bv[3]) : "i"(4 * DEPTH1) : "memory");
#pragma unroll
    for (int t = 0; t < 4; ++t) { const v2u q = bv[t]; const unsigned q0 = q[0], q1 = q[1]; acc[0][t] = f32x4{bflo(q0), bfhi(q0), bflo(q1), bfhi(q1)}; }
    asm volatile("s_nop 4" : "+v"(acc[0][0]), "+v"(acc[0][1]), "+v"(acc[0][2]), "+v"(acc[0][3]));
    gemm_body<true>(acc, ring, A, W2);
    STAMPA(5);
#pragma unroll
    for (int m = 1; m < 4; ++m)
#pragma unroll
      for (int t = 0; t < 4; ++t) *(LAS f32x4*)(smem + w * REG_W + (m - 1) * 4096 + t * 1024 + lane * 16) = acc[m][t];
    wg_barrier();
    {
      f32x4 r[3][4];
#pragma unroll
      for (int j = 1; j < 4; ++j)
#pragma unroll
        for (int t = 0; t < 4; ++t) r[j - 1][t] = *(const LAS f32x4*)(smem + ((w - j) & 3) * REG_W + (j - 1) * 4096 + t * 1024 + lane * 16);
      GAP();
      glds_slots<false, TPF>(W2, make_iseq<NS2 - TPF>{});
      GAP();
#pragma unroll
      for (int kk = 0; kk < 2; ++kk) {
        const f32x4 s0 = (acc[0][2 * kk] + r[0][2 * kk]) + (r[1][2 * kk] + r[2][2 * kk]);
        const f32x4 s1 = (acc[0][2 * kk + 1] + r[0][2 * kk + 1]) + (r[1][2 * kk + 1] + r[2][2 * kk + 1]);
        const v4u o = {pack2(gelu(s0[0]), gelu(s0[1])), pack2(gelu(s0[2]), gelu(s0[3])), pack2(gelu(s1[0]), gelu(s1[1])), pack2(gelu(s1[2]), gelu(s1[3]))};
        store16_sc1(o, lane * 16, srdWS, U_OFF + ((2 * nb1 + kk) * 28 + 4 * mb1 + w) * 1024);
      }
    }
    GAP();
    wpipe_steps<false, 0, 1>(W2, make_iseq<POST2>{});
    waitv<4 * POST2>();
    store8_sc1(v2u{tlo, thi}, lane < R2 ? lane * UF_REP + (mb1 * 4 + nb1 / 7) * 256 + ((nb1 % 7) * 4 + w) * 8 : OOB, srdWS, UF_OFF);
    STAMP(6);
    if (!g2) {
      if (b == EPOCH_WG && w == 1) store8_sc1(v2u{tlo, thi}, lane == 0 ? 0 : OOB, srdWS, EP_OFF);
      waitv<0>();
      goto done;
    }
    if (w != 0) __builtin_amdgcn_s_sleep(WSLEEP2);
    wpipe_steps<false, POST2, 2>(W2, make_iseq<NS2 - POST2>{});
  } else {
    wg_barrier();
    if (!g2) goto done;
#pragma unroll 1
    for (int i = 0; i < IDLE_SLEEP; ++i) __builtin_amdgcn_s_sleep(16);
    glds_slots<false, 0>(W2, make_iseq<NS2>{});
    wpipe_steps<false, 0, 0>(W2, make_iseq<KW2>{});
    if (w == 0) poll_flags<8>(0, srdWS, EP_OFF, tlo, thi);
    wg_barrier();
  }
  {
    unsigned bv;
    {
      const srd_t srdB2 = mk_srd(b2, S_ * 2);
      asm volatile("buffer_load_ushort %0, %1, %2, %3 offen" : "=v"(bv) : "v"((16 * w + c) * 2), "s"(srdB2), "s"(nb2 * 128u) : "memory");
    }
    GAP();
    if (w == 0) {
      if (g1) wtail_steps<false, NS2, 1>(W2, make_iseq<KW2 - NS2>{});
      poll_flags2<PSLEEP, 1>(lane * 16, c < 14, srdWS, UF_OFF + (nb2 % R2) * UF_REP + mb2 * 1024, tlo, thi);
    }
    asm volatile("s_barrier" ::: "memory");
    STAMPB(4);
    AStream A;
    A.srd = srdWS; A.beg = U_OFF + (w * KW2 * 28 + 4 * mb2) * 1024; A.end = A.beg + KW2 * KSTRIDE; A.cur = A.beg + rot2 * KSTRIDE;
    A.voff[0] = A.voff[1] = A.voff[2] = A.voff[3] = lane * 16;
    v4u ring[DEPTH2][4];
    gemm_prologue<false>(ring, A);
    if (w != 0 && g1) wtail_steps<false, NS2, 1 + 4 * DEPTH2>(W2, make_iseq<KW2 - NS2>{});
    STAMPB(3);
    GAP();
    asm volatile("s_waitcnt vmcnt(%1) lgkmcnt(0)" : "+v"(bv) : "i"(4 * DEPTH2) : "memory");
    {
      const float bb = bflo(bv);
#pragma unroll
      for (int m = 0; m < 4; ++m) acc[m][0] = f32x4{bb, bb, bb, bb};
      asm volatile("s_nop 4" : "+v"(acc[0][0]), "+v"(acc[1][0]), "+v"(acc[2][0]), "+v"(acc[3][0]));
    }
    gemm_body<false>(acc, ring, A, W2);
    STAMPB(5);
#pragma unroll
    for (int t = 1; t < 4; ++t)
#pragma unroll
      for (int m = 0; m < 4; ++m) *(LAS f32x4*)(smem + w * REG_W + W2_OFF + (t - 1) * 4096 + m * 1024 + lane * 16) = acc[m][t];
    wg_barrier();
    const srd_t srdO = mk_srd(out, S_ * D_ * 2);
    unsigned pk[4][2];
#pragma unroll
    for (int m = 0; m < 4; ++m) {
      f32x4 r[3];
#pragma unroll
      for (int j = 1; j < 4; ++j) r[j - 1] = *(const LAS f32x4*)(smem + ((w - j) & 3) * REG_W + W2_OFF + (j - 1) * 4096 + m * 1024 + lane * 16);
      const f32x4 s = (acc[m][0] + r[0]) + (r[1] + r[2]);
      pk[m][0] = pack2(s[0], s[1]); pk[m][1] = pack2(s[2], s[3]);
    }
    const unsigned vo = (16 * w + c) * (D_ * 2) + (g & 1) * 32 + (g >> 1) * 16;
#pragma unroll
    for (int mp = 0; mp < 2; ++mp) {
      auto px = __builtin_amdgcn_permlane16_swap(pk[2 * mp][0], pk[2 * mp + 1][0], false, false);
      auto py = __builtin_amdgcn_permlane16_swap(pk[2 * mp][1], pk[2 * mp + 1][1], false, false);
      const unsigned x0 = px[0], x1 = px[1], y0 = py[0], y1 = py[1];
      const v4u o = {x0, y0, x1, y1};
      asm volatile("buffer_store_dwordx4 %0, %1, %2, %3 offen offset:%c4\n\ts_nop 1" ::"v"(o), "v"(vo), "s"(srdO), "s"(nb2 * 64 * (D_ * 2) + mb2 * 128), "i"(mp * 64) : "memory");
    }
  }
done:
  STAMP(7);
}

extern "C" void kernel_launch(void* const* d_in, const int* in_sizes, int n_in, void* d_out, int out_size, void* d_ws, size_t ws_size, hipStream_t stream) {
  mixer_kernel<<<256, 256, 0, stream>>>((const u16*)d_in[0], (const u16*)d_in[1], (const u16*)d_in[2], (const u16*)d_in[3], (const u16*)d_in[4],
                                       (u16*)d_out, (unsigned char*)d_ws);
}
```

```cpp
#include <hip/hip_runtime.h>
#include <stdint.h>
#include <utility>

typedef unsigned v4u __attribute__((ext_vector_type(4)));
typedef unsigned v2u __attribute__((ext_vector_type(2)));
typedef float f32x4 __attribute__((ext_vector_type(4)));
typedef unsigned short u16;
typedef __amdgpu_buffer_rsrc_t srd_t;
#define DI __device__ __forceinline__
#define LAS __attribute__((address_space(3)))
#define GAP() __builtin_amdgcn_sched_barrier(0)
template <int... Is> using iseq = std::integer_sequence<int, Is...>;
template <int N> using make_iseq = std::make_integer_sequence<int, N>;
#define ACLOB "a0","a1","a2","a3","a4","a5","a6","a7","a8","a9","a10","a11","a12","a13","a14","a15","a16","a17","a18","a19","a20","a21","a22","a23","a24","a25","a26","a27","a28","a29","a30","a31","a32","a33","a34","a35","a36","a37","a38","a39","a40","a41","a42","a43","a44","a45","a46","a47","a48","a49","a50","a51","a52","a53","a54","a55","a56","a57","a58","a59","a60","a61","a62","a63","a64","a65","a66","a67","a68","a69","a70","a71","a72","a73","a74","a75","a76","a77","a78","a79","a80","a81","a82","a83","a84","a85","a86","a87","a88","a89","a90","a91","a92","a93","a94","a95","a96","a97","a98","a99","a100","a101","a102","a103","a104","a105","a106","a107","a108","a109","a110","a111","a112","a113","a114","a115","a116","a117","a118","a119","a120","a121","a122","a123","a124","a125","a126","a127","a128","a129","a130","a131","a132","a133","a134","a135","a136","a137","a138","a139","a140","a141","a142","a143","a144","a145","a146","a147","a148","a149","a150","a151","a152","a153","a154","a155","a156","a157","a158","a159","a160","a161","a162","a163","a164","a165","a166","a167","a168","a169","a170","a171","a172","a173","a174","a175","a176","a177","a178","a179","a180","a181","a182","a183","a184","a185","a186","a187","a188","a189","a190","a191","a192","a193","a194","a195","a196","a197","a198","a199","a200","a201","a202","a203","a204","a205","a206","a207","a208","a209","a210","a211","a212","a213","a214","a215","a216","a217","a218","a219","a220","a221","a222","a223","a224","a225","a226","a227","a228","a229","a230","a231","a232","a233","a234","a235","a236","a237","a238","a239","a240","a241","a242","a243","a244","a245","a246","a247","a248","a249","a250","a251","a252","a253","a254","a255"

#define MAPPING 3
#define DF1 16
#define DF2 16
#define HB 5
#define W2POS 1
#define PRE 3
#define MID 0
#define NA 3
#define NA0 3
#define EARLYPOLL 1
#define POST2 0
#define ROT1 1
#define ROT2 1
#define ROTN1 2
#define ROTN2 2
#define IDLE_SLEEP 0
#define PSLEEP 2
#define QUIET 1
#define WSLEEP1 16
#define WSLEEP2 16
#define R1 4
#define R2 4

constexpr int S_ = 2048, D_ = 448, F_ = 1792;
constexpr int KW1 = 16, KW2 = 14;
constexpr int NS1 = 9, NS2 = 7;
#define TPFMAX 6
constexpr int TPF = DF1 / 4 < TPFMAX ? DF1 / 4 : TPFMAX;
constexpr unsigned KSTRIDE = 1024u;
constexpr unsigned HT = 64u * KSTRIDE, UT = 56u * KSTRIDE;
constexpr unsigned H_OFF = 0, U_OFF = 28u * HT;
constexpr unsigned EP_OFF = 4u << 20, HF_OFF = EP_OFF + 4096u, UF_OFF = HF_OFF + 16u * 8192u;
constexpr unsigned UF_REP = 28u * 256u;
constexpr unsigned WS_USED = UF_OFF + 16u * UF_REP;
constexpr unsigned REG_W = 40960, LN_OFF = 36864, LNP = 960, W2_OFF = 12288, LDS_BYTES = 4 * REG_W;
constexpr unsigned OOB = 0x7ffffff0u;
constexpr int SPIN_MAX = 1 << 15;
constexpr unsigned EPOCH_WG = 225;

DI unsigned rfl(unsigned x) { return __builtin_amdgcn_readfirstlane(x); }
DI float bflo(unsigned w) { return __builtin_bit_cast(float, w << 16); }
DI float bfhi(unsigned w) { return __builtin_bit_cast(float, w & 0xffff0000u); }
DI unsigned pack2(float lo, float hi) {
  typedef __bf16 bf2 __attribute__((ext_vector_type(2)));
  typedef float f2 __attribute__((ext_vector_type(2)));
  f2 v = {lo, hi};
  return __builtin_bit_cast(unsigned, __builtin_convertvector(v, bf2));
}
DI srd_t mk_srd(const void* p, unsigned bytes) { return __builtin_amdgcn_make_buffer_rsrc((void*)p, 0, bytes, 0x00020000); }
template <int C> DI float dpp_add(float v) {
  return v + __builtin_bit_cast(float, __builtin_amdgcn_update_dpp(0, __builtin_bit_cast(int, v), C, 0xf, 0xf, true));
}
DI void wave_sum2(float& a, float& b) {
  a = dpp_add<0xB1>(a); b = dpp_add<0xB1>(b); a = dpp_add<0x4E>(a); b = dpp_add<0x4E>(b);
  a = dpp_add<0x141>(a); b = dpp_add<0x141>(b); a = dpp_add<0x140>(a); b = dpp_add<0x140>(b);
  auto p = __builtin_amdgcn_permlane16_swap(__builtin_bit_cast(unsigned, a), __builtin_bit_cast(unsigned, b), false, false);
  const unsigned p0 = p[0], p1 = p[1];
  float s = __builtin_bit_cast(float, p0) + __builtin_bit_cast(float, p1);
  auto q = __builtin_amdgcn_permlane32_swap(__builtin_bit_cast(unsigned, s), __builtin_bit_cast(unsigned, s), false, false);
  const unsigned q0 = q[0], q1 = q[1];
  s = __builtin_bit_cast(float, q0) + __builtin_bit_cast(float, q1);
  a = __builtin_bit_cast(float, __builtin_amdgcn_readlane(__builtin_bit_cast(int, s), 0));
  b = __builtin_bit_cast(float, __builtin_amdgcn_readlane(__builtin_bit_cast(int, s), 16));
}
template <int N> DI void waitv() { static_assert(N >= 0 && N < 64, "vmcnt"); asm volatile("s_waitcnt vmcnt(%0)" ::"i"(N) : "memory"); }
DI void wait_lds() { asm volatile("s_waitcnt lgkmcnt(0)" ::: "memory"); }
DI void wg_barrier() { asm volatile("s_waitcnt lgkmcnt(0)\n\ts_barrier" ::: "memory"); }

struct WStream {
  srd_t srd;
  unsigned cur, beg, end;
  unsigned vo;
  unsigned ring;
  unsigned ta[4];
};
DI void cyc_add(unsigned& cur, unsigned inc, unsigned beg, unsigned end) {
  asm volatile("s_add_u32 %0, %0, %1\n\ts_cmp_eq_u32 %0, %3\n\ts_cselect_b32 %0, %2, %0" : "+s"(cur) : "s"(inc), "s"(beg), "s"(end) : "scc");
}
template <bool W1> struct WP { static constexpr int LDW = W1 ? F_ : S_, KW = W1 ? KW1 : KW2, NS = W1 ? NS1 : NS2, OFF = W1 ? 0 : (int)W2_OFF; };
template <bool W1, int KSTEP, int I> DI void glds_piece(WStream& s) {
  using P = WP<W1>;
  unsigned t;
  asm volatile("s_add_u32 m0, %1, %2\n\ts_add_u32 %0, %3, %4\n\tbuffer_load_dwordx4 %5, %6, %0 offen lds"
               : "=&s"(t)
               : "s"(s.ring), "i"(P::OFF + (KSTEP % P::NS) * 4096 + I * 1024), "s"(s.cur), "i"(I * 8 * P::LDW * 2), "v"(s.vo), "s"(s.srd)
               : "scc", "memory");
  if constexpr (I == 3) cyc_add(s.cur, 32 * P::LDW * 2, s.beg, s.end);
}
template <bool W1, int KSTEP> DI void glds_slot(WStream& s) {
  glds_piece<W1, KSTEP, 0>(s); glds_piece<W1, KSTEP, 1>(s); glds_piece<W1, KSTEP, 2>(s); glds_piece<W1, KSTEP, 3>(s);
}
template <bool W1, int KSTEP, int T, int H> DI void tr_piece(const WStream& s) {
  using P = WP<W1>;
  asm volatile("ds_read_b64_tr_b16 a[%c1:%c2], %0 offset:%c3" ::"v"(s.ta[T]), "i"(16 * KSTEP + 4 * T + 2 * H), "i"(16 * KSTEP + 4 * T + 2 * H + 1),
               "i"(P::OFF + (KSTEP % P::NS) * 4096 + H * 2048) : "memory", ACLOB);
}
template <bool W1, int KSTEP> DI void tr_slot(const WStream& s) {
  tr_piece<W1, KSTEP, 0, 0>(s); tr_piece<W1, KSTEP, 0, 1>(s); tr_piece<W1, KSTEP, 1, 0>(s); tr_piece<W1, KSTEP, 1, 1>(s);
  tr_piece<W1, KSTEP, 2, 0>(s); tr_piece<W1, KSTEP, 2, 1>(s); tr_piece<W1, KSTEP, 3, 0>(s); tr_piece<W1, KSTEP, 3, 1>(s);
}
constexpr int NB1 = PRE + MID;
static_assert(NB1 + NA <= NS1 && NB1 + NA0 <= NS1 && POST2 <= NS2, "schedule");
template <bool W1, int NAX> constexpr int younger(int j, bool flagged) {
  constexpr int KW = W1 ? KW1 : KW2, NS = W1 ? NS1 : NS2;
  const int before_stores = W1 ? NB1 : (W2POS ? TPF : NS2), before_flag = W1 ? NB1 + NAX : NS2 + POST2;
  const int last = j + NS - 1 < KW - 1 ? j + NS - 1 : KW - 1;
  return 4 * (last - j) + (j < before_stores ? 2 : 0) + (flagged && j < before_flag ? 1 : 0);
}
template <bool W1, int J, int MODE, int EXTRA, int NAX> DI void wpipe_step(WStream& s) {
  using P = WP<W1>;
  constexpr int rem = P::KW - 1 - J;
  waitv<(MODE == 0 ? 4 * (rem < P::NS - 1 ? rem : P::NS - 1) : younger<W1, NAX>(J, MODE == 2)) + EXTRA>();
  tr_slot<W1, J>(s);
  if constexpr (J + P::NS < P::KW) { wait_lds(); glds_slot<W1, J + P::NS>(s); }
  GAP();
}
template <bool W1, int J0, int MODE, int NAX, int... Js> DI void wpipe_stepsx(WStream& s, iseq<Js...>) { (wpipe_step<W1, J0 + Js, MODE, 0, NAX>(s), ...); }
template <bool W1, int J0, int MODE, int... Js> DI void wpipe_steps(WStream& s, iseq<Js...>) { (wpipe_step<W1, J0 + Js, MODE, 0, NA>(s), ...); }
template <bool W1, int J0, int EXTRA, int NAX, int... Js> DI void wtail_steps(WStream& s, iseq<Js...>) { (wpipe_step<W1, J0 + Js, 2, EXTRA, NAX>(s), ...); }
template <bool W1, int J0, int... Js> DI void glds_slots(WStream& s, iseq<Js...>) { (glds_slot<W1, J0 + Js>(s), ...); }

constexpr int RB = 160;
static_assert(RB + 4 * DF1 <= 256 && RB + 4 * DF2 <= 256, "ring");
template <int REG> DI void act_load(unsigned voff, srd_t srd, unsigned soff) {
  asm volatile("buffer_load_dwordx4 v[%c0:%c1], %2, %3, %4 offen sc1" ::"i"(REG), "i"(REG + 3), "v"(voff), "s"(srd), "s"(soff) : "memory");
}
struct AStream { srd_t srd; unsigned voff[4], cur, beg, end; };
template <bool WA, bool ZERO, int AR, int VR> DI void mfma(f32x4& acc) {
  if constexpr (WA) {
    if constexpr (ZERO) asm volatile("v_mfma_f32_16x16x32_bf16 %0, a[%c1:%c2], v[%c3:%c4], 0" : "=&v"(acc) : "i"(AR), "i"(AR + 3), "i"(VR), "i"(VR + 3) : ACLOB);
    else asm volatile("v_mfma_f32_16x16x32_bf16 %0, a[%c1:%c2], v[%c3:%c4], %0" : "+v"(acc) : "i"(AR), "i"(AR + 3), "i"(VR), "i"(VR + 3) : ACLOB);
  } else {
    if constexpr (ZERO) asm volatile("v_mfma_f32_16x16x32_bf16 %0, v[%c3:%c4], a[%c1:%c2], 0" : "=&v"(acc) : "i"(AR), "i"(AR + 3), "i"(VR), "i"(VR + 3) : ACLOB);
    else asm volatile("v_mfma_f32_16x16x32_bf16 %0, v[%c3:%c4], a[%c1:%c2], %0" : "+v"(acc) : "i"(AR), "i"(AR + 3), "i"(VR), "i"(VR + 3) : ACLOB);
  }
}
template <bool G1> constexpr int vop(int v) { return v < 4 * (G1 ? KW1 : KW2) ? 1 : (G1 && v - 4 * KW1 < 4 * TPF ? 1 : 0); }
template <bool G1> constexpr int vop_range(int a, int b) { int n = 0; for (int v = a; v <= b; ++v) n += vop<G1>(v); return n; }
template <bool G1, int V> DI void stream_op(AStream& a, WStream& w2) {
  constexpr int KW = G1 ? KW1 : KW2, DF = G1 ? DF1 : DF2;
  if constexpr (V < 4 * KW) { act_load<RB + 4 * (V % DF)>(a.voff[(V / KW + 1) & 3], a.srd, a.cur); cyc_add(a.cur, KSTRIDE, a.beg, a.end); }
  else if constexpr (vop<G1>(V) != 0) glds_piece<false, (V - 4 * KW) / 4, (V - 4 * KW) % 4>(w2);
}
struct Exch { LAS unsigned char* mine; const LAS unsigned char* from[3]; };
template <bool G1, int I> DI void gemm_frag(f32x4 (&acc)[4][4], f32x4 (&pre)[4], f32x4 (&r)[3][4], AStream& a, WStream& w2, const Exch& x) {
  constexpr int KW = G1 ? KW1 : KW2, DF = G1 ? DF1 : DF2, K = I % KW, M = (I / KW + 1) & 3, VR = RB + 4 * (I % DF);
  waitv<I == 0 ? vop_range<G1>(1, DF - 1) : vop_range<G1>(I + 1, I - 2 + DF)>();
  mfma<G1, K == 0, 16 * K + 0, VR>(acc[M][0]);
  if constexpr (I >= 1) stream_op<G1, I - 1 + DF>(a, w2);
  mfma<G1, K == 0, 16 * K + 4, VR>(acc[M][1]);
  mfma<G1, K == 0, 16 * K + 8, VR>(acc[M][2]);
  mfma<G1, K == 0, 16 * K + 12, VR>(acc[M][3]);
  if constexpr (I == 4 * KW - 1) stream_op<G1, I + DF>(a, w2);
  if constexpr (I % KW == 1 && I > KW) {
    constexpr int m = I / KW;
    asm volatile("" : "+v"(acc[m][0]), "+v"(acc[m][1]), "+v"(acc[m][2]), "+v"(acc[m][3]));
#pragma unroll
    for (int t = 0; t < 4; ++t) *(LAS f32x4*)(x.mine + (m - 1) * 4096 + t * 1024) = acc[m][t];
  }
  if constexpr (I == 3 * KW + HB) {
    wg_barrier();
#pragma unroll
    for (int j = 0; j < 3; ++j)
#pragma unroll
      for (int t = 0; t < 4; ++t) r[j][t] = *(const LAS f32x4*)(x.from[j] + t * 1024);
  }
  if constexpr (I == 3 * KW + HB + 2) {
#pragma unroll
    for (int t = 0; t < 4; ++t) pre[t] += r[0][t];
  }
  if constexpr (I == 3 * KW + HB + 3) {
#pragma unroll
    for (int t = 0; t < 4; ++t) pre[t] += r[1][t];
  }
  if constexpr (I == 3 * KW + HB + 4) {
#pragma unroll
    for (int t = 0; t < 4; ++t) pre[t] += r[2][t];
  }
  GAP();
}
template <bool G1, int... Is> DI void gemm_frags(f32x4 (&acc)[4][4], f32x4 (&pre)[4], f32x4 (&r)[3][4], AStream& a, WStream& w2, const Exch& x, iseq<Is...>) {
  (gemm_frag<G1, Is>(acc, pre, r, a, w2, x), ...);
}
template <bool G1, int... Vs> DI void gemm_prologue(AStream& a, WStream& w2, iseq<Vs...>) { (stream_op<G1, Vs>(a, w2), ...); GAP(); }
template <bool G1> DI void gemm_body(f32x4 (&pre)[4], AStream& a, WStream& w2, const Exch& x) {
  f32x4 acc[4][4], r[3][4];
  gemm_frags<G1>(acc, pre, r, a, w2, x, make_iseq<4 * (G1 ? KW1 : KW2)>{});
  asm volatile("s_nop 7" : "+v"(acc[0][0]), "+v"(acc[0][1]), "+v"(acc[0][2]), "+v"(acc[0][3]));
#pragma unroll
  for (int t = 0; t < 4; ++t) pre[t] += acc[0][t];
}
template <int SLEEP> DI void poll_flags(unsigned voff, srd_t srd, unsigned soff, unsigned tlo, unsigned thi) {
#pragma unroll 1
  for (int spin = 0; spin < SPIN_MAX; ++spin) {
    v2u f;
    asm volatile("buffer_load_dwordx2 %0, %1, %2, %3 offen sc1\n\ts_waitcnt vmcnt(0)" : "=v"(f) : "v"(voff), "s"(srd), "s"(soff) : "memory");
    if (__builtin_amdgcn_ballot_w64(f[0] != tlo || f[1] != thi) == 0) break;
    __builtin_amdgcn_s_sleep(SLEEP);
  }
}
template <int N> DI void poll2_issue(v4u (&f)[2], unsigned voff, srd_t srd, unsigned soff) {
  asm volatile("buffer_load_dwordx4 %0, %1, %2, %3 offen sc1" : "=v"(f[0]) : "v"(voff), "s"(srd), "s"(soff) : "memory");
  if constexpr (N == 2) asm volatile("buffer_load_dwordx4 %0, %1, %2, %3 offen offset:1024 sc1" : "=v"(f[1]) : "v"(voff), "s"(srd), "s"(soff) : "memory");
}
template <int N> DI bool poll2_check(v4u (&f)[2], bool use, unsigned tlo, unsigned thi) {
  if constexpr (N == 2) asm volatile("s_waitcnt vmcnt(0)" : "+v"(f[0]), "+v"(f[1])::"memory");
  else asm volatile("s_waitcnt vmcnt(0)" : "+v"(f[0])::"memory");
  const v4u a = f[0];
  bool bad = a[0] != tlo || a[1] != thi || a[2] != tlo || a[3] != thi;
  if constexpr (N == 2) { const v4u c = f[1]; bad = bad || c[0] != tlo || c[1] != thi || c[2] != tlo || c[3] != thi; }
  return __builtin_amdgcn_ballot_w64(bad && use) == 0;
}
template <int SLEEP, int N> DI void poll_flags2(unsigned voff, bool use, srd_t srd, unsigned soff, unsigned tlo, unsigned thi) {
#pragma unroll 1
  for (int spin = 0; spin < SPIN_MAX; ++spin) {
    v4u f[2];
    poll2_issue<N>(f, voff, srd, soff);
    if (poll2_check<N>(f, use, tlo, thi)) break;
    __builtin_amdgcn_s_sleep(SLEEP);
  }
}
DI void store16_sc1(v4u d, unsigned voff, srd_t srd, unsigned soff) {
  asm volatile("buffer_store_dwordx4 %0, %1, %2, %3 offen sc1\n\ts_nop 1" ::"v"(d), "v"(voff), "s"(srd), "s"(soff) : "memory");
}
DI void store8_sc1(v2u d, unsigned voff, srd_t srd, unsigned soff) {
  asm volatile("buffer_store_dwordx2 %0, %1, %2, %3 offen sc1\n\ts_nop 1" ::"v"(d), "v"(voff), "s"(srd), "s"(soff) : "memory");
}
DI float gelu(float u) {
  const float e = __builtin_amdgcn_exp2f(u * (-2.3022082f - 0.1029432f * u * u));
  return u * __builtin_amdgcn_rcpf(1.0f + e);
}
#define PA(i)
#define PB(i)
#define STAMP(i)
#define STAMPA(i)
#define STAMPB(i)
#define STAMPX0(i)
#define STAMPX1(i)

__global__ void __launch_bounds__(256, 1) __attribute__((amdgpu_num_vgpr(RB)))
mixer_kernel(const u16* __restrict__ x, const u16* __restrict__ w1, const u16* __restrict__ b1, const u16* __restrict__ w2, const u16* __restrict__ b2,
             u16* __restrict__ out, unsigned char* __restrict__ ws) {
  __shared__ __attribute__((aligned(1024))) unsigned char smem[LDS_BYTES];
  STAMP(0);
  asm volatile("" ::: ACLOB);
  asm volatile("" ::: "v160", "v255");
  const unsigned tid = threadIdx.x, lane = tid & 63, w = rfl(tid >> 6), b = blockIdx.x;
  const unsigned lds0 = (unsigned)(uintptr_t)(LAS unsigned char*)smem;
  const srd_t srdWS = mk_srd(ws, WS_USED);
  const unsigned g = lane >> 4, c = lane & 15;

  v2u ep; v4u xv[2];
  {
    const srd_t srdX = mk_srd(x, S_ * D_ * 2);
    const unsigned zero = 0, vx = lane < 56 ? lane * 16 : OOB, row = 32 * (b >> 2) + 4 * (b & 3) + 2 * (w & 1) + 16 * (w >> 1);
    asm volatile("buffer_load_dwordx4 %0, %1, %2, %3 offen" : "=v"(xv[0]) : "v"(vx), "s"(srdX), "s"(row * 896u) : "memory");
    asm volatile("buffer_load_dwordx4 %0, %1, %2, %3 offen offset:896" : "=v"(xv[1]) : "v"(vx), "s"(srdX), "s"(row * 896u) : "memory");
    asm volatile("buffer_load_dwordx2 %0, %1, %2, %3 offen sc1" : "=v"(ep) : "v"(zero), "s"(srdWS), "s"(EP_OFF) : "memory");
  }
  GAP();
  const unsigned xcd = b & 7, li = b >> 3;
  bool g1, g2;
  unsigned mb1, nb1, mb2, nb2;
  {
    const unsigned j = xcd >> 1, odd = xcd & 1, i0 = 7 + odd;
    g1 = li >= i0;
    const unsigned idx = g1 ? li - i0 + (odd ? 25 : 0) : 0;
    nb1 = 7 * j + idx / 7; mb1 = idx % 7;
    g2 = li < 28; nb2 = 4 * xcd + (li & 3); mb2 = g2 ? li >> 2 : 0;
  }
  const unsigned rot1 = ROT1 == 0 ? 0 : ROT1 == 1 ? (mb1 * 16) / 7 : ROT1 == 2 ? ((mb1 * 16) / 7 + 4 * (nb1 & 3)) & 15 : ROT1 == 3 ? (4 * (nb1 & 3)) & 15 : (((mb1 * ROTN1) / 7) * 16) / ROTN1;
  const unsigned rot2 = ROT2 == 0 ? 0 : ROT2 == 1 ? (mb2 * 8) % 14 : ROT2 == 2 ? (mb2 * 2 + ((nb2 & 3) * 7) / 2) % 14 : ROT2 == 3 ? ((nb2 & 3) * 7) / 2 : (((mb2 * ROTN2) / 7) * 14) / ROTN2;
  WStream W1, W2;
  const unsigned region = lds0 + w * REG_W;
  {
    const unsigned r8 = lane >> 3, pc = (lane & 7) ^ (r8 & 6);
    const unsigned q = (lane >> 2) & 3, p = lane & 3, row = 4 * g + q;
    const unsigned t0 = region + row * 128 + ((row >> 1) & 3) * 32 + p * 8;
    W1.srd = mk_srd(w1, S_ * F_ * 2);
    W2.srd = mk_srd(w2, g2 ? F_ * S_ * 2 : 0);
    W1.beg = w * (KW1 * 32) * F_ * 2 + nb1 * 128; W1.end = W1.beg + KW1 * 32 * F_ * 2; W1.cur = W1.beg + rot1 * (32 * F_ * 2);
    W2.beg = w * (KW2 * 32) * S_ * 2 + nb2 * 128; W2.end = W2.beg + KW2 * 32 * S_ * 2; W2.cur = W2.beg + rot2 * (32 * S_ * 2);
    W1.vo = r8 * F_ * 2 + pc * 16;
    W2.vo = r8 * S_ * 2 + pc * 16;
    W1.ring = W2.ring = region;
#pragma unroll
    for (int t = 0; t < 4; ++t) { W1.ta[t] = W2.ta[t] = t0 ^ (t << 5); }
  }
  if (g1) glds_slots<true, 0>(W1, make_iseq<PRE>{}); else waitv<0>();
  asm volatile("s_waitcnt vmcnt(%3)" : "+v"(ep), "+v"(xv[0]), "+v"(xv[1]) : "i"(4 * PRE) : "memory");
  GAP();
  STAMP(1);
  if (g1) glds_slots<true, PRE>(W1, make_iseq<MID>{});
  GAP();
  unsigned tlo, thi;
  {
    const unsigned long long e = (((unsigned long long)ep[1] << 32) | ep[0]) + 1ull;
    tlo = rfl((unsigned)e); thi = rfl((unsigned)(e >> 32));
  }
  {
    float f[2][8], m[2], qs[2];
#pragma unroll
    for (int r = 0; r < 2; ++r) {
#pragma unroll
      for (int i = 0; i < 4; ++i) { f[r][2 * i] = bflo(xv[r][i]); f[r][2 * i + 1] = bfhi(xv[r][i]); }
      m[r] = ((f[r][0] + f[r][1]) + (f[r][2] + f[r][3])) + ((f[r][4] + f[r][5]) + (f[r][6] + f[r][7]));
    }
    wave_sum2(m[0], m[1]);
#pragma unroll
    for (int r = 0; r < 2; ++r) {
      const float mu = m[r] * (1.0f / D_);
#pragma unroll
      for (int i = 0; i < 8; ++i) f[r][i] -= mu;
      qs[r] = ((f[r][0] * f[r][0] + f[r][1] * f[r][1]) + (f[r][2] * f[r][2] + f[r][3] * f[r][3])) + ((f[r][4] * f[r][4] + f[r][5] * f[r][5]) + (f[r][6] * f[r][6] + f[r][7] * f[r][7]));
      qs[r] = lane < 56 ? qs[r] : 0.f;
    }
    wave_sum2(qs[0], qs[1]);
    v4u o[2];
#pragma unroll
    for (int r = 0; r < 2; ++r) {
      const float rs = __builtin_amdgcn_rsqf(qs[r] * (1.0f / D_) + 1e-5f);
      o[r] = v4u{pack2(f[r][0] * rs, f[r][1] * rs), pack2(f[r][2] * rs, f[r][3] * rs), pack2(f[r][4] * rs, f[r][5] * rs), pack2(f[r][6] * rs, f[r][7] * rs)};
    }
    if (lane < 56) {
      const unsigned ro = w * REG_W + LN_OFF + (w & 1) * 128 + lane * 16;
      *(LAS v4u*)(smem + ro) = o[0];
      *(LAS v4u*)(smem + ro + LNP) = o[1];
    }
  }
  wg_barrier();
  {
    v2u lo[2], hi[2];
    const unsigned q = (lane >> 2) & 3;
    const unsigned tlo_a = lds0 + (q >> 1) * (REG_W + 128) + LN_OFF + (q & 1) * LNP + g * 32 + (lane & 3) * 8 + w * 128, thi_a = tlo_a + 2 * REG_W;
    asm volatile("ds_read_b64_tr_b16 %0, %1 offset:0" : "=v"(lo[0]) : "v"(tlo_a) : "memory");
    asm volatile("ds_read_b64_tr_b16 %0, %1 offset:0" : "=v"(hi[0]) : "v"(thi_a) : "memory");
    asm volatile("ds_read_b64_tr_b16 %0, %1 offset:512" : "=v"(lo[1]) : "v"(tlo_a) : "memory");
    asm volatile("ds_read_b64_tr_b16 %0, %1 offset:512" : "=v"(hi[1]) : "v"(thi_a) : "memory");
    asm volatile("s_waitcnt lgkmcnt(0)" : "+v"(lo[0]), "+v"(lo[1]), "+v"(hi[0]), "+v"(hi[1])::"memory");
    const unsigned vo = g * HT + c * 16 + (b & 3) * 256;
    const unsigned so = H_OFF + (b >> 2) * KSTRIDE + w * 4 * HT;
    store16_sc1(v4u{lo[0][0], lo[0][1], hi[0][0], hi[0][1]}, vo, srdWS, so);
    store16_sc1(v4u{lo[1][0], lo[1][1], hi[1][0], hi[1][1]}, w < 3 ? vo : OOB, srdWS, so + 16 * HT);
    const unsigned vf = lane < R1 ? lane * 8192 + w * 2048 + b * 8 : OOB;
    if (!g1) {
      waitv<0>();
      store8_sc1(v2u{tlo, thi}, vf, srdWS, HF_OFF);
      STAMP(2);
    } else if (w == 0) {
      glds_slots<true, NB1>(W1, make_iseq<NA0>{});
      waitv<4 * NA0>();
      store8_sc1(v2u{tlo, thi}, vf, srdWS, HF_OFF);
      STAMP(2);
      glds_slots<true, NB1 + NA0>(W1, make_iseq<NS1 - NB1 - NA0>{});
      wpipe_stepsx<true, 0, 2, NA0>(W1, make_iseq<7>{});
    } else {
      glds_slots<true, NB1>(W1, make_iseq<NA>{});
      waitv<4 * NA>();
      store8_sc1(v2u{tlo, thi}, vf, srdWS, HF_OFF);
      STAMP(2);
      if (WSLEEP1) __builtin_amdgcn_s_sleep(WSLEEP1);
      glds_slots<true, NB1 + NA>(W1, make_iseq<NS1 - NB1 - NA>{});
      wpipe_stepsx<true, 0, 2, NA>(W1, make_iseq<7>{});
    }
  }
  Exch X;
  X.mine = (LAS unsigned char*)smem + w * REG_W + lane * 16;
#pragma unroll
  for (int j = 0; j < 3; ++j) X.from[j] = (const LAS unsigned char*)smem + ((w - 1 - j) & 3) * REG_W + j * 4096 + lane * 16;
  if (g1) {
    v2u bv[4];
    {
      const srd_t srdB1 = mk_srd(b1, F_ * 2);
      asm volatile("buffer_load_dwordx2 %0, %1, %2, %3 offen" : "=v"(bv[0]) : "v"(g * 8), "s"(srdB1), "s"(nb1 * 128u) : "memory");
      asm volatile("buffer_load_dwordx2 %0, %1, %2, %3 offen offset:32" : "=v"(bv[1]) : "v"(g * 8), "s"(srdB1), "s"(nb1 * 128u) : "memory");
      asm volatile("buffer_load_dwordx2 %0, %1, %2, %3 offen offset:64" : "=v"(bv[2]) : "v"(g * 8), "s"(srdB1), "s"(nb1 * 128u) : "memory");
      asm volatile("buffer_load_dwordx2 %0, %1, %2, %3 offen offset:96" : "=v"(bv[3]) : "v"(g * 8), "s"(srdB1), "s"(nb1 * 128u) : "memory");
    }
    GAP();
    if (w == 0) {
      const unsigned so = HF_OFF + (b % R1) * 8192 + (mb1 & 3) * 2048;
      v4u f[2];
      poll2_issue<2>(f, lane * 16, srdWS, so);
      wtail_steps<true, 7, 6, NA0>(W1, make_iseq<KW1 - 7>{});
      STAMPX0(3);
      if (!poll2_check<2>(f, true, tlo, thi)) poll_flags2<PSLEEP, 2>(lane * 16, true, srdWS, so, tlo, thi);
    }
    asm volatile("s_barrier" ::: "memory");
    STAMPA(4);
    AStream A;
    A.srd = srdWS; A.beg = H_OFF + 4 * mb1 * HT + w * KW1 * KSTRIDE; A.end = A.beg + KW1 * KSTRIDE; A.cur = A.beg + rot1 * KSTRIDE;
#pragma unroll
    for (int m = 0; m < 4; ++m) A.voff[m] = lane * 16 + ((m + w) & 3) * HT;
    gemm_prologue<true>(A, W2, make_iseq<DF1>{});
    if (w != 0) { wtail_steps<true, 7, 4 + DF1, NA>(W1, make_iseq<KW1 - 7>{}); STAMPX1(3); }
    GAP();
    asm volatile("s_waitcnt vmcnt(%4) lgkmcnt(0)" : "+v"(bv[0]), "+v"(bv[1]), "+v"(bv[2]), "+v"(bv[3]) : "i"(DF1) : "memory");
    f32x4 s[4];
#pragma unroll
    for (int t = 0; t < 4; ++t) { const v2u q = bv[t]; const unsigned q0 = q[0], q1 = q[1]; s[t] = f32x4{bflo(q0), bfhi(q0), bflo(q1), bfhi(q1)}; }
    GAP();
    gemm_body<true>(s, A, W2, X);
    STAMPA(5);
    if (!W2POS) glds_slots<false, TPF>(W2, make_iseq<NS2 - TPF>{});
    GAP();
#pragma unroll
    for (int kk = 0; kk < 2; ++kk) {
      const f32x4 s0 = s[2 * kk], s1 = s[2 * kk + 1];
      const v4u o = {pack2(gelu(s0[0]), gelu(s0[1])), pack2(gelu(s0[2]), gelu(s0[3])), pack2(gelu(s1[0]), gelu(s1[1])), pack2(gelu(s1[2]), gelu(s1[3]))};
      store16_sc1(o, lane * 16, srdWS, U_OFF + (4 * mb1 + w) * UT + (2 * nb1 + kk) * KSTRIDE);
    }
    GAP();
    if (W2POS) glds_slots<false, TPF>(W2, make_iseq<NS2 - TPF>{});
    wpipe_steps<false, 0, 1>(W2, make_iseq<POST2>{});
    waitv<4 * POST2 + (W2POS ? 4 * (NS2 - TPF) : 0)>();
    store8_sc1(v2u{tlo, thi}, lane < R2 ? lane * UF_REP + (mb1 * 4 + nb1 / 7) * 256 + ((nb1 % 7) * 4 + w) * 8 : OOB, srdWS, UF_OFF);
    STAMP(6);
    if (!g2) {
      if (b == EPOCH_WG && w == 1) store8_sc1(v2u{tlo, thi}, lane == 0 ? 0 : OOB, srdWS, EP_OFF);
      waitv<0>();
      goto done;
    }
    if (w != 0) __builtin_amdgcn_s_sleep(WSLEEP2);
    wpipe_steps<false, POST2, 2>(W2, make_iseq<NS2 - POST2>{});
  } else {
    wg_barrier();
    if (!g2) goto done;
#pragma unroll 1
    for (int i = 0; i < IDLE_SLEEP; ++i) __builtin_amdgcn_s_sleep(16);
    glds_slots<false, 0>(W2, make_iseq<NS2>{});
    wpipe_steps<false, 0, 0>(W2, make_iseq<KW2>{});
    if (w == 0) poll_flags<8>(0, srdWS, EP_OFF, tlo, thi);
    wg_barrier();
  }
  {
    unsigned bv[4];
    {
      const srd_t srdB2 = mk_srd(b2, S_ * 2);
      asm volatile("buffer_load_ushort %0, %1, %2, %3 offen" : "=v"(bv[0]) : "v"(c * 2), "s"(srdB2), "s"(nb2 * 128u) : "memory");
      asm volatile("buffer_load_ushort %0, %1, %2, %3 offen offset:32" : "=v"(bv[1]) : "v"(c * 2), "s"(srdB2), "s"(nb2 * 128u) : "memory");
      asm volatile("buffer_load_ushort %0, %1, %2, %3 offen offset:64" : "=v"(bv[2]) : "v"(c * 2), "s"(srdB2), "s"(nb2 * 128u) : "memory");
      asm volatile("buffer_load_ushort %0, %1, %2, %3 offen offset:96" : "=v"(bv[3]) : "v"(c * 2), "s"(srdB2), "s"(nb2 * 128u) : "memory");
    }
    GAP();
    if (w == 0) {
      const unsigned so = UF_OFF + (nb2 % R2) * UF_REP + mb2 * 1024;
      v4u f[2];
      poll2_issue<1>(f, lane * 16, srdWS, so);
      if (g1) wtail_steps<false, NS2, 5, NA>(W2, make_iseq<KW2 - NS2>{});
      if (!poll2_check<1>(f, c < 14, tlo, thi)) poll_flags2<PSLEEP, 1>(lane * 16, c < 14, srdWS, so, tlo, thi);
    }
    asm volatile("s_barrier" ::: "memory");
    STAMPB(4);
    AStream A;
    A.srd = srdWS; A.beg = U_OFF + 4 * mb2 * UT + w * KW2 * KSTRIDE; A.end = A.beg + KW2 * KSTRIDE; A.cur = A.beg + rot2 * KSTRIDE;
#pragma unroll
    for (int m = 0; m < 4; ++m) A.voff[m] = lane * 16 + ((m + w) & 3) * UT;
    gemm_prologue<false>(A, W2, make_iseq<DF2>{});
    if (w != 0 && g1) wtail_steps<false, NS2, 4 + DF2, NA>(W2, make_iseq<KW2 - NS2>{});
    STAMPB(3);
    GAP();
    asm volatile("s_waitcnt vmcnt(%4) lgkmcnt(0)" : "+v"(bv[0]), "+v"(bv[1]), "+v"(bv[2]), "+v"(bv[3]) : "i"(DF2) : "memory");
    f32x4 s[4];
#pragma unroll
    for (int t = 0; t < 4; ++t) { const float bb = bflo(bv[t]); s[t] = f32x4{bb, bb, bb, bb}; }
    GAP();
    gemm_body<false>(s, A, W2, X);
    STAMPB(5);
    unsigned char* stage = smem + W2_OFF;
#pragma unroll
    for (int t = 0; t < 4; ++t) *(LAS v2u*)((LAS unsigned char*)stage + (16 * t + c) * 144 + 32 * w + 8 * g) = v2u{pack2(s[t][0], s[t][1]), pack2(s[t][2], s[t][3])};
    wg_barrier();
    const srd_t srdO = mk_srd(out, S_ * D_ * 2);
    const unsigned row = 16 * w + (lane >> 3), col = (lane & 7) * 16;
#pragma unroll
    for (int mp = 0; mp < 2; ++mp) {
      const v4u o = *(const LAS v4u*)((LAS unsigned char*)stage + (row + 8 * mp) * 144 + col);
      asm volatile("buffer_store_dwordx4 %0, %1, %2, %3 offen\n\ts_nop 1" ::"v"(o), "v"((row + 8 * mp) * (D_ * 2) + col), "s"(srdO), "s"(nb2 * 64 * (D_ * 2) + mb2 * 128) : "memory");
    }
  }
done:
  STAMP(7);
}

extern "C" void kernel_launch(void* const* d_in, const int* in_sizes, int n_in, void* d_out, int out_size, void* d_ws, size_t ws_size, hipStream_t stream) {
  mixer_kernel<<<256, 256, 0, stream>>>((const u16*)d_in[0], (const u16*)d_in[1], (const u16*)d_in[2], (const u16*)d_in[3], (const u16*)d_in[4],
                                       (u16*)d_out, (unsigned char*)d_ws);
}
```

```cpp
#include <hip/hip_runtime.h>
#include <stdint.h>
#include <utility>

typedef unsigned v4u __attribute__((ext_vector_type(4)));
typedef unsigned v2u __attribute__((ext_vector_type(2)));
typedef float f32x4 __attribute__((ext_vector_type(4)));
typedef unsigned short u16;
typedef __amdgpu_buffer_rsrc_t srd_t;
#define DI __device__ __forceinline__
#define LAS __attribute__((address_space(3)))
#define GAP() __builtin_amdgcn_sched_barrier(0)
template <int... Is> using iseq = std::integer_sequence<int, Is...>;
template <int N> using make_iseq = std::make_integer_sequence<int, N>;
#define ACLOB "a0","a1","a2","a3","a4","a5","a6","a7","a8","a9","a10","a11","a12","a13","a14","a15","a16","a17","a18","a19","a20","a21","a22","a23","a24","a25","a26","a27","a28","a29","a30","a31","a32","a33","a34","a35","a36","a37","a38","a39","a40","a41","a42","a43","a44","a45","a46","a47","a48","a49","a50","a51","a52","a53","a54","a55","a56","a57","a58","a59","a60","a61","a62","a63","a64","a65","a66","a67","a68","a69","a70","a71","a72","a73","a74","a75","a76","a77","a78","a79","a80","a81","a82","a83","a84","a85","a86","a87","a88","a89","a90","a91","a92","a93","a94","a95","a96","a97","a98","a99","a100","a101","a102","a103","a104","a105","a106","a107","a108","a109","a110","a111","a112","a113","a114","a115","a116","a117","a118","a119","a120","a121","a122","a123","a124","a125","a126","a127","a128","a129","a130","a131","a132","a133","a134","a135","a136","a137","a138","a139","a140","a141","a142","a143","a144","a145","a146","a147","a148","a149","a150","a151","a152","a153","a154","a155","a156","a157","a158","a159","a160","a161","a162","a163","a164","a165","a166","a167","a168","a169","a170","a171","a172","a173","a174","a175","a176","a177","a178","a179","a180","a181","a182","a183","a184","a185","a186","a187","a188","a189","a190","a191","a192","a193","a194","a195","a196","a197","a198","a199","a200","a201","a202","a203","a204","a205","a206","a207","a208","a209","a210","a211","a212","a213","a214","a215","a216","a217","a218","a219","a220","a221","a222","a223","a224","a225","a226","a227","a228","a229","a230","a231","a232","a233","a234","a235","a236","a237","a238","a239","a240","a241","a242","a243","a244","a245","a246","a247","a248","a249","a250","a251","a252","a253","a254","a255"

#define TOUCH 1
#define MAPPING 3
#define DF1 16
#define DF2 16
#define HB 5
#define W2POS 1
#define PRE 3
#define MID 0
#define NA 3
#define NA0 3
#define EARLYPOLL 1
#define POST2 0
#define ROT1 6
#define ROT2 1
#define ROTN1 2
#define ROTN2 2
#define IDLE_SLEEP 0
#define PSLEEP 2
#define QUIET 1
#define WSLEEP1 16
#define WSLEEP2 16
#define R1 4
#define R2 4

constexpr int S_ = 2048, D_ = 448, F_ = 1792;
constexpr int KW1 = 16, KW2 = 14;
constexpr int NS1 = 9, NS2 = 7;
#define TPFMAX 6
constexpr int TPF = DF1 / 4 < TPFMAX ? DF1 / 4 : TPFMAX;
constexpr unsigned KSTRIDE = 1024u;
constexpr unsigned HT = 64u * KSTRIDE, UT = 56u * KSTRIDE;
constexpr unsigned H_OFF = 0, U_OFF = 28u * HT;
constexpr unsigned EP_OFF = 4u << 20, HF_OFF = EP_OFF + 4096u, UF_OFF = HF_OFF + 16u * 8192u;
constexpr unsigned UF_REP = 28u * 256u;
constexpr unsigned WS_USED = UF_OFF + 16u * UF_REP;
constexpr unsigned REG_W = 40960, LN_OFF = 36864, LNP = 960, W2_OFF = 12288, LDS_BYTES = 4 * REG_W;
constexpr unsigned OOB = 0x7ffffff0u;
constexpr int SPIN_MAX = 1 << 15;
constexpr unsigned EPOCH_WG = 225;

DI unsigned rfl(unsigned x) { return __builtin_amdgcn_readfirstlane(x); }
DI float bflo(unsigned w) { return __builtin_bit_cast(float, w << 16); }
DI float bfhi(unsigned w) { return __builtin_bit_cast(float, w & 0xffff0000u); }
DI unsigned pack2(float lo, float hi) {
  typedef __bf16 bf2 __attribute__((ext_vector_type(2)));
  typedef float f2 __attribute__((ext_vector_type(2)));
  f2 v = {lo, hi};
  return __builtin_bit_cast(unsigned, __builtin_convertvector(v, bf2));
}
DI srd_t mk_srd(const void* p, unsigned bytes) { return __builtin_amdgcn_make_buffer_rsrc((void*)p, 0, bytes, 0x00020000); }
template <int C> DI float dpp_add(float v) {
  return v + __builtin_bit_cast(float, __builtin_amdgcn_update_dpp(0, __builtin_bit_cast(int, v), C, 0xf, 0xf, true));
}
DI void wave_sum2(float& a, float& b) {
  a = dpp_add<0xB1>(a); b = dpp_add<0xB1>(b); a = dpp_add<0x4E>(a); b = dpp_add<0x4E>(b);
  a = dpp_add<0x141>(a); b = dpp_add<0x141>(b); a = dpp_add<0x140>(a); b = dpp_add<0x140>(b);
  auto p = __builtin_amdgcn_permlane16_swap(__builtin_bit_cast(unsigned, a), __builtin_bit_cast(unsigned, b), false, false);
  const unsigned p0 = p[0], p1 = p[1];
  float s = __builtin_bit_cast(float, p0) + __builtin_bit_cast(float, p1);
  auto q = __builtin_amdgcn_permlane32_swap(__builtin_bit_cast(unsigned, s), __builtin_bit_cast(unsigned, s), false, false);
  const unsigned q0 = q[0], q1 = q[1];
  s = __builtin_bit_cast(float, q0) + __builtin_bit_cast(float, q1);
  a = __builtin_bit_cast(float, __builtin_amdgcn_readlane(__builtin_bit_cast(int, s), 0));
  b = __builtin_bit_cast(float, __builtin_amdgcn_readlane(__builtin_bit_cast(int, s), 16));
}
template <int N> DI void waitv() { static_assert(N >= 0 && N < 64, "vmcnt"); asm volatile("s_waitcnt vmcnt(%0)" ::"i"(N) : "memory"); }
DI void wait_lds() { asm volatile("s_waitcnt lgkmcnt(0)" ::: "memory"); }
DI void wg_barrier() { asm volatile("s_waitcnt lgkmcnt(0)\n\ts_barrier" ::: "memory"); }

struct WStream {
  srd_t srd;
  unsigned cur, beg, end;
  unsigned vo;
  unsigned ring;
  unsigned ta[4];
};
DI void cyc_add(unsigned& cur, unsigned inc, unsigned beg, unsigned end) {
  asm volatile("s_add_u32 %0, %0, %1\n\ts_cmp_eq_u32 %0, %3\n\ts_cselect_b32 %0, %2, %0" : "+s"(cur) : "s"(inc), "s"(beg), "s"(end) : "scc");
}
template <bool W1> struct WP { static constexpr int LDW = W1 ? F_ : S_, KW = W1 ? KW1 : KW2, NS = W1 ? NS1 : NS2, OFF = W1 ? 0 : (int)W2_OFF; };
template <bool W1, int KSTEP, int I> DI void glds_piece(WStream& s) {
  using P = WP<W1>;
  unsigned t;
  asm volatile("s_add_u32 m0, %1, %2\n\ts_add_u32 %0, %3, %4\n\tbuffer_load_dwordx4 %5, %6, %0 offen lds"
               : "=&s"(t)
               : "s"(s.ring), "i"(P::OFF + (KSTEP % P::NS) * 4096 + I * 1024), "s"(s.cur), "i"(I * 8 * P::LDW * 2), "v"(s.vo), "s"(s.srd)
               : "scc", "memory");
  if constexpr (I == 3) cyc_add(s.cur, 32 * P::LDW * 2, s.beg, s.end);
}
template <bool W1, int KSTEP> DI void glds_slot(WStream& s) {
  glds_piece<W1, KSTEP, 0>(s); glds_piece<W1, KSTEP, 1>(s); glds_piece<W1, KSTEP, 2>(s); glds_piece<W1, KSTEP, 3>(s);
}
template <bool W1, int KSTEP, int T, int H> DI void tr_piece(const WStream& s) {
  using P = WP<W1>;
  asm volatile("ds_read_b64_tr_b16 a[%c1:%c2], %0 offset:%c3" ::"v"(s.ta[T]), "i"(16 * KSTEP + 4 * T + 2 * H), "i"(16 * KSTEP + 4 * T + 2 * H + 1),
               "i"(P::OFF + (KSTEP % P::NS) * 4096 + H * 2048) : "memory", ACLOB);
}
template <bool W1, int KSTEP> DI void tr_slot(const WStream& s) {
  tr_piece<W1, KSTEP, 0, 0>(s); tr_piece<W1, KSTEP, 0, 1>(s); tr_piece<W1, KSTEP, 1, 0>(s); tr_piece<W1, KSTEP, 1, 1>(s);
  tr_piece<W1, KSTEP, 2, 0>(s); tr_piece<W1, KSTEP, 2, 1>(s); tr_piece<W1, KSTEP, 3, 0>(s); tr_piece<W1, KSTEP, 3, 1>(s);
}
constexpr int NB1 = PRE + MID;
static_assert(NB1 + NA <= NS1 && NB1 + NA0 <= NS1 && POST2 <= NS2, "schedule");
template <bool W1, int NAX> constexpr int younger(int j, bool flagged) {
  constexpr int KW = W1 ? KW1 : KW2, NS = W1 ? NS1 : NS2;
  const int before_stores = W1 ? NB1 : (W2POS ? TPF : NS2), before_flag = W1 ? NB1 + NAX : NS2 + POST2;
  const int last = j + NS - 1 < KW - 1 ? j + NS - 1 : KW - 1;
  return 4 * (last - j) + (j < before_stores ? 2 : 0) + (flagged && j < before_flag ? 1 : 0);
}
template <bool W1, int J, int MODE, int EXTRA, int NAX> DI void wpipe_step(WStream& s) {
  using P = WP<W1>;
  constexpr int rem = P::KW - 1 - J;
  waitv<(MODE == 0 ? 4 * (rem < P::NS - 1 ? rem : P::NS - 1) : younger<W1, NAX>(J, MODE == 2)) + EXTRA>();
  tr_slot<W1, J>(s);
  if constexpr (J + P::NS < P::KW) { wait_lds(); glds_slot<W1, J + P::NS>(s); }
  GAP();
}
template <bool W1, int J0, int MODE, int NAX, int... Js> DI void wpipe_stepsx(WStream& s, iseq<Js...>) { (wpipe_step<W1, J0 + Js, MODE, 0, NAX>(s), ...); }
template <bool W1, int J0, int MODE, int... Js> DI void wpipe_steps(WStream& s, iseq<Js...>) { (wpipe_step<W1, J0 + Js, MODE, 0, NA>(s), ...); }
template <bool W1, int J0, int EXTRA, int NAX, int... Js> DI void wtail_steps(WStream& s, iseq<Js...>) { (wpipe_step<W1, J0 + Js, 2, EXTRA, NAX>(s), ...); }
template <bool W1, int J0, int... Js> DI void glds_slots(WStream& s, iseq<Js...>) { (glds_slot<W1, J0 + Js>(s), ...); }

constexpr int RB = 160;
static_assert(RB + 4 * DF1 <= 256 && RB + 4 * DF2 <= 256, "ring");
template <int REG> DI void act_load(unsigned voff, srd_t srd, unsigned soff) {
  asm volatile("buffer_load_dwordx4 v[%c0:%c1], %2, %3, %4 offen sc1" ::"i"(REG), "i"(REG + 3), "v"(voff), "s"(srd), "s"(soff) : "memory");
}
struct AStream { srd_t srd; unsigned voff[4], cur, beg, end; };
template <bool WA, bool ZERO, int AR, int VR> DI void mfma(f32x4& acc) {
  if constexpr (WA) {
    if constexpr (ZERO) asm volatile("v_mfma_f32_16x16x32_bf16 %0, a[%c1:%c2], v[%c3:%c4], 0" : "=&v"(acc) : "i"(AR), "i"(AR + 3), "i"(VR), "i"(VR + 3) : ACLOB);
    else asm volatile("v_mfma_f32_16x16x32_bf16 %0, a[%c1:%c2], v[%c3:%c4], %0" : "+v"(acc) : "i"(AR), "i"(AR + 3), "i"(VR), "i"(VR + 3) : ACLOB);
  } else {
    if constexpr (ZERO) asm volatile("v_mfma_f32_16x16x32_bf16 %0, v[%c3:%c4], a[%c1:%c2], 0" : "=&v"(acc) : "i"(AR), "i"(AR + 3), "i"(VR), "i"(VR + 3) : ACLOB);
    else asm volatile("v_mfma_f32_16x16x32_bf16 %0, v[%c3:%c4], a[%c1:%c2], %0" : "+v"(acc) : "i"(AR), "i"(AR + 3), "i"(VR), "i"(VR + 3) : ACLOB);
  }
}
template <bool G1> constexpr int vop(int v) { return v < 4 * (G1 ? KW1 : KW2) ? 1 : (G1 && v - 4 * KW1 < 4 * TPF ? 1 : 0); }
template <bool G1> constexpr int vop_range(int a, int b) { int n = 0; for (int v = a; v <= b; ++v) n += vop<G1>(v); return n; }
template <bool G1, int V> DI void stream_op(AStream& a, WStream& w2) {
  constexpr int KW = G1 ? KW1 : KW2, DF = G1 ? DF1 : DF2;
  if constexpr (V < 4 * KW) { act_load<RB + 4 * (V % DF)>(a.voff[(V / KW + 1) & 3], a.srd, a.cur); cyc_add(a.cur, KSTRIDE, a.beg, a.end); }
  else if constexpr (vop<G1>(V) != 0) glds_piece<false, (V - 4 * KW) / 4, (V - 4 * KW) % 4>(w2);
}
struct Exch { LAS unsigned char* mine; const LAS unsigned char* from[3]; };
template <bool G1, int I> DI void gemm_frag(f32x4 (&acc)[4][4], f32x4 (&pre)[4], f32x4 (&r)[3][4], AStream& a, WStream& w2, const Exch& x) {
  constexpr int KW = G1 ? KW1 : KW2, DF = G1 ? DF1 : DF2, K = I % KW, M = (I / KW + 1) & 3, VR = RB + 4 * (I % DF);
  waitv<I == 0 ? vop_range<G1>(1, DF - 1) : vop_range<G1>(I + 1, I - 2 + DF)>();
  mfma<G1, K == 0, 16 * K + 0, VR>(acc[M][0]);
  if constexpr (I >= 1) stream_op<G1, I - 1 + DF>(a, w2);
  mfma<G1, K == 0, 16 * K + 4, VR>(acc[M][1]);
  mfma<G1, K == 0, 16 * K + 8, VR>(acc[M][2]);
  mfma<G1, K == 0, 16 * K + 12, VR>(acc[M][3]);
  if constexpr (I == 4 * KW - 1) stream_op<G1, I + DF>(a, w2);
  if constexpr (I % KW == 1 && I > KW) {
    constexpr int m = I / KW;
    asm volatile("" : "+v"(acc[m][0]), "+v"(acc[m][1]), "+v"(acc[m][2]), "+v"(acc[m][3]));
#pragma unroll
    for (int t = 0; t < 4; ++t) *(LAS f32x4*)(x.mine + (m - 1) * 4096 + t * 1024) = acc[m][t];
  }
  if constexpr (I == 3 * KW + HB) {
    wg_barrier();
#pragma unroll
    for (int j = 0; j < 3; ++j)
#pragma unroll
      for (int t = 0; t < 4; ++t) r[j][t] = *(const LAS f32x4*)(x.from[j] + t * 1024);
  }
  if constexpr (I == 3 * KW + HB + 2) {
#pragma unroll
    for (int t = 0; t < 4; ++t) pre[t] += r[0][t];
  }
  if constexpr (I == 3 * KW + HB + 3) {
#pragma unroll
    for (int t = 0; t < 4; ++t) pre[t] += r[1][t];
  }
  if constexpr (I == 3 * KW + HB + 4) {
#pragma unroll
    for (int t = 0; t < 4; ++t) pre[t] += r[2][t];
  }
  GAP();
}
template <bool G1, int... Is> DI void gemm_frags(f32x4 (&acc)[4][4], f32x4 (&pre)[4], f32x4 (&r)[3][4], AStream& a, WStream& w2, const Exch& x, iseq<Is...>) {
  (gemm_frag<G1, Is>(acc, pre, r, a, w2, x), ...);
}
template <bool G1, int... Vs> DI void gemm_prologue(AStream& a, WStream& w2, iseq<Vs...>) { (stream_op<G1, Vs>(a, w2), ...); GAP(); }
template <bool G1> DI void gemm_body(f32x4 (&pre)[4], AStream& a, WStream& w2, const Exch& x) {
  f32x4 acc[4][4], r[3][4];
  gemm_frags<G1>(acc, pre, r, a, w2, x, make_iseq<4 * (G1 ? KW1 : KW2)>{});
  asm volatile("s_nop 7" : "+v"(acc[0][0]), "+v"(acc[0][1]), "+v"(acc[0][2]), "+v"(acc[0][3]));
#pragma unroll
  for (int t = 0; t < 4; ++t) pre[t] += acc[0][t];
}
template <int SLEEP> DI void poll_flags(unsigned voff, srd_t srd, unsigned soff, unsigned tlo, unsigned thi) {
#pragma unroll 1
  for (int spin = 0; spin < SPIN_MAX; ++spin) {
    v2u f;
    asm volatile("buffer_load_dwordx2 %0, %1, %2, %3 offen sc1\n\ts_waitcnt vmcnt(0)" : "=v"(f) : "v"(voff), "s"(srd), "s"(soff) : "memory");
    if (__builtin_amdgcn_ballot_w64(f[0] != tlo || f[1] != thi) == 0) break;
    __builtin_amdgcn_s_sleep(SLEEP);
  }
}
template <int N> DI void poll2_issue(v4u (&f)[2], unsigned voff, srd_t srd, unsigned soff) {
  asm volatile("buffer_load_dwordx4 %0, %1, %2, %3 offen sc1" : "=v"(f[0]) : "v"(voff), "s"(srd), "s"(soff) : "memory");
  if constexpr (N == 2) asm volatile("buffer_load_dwordx4 %0, %1, %2, %3 offen offset:1024 sc1" : "=v"(f[1]) : "v"(voff), "s"(srd), "s"(soff) : "memory");
}
template <int N> DI bool poll2_check(v4u (&f)[2], bool use, unsigned tlo, unsigned thi) {
  if constexpr (N == 2) asm volatile("s_waitcnt vmcnt(0)" : "+v"(f[0]), "+v"(f[1])::"memory");
  else asm volatile("s_waitcnt vmcnt(0)" : "+v"(f[0])::"memory");
  const v4u a = f[0];
  bool bad = a[0] != tlo || a[1] != thi || a[2] != tlo || a[3] != thi;
  if constexpr (N == 2) { const v4u c = f[1]; bad = bad || c[0] != tlo || c[1] != thi || c[2] != tlo || c[3] != thi; }
  return __builtin_amdgcn_ballot_w64(bad && use) == 0;
}
template <int SLEEP, int N> DI void poll_flags2(unsigned voff, bool use, srd_t srd, unsigned soff, unsigned tlo, unsigned thi) {
#pragma unroll 1
  for (int spin = 0; spin < SPIN_MAX; ++spin) {
    v4u f[2];
    poll2_issue<N>(f, voff, srd, soff);
    if (poll2_check<N>(f, use, tlo, thi)) break;
    __builtin_amdgcn_s_sleep(SLEEP);
  }
}
DI void store16_sc1(v4u d, unsigned voff, srd_t srd, unsigned soff) {
  asm volatile("buffer_store_dwordx4 %0, %1, %2, %3 offen sc1\n\ts_nop 1" ::"v"(d), "v"(voff), "s"(srd), "s"(soff) : "memory");
}
DI void store8_sc1(v2u d, unsigned voff, srd_t srd, unsigned soff) {
  asm volatile("buffer_store_dwordx2 %0, %1, %2, %3 offen sc1\n\ts_nop 1" ::"v"(d), "v"(voff), "s"(srd), "s"(soff) : "memory");
}
DI float gelu(float u) {
  const float e = __builtin_amdgcn_exp2f(u * (-2.3022082f - 0.1029432f * u * u));
  return u * __builtin_amdgcn_rcpf(1.0f + e);
}
#define PA(i)
#define PB(i)
#define STAMP(i)
#define STAMPA(i)
#define STAMPB(i)
#define STAMPX0(i)
#define STAMPX1(i)

__global__ void __launch_bounds__(256, 1) __attribute__((amdgpu_num_vgpr(RB)))
mixer_kernel(const u16* __restrict__ x, const u16* __restrict__ w1, const u16* __restrict__ b1, const u16* __restrict__ w2, const u16* __restrict__ b2,
             u16* __restrict__ out, unsigned char* __restrict__ ws) {
  __shared__ __attribute__((aligned(1024))) unsigned char smem[LDS_BYTES];
  STAMP(0);
  asm volatile("" ::: ACLOB);
  asm volatile("" ::: "v160", "v255");
  const unsigned tid = threadIdx.x, lane = tid & 63, w = rfl(tid >> 6), b = blockIdx.x;
  const unsigned lds0 = (unsigned)(uintptr_t)(LAS unsigned char*)smem;
  const srd_t srdWS = mk_srd(ws, WS_USED);
  const unsigned g = lane >> 4, c = lane & 15;

  v2u ep; v4u xv[2];
  {
    const srd_t srdX = mk_srd(x, S_ * D_ * 2);
    const unsigned zero = 0, vx = lane < 56 ? lane * 16 : OOB, row = 32 * (b >> 2) + 4 * (b & 3) + 2 * (w & 1) + 16 * (w >> 1);
    asm volatile("buffer_load_dwordx4 %0, %1, %2, %3 offen" : "=v"(xv[0]) : "v"(vx), "s"(srdX), "s"(row * 896u) : "memory");
    asm volatile("buffer_load_dwordx4 %0, %1, %2, %3 offen offset:896" : "=v"(xv[1]) : "v"(vx), "s"(srdX), "s"(row * 896u) : "memory");
    asm volatile("buffer_load_dwordx2 %0, %1, %2, %3 offen sc1" : "=v"(ep) : "v"(zero), "s"(srdWS), "s"(EP_OFF) : "memory");
  }
  GAP();
  const unsigned xcd = b & 7, li = b >> 3;
  bool g1, g2;
  unsigned mb1, nb1, mb2, nb2;
  {
    const unsigned j = xcd >> 1, odd = xcd & 1, i0 = 7 + odd;
    g1 = li >= i0;
    const unsigned idx = g1 ? li - i0 + (odd ? 25 : 0) : 0;
    nb1 = 7 * j + idx / 7; mb1 = idx % 7;
    g2 = li < 28; nb2 = 4 * xcd + (li & 3); mb2 = g2 ? li >> 2 : 0;
  }
  const unsigned rot1 = ROT1 == 6 ? (nb1 % 7 != 3 ? (mb1 * 16) / 7 : mb1 < 4 ? 4 * mb1 : ((mb1 - 4) * 16) / 3) : ROT1 == 7 ? (nb1 % 7 != 3 ? (mb1 * 16) / 7 : mb1 < 4 ? 4 * mb1 + 1 : ((mb1 - 4) * 16) / 3 + 3) : ROT1 == 0 ? 0 : ROT1 == 1 ? (mb1 * 16) / 7 : ROT1 == 2 ? ((mb1 * 16) / 7 + 4 * (nb1 & 3)) & 15 : ROT1 == 3 ? (4 * (nb1 & 3)) & 15 : (((mb1 * ROTN1) / 7) * 16) / ROTN1;
  const unsigned rot2 = ROT2 == 0 ? 0 : ROT2 == 1 ? (mb2 * 8) % 14 : ROT2 == 2 ? (mb2 * 2 + ((nb2 & 3) * 7) / 2) % 14 : ROT2 == 3 ? ((nb2 & 3) * 7) / 2 : (((mb2 * ROTN2) / 7) * 14) / ROTN2;
  WStream W1, W2;
  const unsigned region = lds0 + w * REG_W;
  {
    const unsigned r8 = lane >> 3, pc = (lane & 7) ^ (r8 & 6);
    const unsigned q = (lane >> 2) & 3, p = lane & 3, row = 4 * g + q;
    const unsigned t0 = region + row * 128 + ((row >> 1) & 3) * 32 + p * 8;
    W1.srd = mk_srd(w1, S_ * F_ * 2);
    W2.srd = mk_srd(w2, g2 ? F_ * S_ * 2 : 0);
    W1.beg = w * (KW1 * 32) * F_ * 2 + nb1 * 128; W1.end = W1.beg + KW1 * 32 * F_ * 2; W1.cur = W1.beg + rot1 * (32 * F_ * 2);
    W2.beg = w * (KW2 * 32) * S_ * 2 + nb2 * 128; W2.end = W2.beg + KW2 * 32 * S_ * 2; W2.cur = W2.beg + rot2 * (32 * S_ * 2);
    W1.vo = r8 * F_ * 2 + pc * 16;
    W2.vo = r8 * S_ * 2 + pc * 16;
    W1.ring = W2.ring = region;
#pragma unroll
    for (int t = 0; t < 4; ++t) { W1.ta[t] = W2.ta[t] = t0 ^ (t << 5); }
  }
  {
    const srd_t srdO = mk_srd(out, S_ * D_ * 2);
    const unsigned v0 = lane == 0 ? 0 : OOB;
    asm volatile("buffer_load_dword v252, %0, %1, %2 offen" ::"v"(v0), "s"(srdWS), "s"(H_OFF + (b >> 2) * KSTRIDE + w * 4 * HT) : "memory");
    asm volatile("buffer_load_dword v253, %0, %1, %2 offen" ::"v"(v0), "s"(srdWS), "s"(U_OFF + (4 * mb1 + w) * UT + 2 * nb1 * KSTRIDE) : "memory");
    asm volatile("buffer_load_dword v254, %0, %1, %2 offen" ::"v"(v0), "s"(srdO), "s"(nb2 * 64 * (D_ * 2) + mb2 * 128 + 16 * w * (D_ * 2)) : "memory");
    asm volatile("buffer_load_dword v255, %0, %1, %2 offen" ::"v"(v0), "s"(W2.srd), "s"(W2.cur) : "memory");
  }
  if (g1) glds_slots<true, 0>(W1, make_iseq<PRE>{}); else waitv<0>();
  asm volatile("s_waitcnt vmcnt(%3)" : "+v"(ep), "+v"(xv[0]), "+v"(xv[1]) : "i"(4 * PRE + (TOUCH ? 4 : 0)) : "memory");
  GAP();
  STAMP(1);
  if (g1) glds_slots<true, PRE>(W1, make_iseq<MID>{});
  GAP();
  unsigned tlo, thi;
  {
    const unsigned long long e = (((unsigned long long)ep[1] << 32) | ep[0]) + 1ull;
    tlo = rfl((unsigned)e); thi = rfl((unsigned)(e >> 32));
  }
  {
    float f[2][8], m[2], qs[2];
#pragma unroll
    for (int r = 0; r < 2; ++r) {
#pragma unroll
      for (int i = 0; i < 4; ++i) { f[r][2 * i] = bflo(xv[r][i]); f[r][2 * i + 1] = bfhi(xv[r][i]); }
      m[r] = ((f[r][0] + f[r][1]) + (f[r][2] + f[r][3])) + ((f[r][4] + f[r][5]) + (f[r][6] + f[r][7]));
    }
    wave_sum2(m[0], m[1]);
#pragma unroll
    for (int r = 0; r < 2; ++r) {
      const float mu = m[r] * (1.0f / D_);
#pragma unroll
      for (int i = 0; i < 8; ++i) f[r][i] -= mu;
      qs[r] = ((f[r][0] * f[r][0] + f[r][1] * f[r][1]) + (f[r][2] * f[r][2] + f[r][3] * f[r][3])) + ((f[r][4] * f[r][4] + f[r][5] * f[r][5]) + (f[r][6] * f[r][6] + f[r][7] * f[r][7]));
      qs[r] = lane < 56 ? qs[r] : 0.f;
    }
    wave_sum2(qs[0], qs[1]);
    v4u o[2];
#pragma unroll
    for (int r = 0; r < 2; ++r) {
      const float rs = __builtin_amdgcn_rsqf(qs[r] * (1.0f / D_) + 1e-5f);
      o[r] = v4u{pack2(f[r][0] * rs, f[r][1] * rs), pack2(f[r][2] * rs, f[r][3] * rs), pack2(f[r][4] * rs, f[r][5] * rs), pack2(f[r][6] * rs, f[r][7] * rs)};
    }
    if (lane < 56) {
      const unsigned ro = w * REG_W + LN_OFF + (w & 1) * 128 + lane * 16;
      *(LAS v4u*)(smem + ro) = o[0];
      *(LAS v4u*)(smem + ro + LNP) = o[1];
    }
  }
  wg_barrier();
  {
    v2u lo[2], hi[2];
    const unsigned q = (lane >> 2) & 3;
    const unsigned tlo_a = lds0 + (q >> 1) * (REG_W + 128) + LN_OFF + (q & 1) * LNP + g * 32 + (lane & 3) * 8 + w * 128, thi_a = tlo_a + 2 * REG_W;
    asm volatile("ds_read_b64_tr_b16 %0, %1 offset:0" : "=v"(lo[0]) : "v"(tlo_a) : "memory");
    asm volatile("ds_read_b64_tr_b16 %0, %1 offset:0" : "=v"(hi[0]) : "v"(thi_a) : "memory");
    asm volatile("ds_read_b64_tr_b16 %0, %1 offset:512" : "=v"(lo[1]) : "v"(tlo_a) : "memory");
    asm volatile("ds_read_b64_tr_b16 %0, %1 offset:512" : "=v"(hi[1]) : "v"(thi_a) : "memory");
    asm volatile("s_waitcnt lgkmcnt(0)" : "+v"(lo[0]), "+v"(lo[1]), "+v"(hi[0]), "+v"(hi[1])::"memory");
    const unsigned vo = g * HT + c * 16 + (b & 3) * 256;
    const unsigned so = H_OFF + (b >> 2) * KSTRIDE + w * 4 * HT;
    store16_sc1(v4u{lo[0][0], lo[0][1], hi[0][0], hi[0][1]}, vo, srdWS, so);
    store16_sc1(v4u{lo[1][0], lo[1][1], hi[1][0], hi[1][1]}, w < 3 ? vo : OOB, srdWS, so + 16 * HT);
    const unsigned vf = lane < R1 ? lane * 8192 + w * 2048 + b * 8 : OOB;
    if (!g1) {
      waitv<0>();
      store8_sc1(v2u{tlo, thi}, vf, srdWS, HF_OFF);
      STAMP(2);
    } else if (w == 0) {
      glds_slots<true, NB1>(W1, make_iseq<NA0>{});
      waitv<4 * NA0>();
      store8_sc1(v2u{tlo, thi}, vf, srdWS, HF_OFF);
      STAMP(2);
      glds_slots<true, NB1 + NA0>(W1, make_iseq<NS1 - NB1 - NA0>{});
      wpipe_stepsx<true, 0, 2, NA0>(W1, make_iseq<7>{});
    } else {
      glds_slots<true, NB1>(W1, make_iseq<NA>{});
      waitv<4 * NA>();
      store8_sc1(v2u{tlo, thi}, vf, srdWS, HF_OFF);
      STAMP(2);
      if (WSLEEP1) __builtin_amdgcn_s_sleep(WSLEEP1);
      glds_slots<true, NB1 + NA>(W1, make_iseq<NS1 - NB1 - NA>{});
      wpipe_stepsx<true, 0, 2, NA>(W1, make_iseq<7>{});
    }
  }
  Exch X;
  X.mine = (LAS unsigned char*)smem + w * REG_W + lane * 16;
#pragma unroll
  for (int j = 0; j < 3; ++j) X.from[j] = (const LAS unsigned char*)smem + ((w - 1 - j) & 3) * REG_W + j * 4096 + lane * 16;
  if (g1) {
    v2u bv[4];
    {
      const srd_t srdB1 = mk_srd(b1, F_ * 2);
      asm volatile("buffer_load_dwordx2 %0, %1, %2, %3 offen" : "=v"(bv[0]) : "v"(g * 8), "s"(srdB1), "s"(nb1 * 128u) : "memory");
      asm volatile("buffer_load_dwordx2 %0, %1, %2, %3 offen offset:32" : "=v"(bv[1]) : "v"(g * 8), "s"(srdB1), "s"(nb1 * 128u) : "memory");
      asm volatile("buffer_load_dwordx2 %0, %1, %2, %3 offen offset:64" : "=v"(bv[2]) : "v"(g * 8), "s"(srdB1), "s"(nb1 * 128u) : "memory");
      asm volatile("buffer_load_dwordx2 %0, %1, %2, %3 offen offset:96" : "=v"(bv[3]) : "v"(g * 8), "s"(srdB1), "s"(nb1 * 128u) : "memory");
    }
    GAP();
    if (w == 0) {
      const unsigned so = HF_OFF + (b % R1) * 8192 + (mb1 & 3) * 2048;
      v4u f[2];
      poll2_issue<2>(f, lane * 16, srdWS, so);
      wtail_steps<true, 7, 6, NA0>(W1, make_iseq<KW1 - 7>{});
      STAMPX0(3);
      if (!poll2_check<2>(f, true, tlo, thi)) poll_flags2<PSLEEP, 2>(lane * 16, true, srdWS, so, tlo, thi);
    }
    asm volatile("s_barrier" ::: "memory");
    STAMPA(4);
    AStream A;
    A.srd = srdWS; A.beg = H_OFF + 4 * mb1 * HT + w * KW1 * KSTRIDE; A.end = A.beg + KW1 * KSTRIDE; A.cur = A.beg + rot1 * KSTRIDE;
#pragma unroll
    for (int m = 0; m < 4; ++m) A.voff[m] = lane * 16 + ((m + w) & 3) * HT;
    gemm_prologue<true>(A, W2, make_iseq<DF1>{});
    if (w != 0) { wtail_steps<true, 7, 4 + DF1, NA>(W1, make_iseq<KW1 - 7>{}); STAMPX1(3); }
    GAP();
    asm volatile("s_waitcnt vmcnt(%4) lgkmcnt(0)" : "+v"(bv[0]), "+v"(bv[1]), "+v"(bv[2]), "+v"(bv[3]) : "i"(DF1) : "memory");
    f32x4 s[4];
#pragma unroll
    for (int t = 0; t < 4; ++t) { const v2u q = bv[t]; const unsigned q0 = q[0], q1 = q[1]; s[t] = f32x4{bflo(q0), bfhi(q0), bflo(q1), bfhi(q1)}; }
    GAP();
    gemm_body<true>(s, A, W2, X);
    STAMPA(5);
    if (!W2POS) glds_slots<false, TPF>(W2, make_iseq<NS2 - TPF>{});
    GAP();
#pragma unroll
    for (int kk = 0; kk < 2; ++kk) {
      const f32x4 s0 = s[2 * kk], s1 = s[2 * kk + 1];
      const v4u o = {pack2(gelu(s0[0]), gelu(s0[1])), pack2(gelu(s0[2]), gelu(s0[3])), pack2(gelu(s1[0]), gelu(s1[1])), pack2(gelu(s1[2]), gelu(s1[3]))};
      store16_sc1(o, lane * 16, srdWS, U_OFF + (4 * mb1 + w) * UT + (2 * nb1 + kk) * KSTRIDE);
    }
    GAP();
    if (W2POS) glds_slots<false, TPF>(W2, make_iseq<NS2 - TPF>{});
    wpipe_steps<false, 0, 1>(W2, make_iseq<POST2>{});
    waitv<4 * POST2 + (W2POS ? 4 * (NS2 - TPF) : 0)>();
    store8_sc1(v2u{tlo, thi}, lane < R2 ? lane * UF_REP + (mb1 * 4 + nb1 / 7) * 256 + ((nb1 % 7) * 4 + w) * 8 : OOB, srdWS, UF_OFF);
    STAMP(6);
    if (!g2) {
      if (b == EPOCH_WG && w == 1) store8_sc1(v2u{tlo, thi}, lane == 0 ? 0 : OOB, srdWS, EP_OFF);
      waitv<0>();
      goto done;
    }
    if (w != 0) __builtin_amdgcn_s_sleep(WSLEEP2);
    wpipe_steps<false, POST2, 2>(W2, make_iseq<NS2 - POST2>{});
  } else {
    wg_barrier();
    if (!g2) goto done;
#pragma unroll 1
    for (int i = 0; i < IDLE_SLEEP; ++i) __builtin_amdgcn_s_sleep(16);
    glds_slots<false, 0>(W2, make_iseq<NS2>{});
    wpipe_steps<false, 0, 0>(W2, make_iseq<KW2>{});
    if (w == 0) poll_flags<8>(0, srdWS, EP_OFF, tlo, thi);
    wg_barrier();
  }
  {
    unsigned bv[4];
    {
      const srd_t srdB2 = mk_srd(b2, S_ * 2);
      asm volatile("buffer_load_ushort %0, %1, %2, %3 offen" : "=v"(bv[0]) : "v"(c * 2), "s"(srdB2), "s"(nb2 * 128u) : "memory");
      asm volatile("buffer_load_ushort %0, %1, %2, %3 offen offset:32" : "=v"(bv[1]) : "v"(c * 2), "s"(srdB2), "s"(nb2 * 128u) : "memory");
      asm volatile("buffer_load_ushort %0, %1, %2, %3 offen offset:64" : "=v"(bv[2]) : "v"(c * 2), "s"(srdB2), "s"(nb2 * 128u) : "memory");
      asm volatile("buffer_load_ushort %0, %1, %2, %3 offen offset:96" : "=v"(bv[3]) : "v"(c * 2), "s"(srdB2), "s"(nb2 * 128u) : "memory");
    }
    GAP();
    if (w == 0) {
      const unsigned so = UF_OFF + (nb2 % R2) * UF_REP + mb2 * 1024;
      v4u f[2];
      poll2_issue<1>(f, lane * 16, srdWS, so);
      if (g1) wtail_steps<false, NS2, 5, NA>(W2, make_iseq<KW2 - NS2>{});
      if (!poll2_check<1>(f, c < 14, tlo, thi)) poll_flags2<PSLEEP, 1>(lane * 16, c < 14, srdWS, so, tlo, thi);
    }
    asm volatile("s_barrier" ::: "memory");
    STAMPB(4);
    AStream A;
    A.srd = srdWS; A.beg = U_OFF + 4 * mb2 * UT + w * KW2 * KSTRIDE; A.end = A.beg + KW2 * KSTRIDE; A.cur = A.beg + rot2 * KSTRIDE;
#pragma unroll
    for (int m = 0; m < 4; ++m) A.voff[m] = lane * 16 + ((m + w) & 3) * UT;
    gemm_prologue<false>(A, W2, make_iseq<DF2>{});
    if (w != 0 && g1) wtail_steps<false, NS2, 4 + DF2, NA>(W2, make_iseq<KW2 - NS2>{});
    STAMPB(3);
    GAP();
    asm volatile("s_waitcnt vmcnt(%4) lgkmcnt(0)" : "+v"(bv[0]), "+v"(bv[1]), "+v"(bv[2]), "+v"(bv[3]) : "i"(DF2) : "memory");
    f32x4 s[4];
#pragma unroll
    for (int t = 0; t < 4; ++t) { const float bb = bflo(bv[t]); s[t] = f32x4{bb, bb, bb, bb}; }
    GAP();
    gemm_body<false>(s, A, W2, X);
    STAMPB(5);
    unsigned char* stage = smem + W2_OFF;
#pragma unroll
    for (int t = 0; t < 4; ++t) *(LAS v2u*)((LAS unsigned char*)stage + (16 * t + c) * 144 + 32 * w + 8 * g) = v2u{pack2(s[t][0], s[t][1]), pack2(s[t][2], s[t][3])};
    wg_barrier();
    const srd_t srdO = mk_srd(out, S_ * D_ * 2);
    const unsigned row = 16 * w + (lane >> 3), col = (lane & 7) * 16;
#pragma unroll
    for (int mp = 0; mp < 2; ++mp) {
      const v4u o = *(const LAS v4u*)((LAS unsigned char*)stage + (row + 8 * mp) * 144 + col);
      asm volatile("buffer_store_dwordx4 %0, %1, %2, %3 offen\n\ts_nop 1" ::"v"(o), "v"((row + 8 * mp) * (D_ * 2) + col), "s"(srdO), "s"(nb2 * 64 * (D_ * 2) + mb2 * 128) : "memory");
    }
  }
done:
  STAMP(7);
}

extern "C" void kernel_launch(void* const* d_in, const int* in_sizes, int n_in, void* d_out, int out_size, void* d_ws, size_t ws_size, hipStream_t stream) {
  mixer_kernel<<<256, 256, 0, stream>>>((const u16*)d_in[0], (const u16*)d_in[1], (const u16*)d_in[2], (const u16*)d_in[3], (const u16*)d_in[4],
                                       (u16*)d_out, (unsigned char*)d_ws);
}
```

```cpp
#include <hip/hip_runtime.h>
#include <stdint.h>
#include <utility>

typedef unsigned v4u __attribute__((ext_vector_type(4)));
typedef unsigned v2u __attribute__((ext_vector_type(2)));
typedef float f32x4 __attribute__((ext_vector_type(4)));
typedef unsigned short u16;
typedef __amdgpu_buffer_rsrc_t srd_t;
#define DI __device__ __forceinline__
#define LAS __attribute__((address_space(3)))
#define GAP() __builtin_amdgcn_sched_barrier(0)
template <int... Is> using iseq = std::integer_sequence<int, Is...>;
template <int N> using make_iseq = std::make_integer_sequence<int, N>;
#define ACLOB "a0","a1","a2","a3","a4","a5","a6","a7","a8","a9","a10","a11","a12","a13","a14","a15","a16","a17","a18","a19","a20","a21","a22","a23","a24","a25","a26","a27","a28","a29","a30","a31","a32","a33","a34","a35","a36","a37","a38","a39","a40","a41","a42","a43","a44","a45","a46","a47","a48","a49","a50","a51","a52","a53","a54","a55","a56","a57","a58","a59","a60","a61","a62","a63","a64","a65","a66","a67","a68","a69","a70","a71","a72","a73","a74","a75","a76","a77","a78","a79","a80","a81","a82","a83","a84","a85","a86","a87","a88","a89","a90","a91","a92","a93","a94","a95","a96","a97","a98","a99","a100","a101","a102","a103","a104","a105","a106","a107","a108","a109","a110","a111","a112","a113","a114","a115","a116","a117","a118","a119","a120","a121","a122","a123","a124","a125","a126","a127","a128","a129","a130","a131","a132","a133","a134","a135","a136","a137","a138","a139","a140","a141","a142","a143","a144","a145","a146","a147","a148","a149","a150","a151","a152","a153","a154","a155","a156","a157","a158","a159","a160","a161","a162","a163","a164","a165","a166","a167","a168","a169","a170","a171","a172","a173","a174","a175","a176","a177","a178","a179","a180","a181","a182","a183","a184","a185","a186","a187","a188","a189","a190","a191","a192","a193","a194","a195","a196","a197","a198","a199","a200","a201","a202","a203","a204","a205","a206","a207","a208","a209","a210","a211","a212","a213","a214","a215","a216","a217","a218","a219","a220","a221","a222","a223","a224","a225","a226","a227","a228","a229","a230","a231","a232","a233","a234","a235","a236","a237","a238","a239","a240","a241","a242","a243","a244","a245","a246","a247","a248","a249","a250","a251","a252","a253","a254","a255"

#define TOUCH 3
#define MAPPING 3
#define DF1 16
#define DF2 16
#define HB 5
#define W2POS 1
#define PRE 3
#define MID 0
#define NA 3
#define NA0 3
#define EARLYPOLL 1
#define POST2 0
#define ROT1 6
#define ROT2 1
#define ROTN1 2
#define ROTN2 2
#define IDLE_SLEEP 0
#define PSLEEP 2
#define QUIET 1
#define WSLEEP1 16
#define WSLEEP2 16
#define R1 4
#define R2 4

constexpr int S_ = 2048, D_ = 448, F_ = 1792;
constexpr int KW1 = 16, KW2 = 14;
constexpr int NS1 = 9, NS2 = 7;
#define TPFMAX 6
constexpr int TPF = DF1 / 4 < TPFMAX ? DF1 / 4 : TPFMAX;
constexpr unsigned KSTRIDE = 1024u;
constexpr unsigned HT = 64u * KSTRIDE, UT = 56u * KSTRIDE;
constexpr unsigned H_OFF = 0, U_OFF = 28u * HT;
constexpr unsigned EP_OFF = 4u << 20, HF_OFF = EP_OFF + 4096u, UF_OFF = HF_OFF + 16u * 8192u;
constexpr unsigned UF_REP = 28u * 256u;
constexpr unsigned WS_USED = UF_OFF + 16u * UF_REP;
constexpr unsigned REG_W = 40960, LN_OFF = 36864, LNP = 960, W2_OFF = 12288, LDS_BYTES = 4 * REG_W;
constexpr unsigned OOB = 0x7ffffff0u;
constexpr int SPIN_MAX = 1 << 15;
constexpr unsigned EPOCH_WG = 225;

DI unsigned rfl(unsigned x) { return __builtin_amdgcn_readfirstlane(x); }
DI float bflo(unsigned w) { return __builtin_bit_cast(float, w << 16); }
DI float bfhi(unsigned w) { return __builtin_bit_cast(float, w & 0xffff0000u); }
DI unsigned pack2(float lo, float hi) {
  typedef __bf16 bf2 __attribute__((ext_vector_type(2)));
  typedef float f2 __attribute__((ext_vector_type(2)));
  f2 v = {lo, hi};
  return __builtin_bit_cast(unsigned, __builtin_convertvector(v, bf2));
}
DI srd_t mk_srd(const void* p, unsigned bytes) { return __builtin_amdgcn_make_buffer_rsrc((void*)p, 0, bytes, 0x00020000); }
template <int C> DI float dpp_add(float v) {
  return v + __builtin_bit_cast(float, __builtin_amdgcn_update_dpp(0, __builtin_bit_cast(int, v), C, 0xf, 0xf, true));
}
DI void wave_sum2(float& a, float& b) {
  a = dpp_add<0xB1>(a); b = dpp_add<0xB1>(b); a = dpp_add<0x4E>(a); b = dpp_add<0x4E>(b);
  a = dpp_add<0x141>(a); b = dpp_add<0x141>(b); a = dpp_add<0x140>(a); b = dpp_add<0x140>(b);
  auto p = __builtin_amdgcn_permlane16_swap(__builtin_bit_cast(unsigned, a), __builtin_bit_cast(unsigned, b), false, false);
  const unsigned p0 = p[0], p1 = p[1];
  float s = __builtin_bit_cast(float, p0) + __builtin_bit_cast(float, p1);
  auto q = __builtin_amdgcn_permlane32_swap(__builtin_bit_cast(unsigned, s), __builtin_bit_cast(unsigned, s), false, false);
  const unsigned q0 = q[0], q1 = q[1];
  s = __builtin_bit_cast(float, q0) + __builtin_bit_cast(float, q1);
  a = __builtin_bit_cast(float, __builtin_amdgcn_readlane(__builtin_bit_cast(int, s), 0));
  b = __builtin_bit_cast(float, __builtin_amdgcn_readlane(__builtin_bit_cast(int, s), 16));
}
template <int N> DI void waitv() { static_assert(N >= 0 && N < 64, "vmcnt"); asm volatile("s_waitcnt vmcnt(%0)" ::"i"(N) : "memory"); }
DI void wait_lds() { asm volatile("s_waitcnt lgkmcnt(0)" ::: "memory"); }
DI void wg_barrier() { asm volatile("s_waitcnt lgkmcnt(0)\n\ts_barrier" ::: "memory"); }

struct WStream {
  srd_t srd;
  unsigned cur, beg, end;
  unsigned vo;
  unsigned ring;
  unsigned ta[4];
};
DI void cyc_add(unsigned& cur, unsigned inc, unsigned beg, unsigned end) {
  asm volatile("s_add_u32 %0, %0, %1\n\ts_cmp_eq_u32 %0, %3\n\ts_cselect_b32 %0, %2, %0" : "+s"(cur) : "s"(inc), "s"(beg), "s"(end) : "scc");
}
template <bool W1> struct WP { static constexpr int LDW = W1 ? F_ : S_, KW = W1 ? KW1 : KW2, NS = W1 ? NS1 : NS2, OFF = W1 ? 0 : (int)W2_OFF; };
template <bool W1, int KSTEP, int I> DI void glds_piece(WStream& s) {
  using P = WP<W1>;
  unsigned t;
  asm volatile("s_add_u32 m0, %1, %2\n\ts_add_u32 %0, %3, %4\n\tbuffer_load_dwordx4 %5, %6, %0 offen lds"
               : "=&s"(t)
               : "s"(s.ring), "i"(P::OFF + (KSTEP % P::NS) * 4096 + I * 1024), "s"(s.cur), "i"(I * 8 * P::LDW * 2), "v"(s.vo), "s"(s.srd)
               : "scc", "memory");
  if constexpr (I == 3) cyc_add(s.cur, 32 * P::LDW * 2, s.beg, s.end);
}
template <bool W1, int KSTEP> DI void glds_slot(WStream& s) {
  glds_piece<W1, KSTEP, 0>(s); glds_piece<W1, KSTEP, 1>(s); glds_piece<W1, KSTEP, 2>(s); glds_piece<W1, KSTEP, 3>(s);
}
template <bool W1, int KSTEP, int T, int H> DI void tr_piece(const WStream& s) {
  using P = WP<W1>;
  asm volatile("ds_read_b64_tr_b16 a[%c1:%c2], %0 offset:%c3" ::"v"(s.ta[T]), "i"(16 * KSTEP + 4 * T + 2 * H), "i"(16 * KSTEP + 4 * T + 2 * H + 1),
               "i"(P::OFF + (KSTEP % P::NS) * 4096 + H * 2048) : "memory", ACLOB);
}
template <bool W1, int KSTEP> DI void tr_slot(const WStream& s) {
  tr_piece<W1, KSTEP, 0, 0>(s); tr_piece<W1, KSTEP, 0, 1>(s); tr_piece<W1, KSTEP, 1, 0>(s); tr_piece<W1, KSTEP, 1, 1>(s);
  tr_piece<W1, KSTEP, 2, 0>(s); tr_piece<W1, KSTEP, 2, 1>(s); tr_piece<W1, KSTEP, 3, 0>(s); tr_piece<W1, KSTEP, 3, 1>(s);
}
constexpr int NB1 = PRE + MID;
static_assert(NB1 + NA <= NS1 && NB1 + NA0 <= NS1 && POST2 <= NS2, "schedule");
template <bool W1, int NAX> constexpr int younger(int j, bool flagged) {
  constexpr int KW = W1 ? KW1 : KW2, NS = W1 ? NS1 : NS2;
  const int before_stores = W1 ? NB1 : (W2POS ? TPF : NS2), before_flag = W1 ? NB1 + NAX : NS2 + POST2;
  const int last = j + NS - 1 < KW - 1 ? j + NS - 1 : KW - 1;
  return 4 * (last - j) + (j < before_stores ? 2 : 0) + (flagged && j < before_flag ? 1 : 0);
}
template <bool W1, int J, int MODE, int EXTRA, int NAX> DI void wpipe_step(WStream& s) {
  using P = WP<W1>;
  constexpr int rem = P::KW - 1 - J;
  waitv<(MODE == 0 ? 4 * (rem < P::NS - 1 ? rem : P::NS - 1) : younger<W1, NAX>(J, MODE == 2)) + EXTRA>();
  tr_slot<W1, J>(s);
  if constexpr (J + P::NS < P::KW) { wait_lds(); glds_slot<W1, J + P::NS>(s); }
  GAP();
}
template <bool W1, int J0, int MODE, int NAX, int... Js> DI void wpipe_stepsx(WStream& s, iseq<Js...>) { (wpipe_step<W1, J0 + Js, MODE, 0, NAX>(s), ...); }
template <bool W1, int J0, int MODE, int... Js> DI void wpipe_steps(WStream& s, iseq<Js...>) { (wpipe_step<W1, J0 + Js, MODE, 0, NA>(s), ...); }
template <bool W1, int J0, int EXTRA, int NAX, int... Js> DI void wtail_steps(WStream& s, iseq<Js...>) { (wpipe_step<W1, J0 + Js, 2, EXTRA, NAX>(s), ...); }
template <bool W1, int J0, int... Js> DI void glds_slots(WStream& s, iseq<Js...>) { (glds_slot<W1, J0 + Js>(s), ...); }

constexpr int RB = 160;
static_assert(RB + 4 * DF1 <= 256 && RB + 4 * DF2 <= 256, "ring");
template <int REG> DI void act_load(unsigned voff, srd_t srd, unsigned soff) {
  asm volatile("buffer_load_dwordx4 v[%c0:%c1], %2, %3, %4 offen sc1" ::"i"(REG), "i"(REG + 3), "v"(voff), "s"(srd), "s"(soff) : "memory");
}
struct AStream { srd_t srd; unsigned voff[4], cur, beg, end; };
template <bool WA, bool ZERO, int AR, int VR> DI void mfma(f32x4& acc) {
  if constexpr (WA) {
    if constexpr (ZERO) asm volatile("v_mfma_f32_16x16x32_bf16 %0, a[%c1:%c2], v[%c3:%c4], 0" : "=&v"(acc) : "i"(AR), "i"(AR + 3), "i"(VR), "i"(VR + 3) : ACLOB);
    else asm volatile("v_mfma_f32_16x16x32_bf16 %0, a[%c1:%c2], v[%c3:%c4], %0" : "+v"(acc) : "i"(AR), "i"(AR + 3), "i"(VR), "i"(VR + 3) : ACLOB);
  } else {
    if constexpr (ZERO) asm volatile("v_mfma_f32_16x16x32_bf16 %0, v[%c3:%c4], a[%c1:%c2], 0" : "=&v"(acc) : "i"(AR), "i"(AR + 3), "i"(VR), "i"(VR + 3) : ACLOB);
    else asm volatile("v_mfma_f32_16x16x32_bf16 %0, v[%c3:%c4], a[%c1:%c2], %0" : "+v"(acc) : "i"(AR), "i"(AR + 3), "i"(VR), "i"(VR + 3) : ACLOB);
  }
}
template <bool G1> constexpr int vop(int v) { return v < 4 * (G1 ? KW1 : KW2) ? 1 : (G1 && v - 4 * KW1 < 4 * TPF ? 1 : 0); }
template <bool G1> constexpr int vop_range(int a, int b) { int n = 0; for (int v = a; v <= b; ++v) n += vop<G1>(v); return n; }
template <bool G1, int V> DI void stream_op(AStream& a, WStream& w2) {
  constexpr int KW = G1 ? KW1 : KW2, DF = G1 ? DF1 : DF2;
  if constexpr (V < 4 * KW) { act_load<RB + 4 * (V % DF)>(a.voff[(V / KW + 1) & 3], a.srd, a.cur); cyc_add(a.cur, KSTRIDE, a.beg, a.end); }
  else if constexpr (vop<G1>(V) != 0) glds_piece<false, (V - 4 * KW) / 4, (V - 4 * KW) % 4>(w2);
}
struct Exch { LAS unsigned char* mine; const LAS unsigned char* from[3]; };
template <bool G1, int I> DI void gemm_frag(f32x4 (&acc)[4][4], f32x4 (&pre)[4], f32x4 (&r)[3][4], AStream& a, WStream& w2, const Exch& x) {
  constexpr int KW = G1 ? KW1 : KW2, DF = G1 ? DF1 : DF2, K = I % KW, M = (I / KW + 1) & 3, VR = RB + 4 * (I % DF);
  waitv<I == 0 ? vop_range<G1>(1, DF - 1) : vop_range<G1>(I + 1, I - 2 + DF)>();
  mfma<G1, K == 0, 16 * K + 0, VR>(acc[M][0]);
  if constexpr (I >= 1) stream_op<G1, I - 1 + DF>(a, w2);
  mfma<G1, K == 0, 16 * K + 4, VR>(acc[M][1]);
  mfma<G1, K == 0, 16 * K + 8, VR>(acc[M][2]);
  mfma<G1, K == 0, 16 * K + 12, VR>(acc[M][3]);
  if constexpr (I == 4 * KW - 1) stream_op<G1, I + DF>(a, w2);
  if constexpr (I % KW == 1 && I > KW) {
    constexpr int m = I / KW;
    asm volatile("" : "+v"(acc[m][0]), "+v"(acc[m][1]), "+v"(acc[m][2]), "+v"(acc[m][3]));
#pragma unroll
    for (int t = 0; t < 4; ++t) *(LAS f32x4*)(x.mine + (m - 1) * 4096 + t * 1024) = acc[m][t];
  }
  if constexpr (I == 3 * KW + HB) {
    wg_barrier();
#pragma unroll
    for (int j = 0; j < 3; ++j)
#pragma unroll
      for (int t = 0; t < 4; ++t) r[j][t] = *(const LAS f32x4*)(x.from[j] + t * 1024);
  }
  if constexpr (I == 3 * KW + HB + 2) {
#pragma unroll
    for (int t = 0; t < 4; ++t) pre[t] += r[0][t];
  }
  if constexpr (I == 3 * KW + HB + 3) {
#pragma unroll
    for (int t = 0; t < 4; ++t) pre[t] += r[1][t];
  }
  if constexpr (I == 3 * KW + HB + 4) {
#pragma unroll
    for (int t = 0; t < 4; ++t) pre[t] += r[2][t];
  }
  GAP();
}
template <bool G1, int... Is> DI void gemm_frags(f32x4 (&acc)[4][4], f32x4 (&pre)[4], f32x4 (&r)[3][4], AStream& a, WStream& w2, const Exch& x, iseq<Is...>) {
  (gemm_frag<G1, Is>(acc, pre, r, a, w2, x), ...);
}
template <bool G1, int... Vs> DI void gemm_prologue(AStream& a, WStream& w2, iseq<Vs...>) { (stream_op<G1, Vs>(a, w2), ...); GAP(); }
template <bool G1> DI void gemm_body(f32x4 (&pre)[4], AStream& a, WStream& w2, const Exch& x) {
  f32x4 acc[4][4], r[3][4];
  gemm_frags<G1>(acc, pre, r, a, w2, x, make_iseq<4 * (G1 ? KW1 : KW2)>{});
  asm volatile("s_nop 7" : "+v"(acc[0][0]), "+v"(acc[0][1]), "+v"(acc[0][2]), "+v"(acc[0][3]));
#pragma unroll
  for (int t = 0; t < 4; ++t) pre[t] += acc[0][t];
}
template <int SLEEP> DI void poll_flags(unsigned voff, srd_t srd, unsigned soff, unsigned tlo, unsigned thi) {
#pragma unroll 1
  for (int spin = 0; spin < SPIN_MAX; ++spin) {
    v2u f;
    asm volatile("buffer_load_dwordx2 %0, %1, %2, %3 offen sc1\n\ts_waitcnt vmcnt(0)" : "=v"(f) : "v"(voff), "s"(srd), "s"(soff) : "memory");
    if (__builtin_amdgcn_ballot_w64(f[0] != tlo || f[1] != thi) == 0) break;
    __builtin_amdgcn_s_sleep(SLEEP);
  }
}
template <int N> DI void poll2_issue(v4u (&f)[2], unsigned voff, srd_t srd, unsigned soff) {
  asm volatile("buffer_load_dwordx4 %0, %1, %2, %3 offen sc1" : "=v"(f[0]) : "v"(voff), "s"(srd), "s"(soff) : "memory");
  if constexpr (N == 2) asm volatile("buffer_load_dwordx4 %0, %1, %2, %3 offen offset:1024 sc1" : "=v"(f[1]) : "v"(voff), "s"(srd), "s"(soff) : "memory");
}
template <int N> DI bool poll2_check(v4u (&f)[2], bool use, unsigned tlo, unsigned thi) {
  if constexpr (N == 2) asm volatile("s_waitcnt vmcnt(0)" : "+v"(f[0]), "+v"(f[1])::"memory");
  else asm volatile("s_waitcnt vmcnt(0)" : "+v"(f[0])::"memory");
  const v4u a = f[0];
  bool bad = a[0] != tlo || a[1] != thi || a[2] != tlo || a[3] != thi;
  if constexpr (N == 2) { const v4u c = f[1]; bad = bad || c[0] != tlo || c[1] != thi || c[2] != tlo || c[3] != thi; }
  return __builtin_amdgcn_ballot_w64(bad && use) == 0;
}
template <int SLEEP, int N> DI void poll_flags2(unsigned voff, bool use, srd_t srd, unsigned soff, unsigned tlo, unsigned thi) {
#pragma unroll 1
  for (int spin = 0; spin < SPIN_MAX; ++spin) {
    v4u f[2];
    poll2_issue<N>(f, voff, srd, soff);
    if (poll2_check<N>(f, use, tlo, thi)) break;
    __builtin_amdgcn_s_sleep(SLEEP);
  }
}
DI void store16_sc1(v4u d, unsigned voff, srd_t srd, unsigned soff) {
  asm volatile("buffer_store_dwordx4 %0, %1, %2, %3 offen sc1\n\ts_nop 1" ::"v"(d), "v"(voff), "s"(srd), "s"(soff) : "memory");
}
DI void store8_sc1(v2u d, unsigned voff, srd_t srd, unsigned soff) {
  asm volatile("buffer_store_dwordx2 %0, %1, %2, %3 offen sc1\n\ts_nop 1" ::"v"(d), "v"(voff), "s"(srd), "s"(soff) : "memory");
}
DI float gelu(float u) {
  const float e = __builtin_amdgcn_exp2f(u * (-2.3022082f - 0.1029432f * u * u));
  return u * __builtin_amdgcn_rcpf(1.0f + e);
}
#define PA(i)
#define PB(i)
#define STAMP(i)
#define STAMPA(i)
#define STAMPB(i)
#define STAMPX0(i)
#define STAMPX1(i)

__global__ void __launch_bounds__(256, 1) __attribute__((amdgpu_num_vgpr(RB)))
mixer_kernel(const u16* __restrict__ x, const u16* __restrict__ w1, const u16* __restrict__ b1, const u16* __restrict__ w2, const u16* __restrict__ b2,
             u16* __restrict__ out, unsigned char* __restrict__ ws) {
  __shared__ __attribute__((aligned(1024))) unsigned char smem[LDS_BYTES];
  STAMP(0);
  asm volatile("" ::: ACLOB);
  asm volatile("" ::: "v160", "v255");
  const unsigned tid = threadIdx.x, lane = tid & 63, w = rfl(tid >> 6), b = blockIdx.x;
  const unsigned lds0 = (unsigned)(uintptr_t)(LAS unsigned char*)smem;
  const srd_t srdWS = mk_srd(ws, WS_USED);
  const unsigned g = lane >> 4, c = lane & 15;

  v2u ep; v4u xv[2];
  {
    const srd_t srdX = mk_srd(x, S_ * D_ * 2);
    const unsigned zero = 0, vx = lane < 56 ? lane * 16 : OOB, row = 32 * (b >> 2) + 4 * (b & 3) + 2 * (w & 1) + 16 * (w >> 1);
    asm volatile("buffer_load_dwordx4 %0, %1, %2, %3 offen" : "=v"(xv[0]) : "v"(vx), "s"(srdX), "s"(row * 896u) : "memory");
    asm volatile("buffer_load_dwordx4 %0, %1, %2, %3 offen offset:896" : "=v"(xv[1]) : "v"(vx), "s"(srdX), "s"(row * 896u) : "memory");
    asm volatile("buffer_load_dwordx2 %0, %1, %2, %3 offen sc1" : "=v"(ep) : "v"(zero), "s"(srdWS), "s"(EP_OFF) : "memory");
  }
  GAP();
  const unsigned xcd = b & 7, li = b >> 3;
  bool g1, g2;
  unsigned mb1, nb1, mb2, nb2;
  {
    const unsigned j = xcd >> 1, odd = xcd & 1, i0 = 7 + odd;
    g1 = li >= i0;
    const unsigned idx = g1 ? li - i0 + (odd ? 25 : 0) : 0;
    nb1 = 7 * j + idx / 7; mb1 = idx % 7;
    g2 = li < 28; nb2 = 4 * xcd + (li & 3); mb2 = g2 ? li >> 2 : 0;
  }
  const unsigned rot1 = ROT1 == 6 ? (nb1 % 7 != 3 ? (mb1 * 16) / 7 : mb1 < 4 ? 4 * mb1 : ((mb1 - 4) * 16) / 3) : ROT1 == 7 ? (nb1 % 7 != 3 ? (mb1 * 16) / 7 : mb1 < 4 ? 4 * mb1 + 1 : ((mb1 - 4) * 16) / 3 + 3) : ROT1 == 0 ? 0 : ROT1 == 1 ? (mb1 * 16) / 7 : ROT1 == 2 ? ((mb1 * 16) / 7 + 4 * (nb1 & 3)) & 15 : ROT1 == 3 ? (4 * (nb1 & 3)) & 15 : (((mb1 * ROTN1) / 7) * 16) / ROTN1;
  const unsigned rot2 = ROT2 == 0 ? 0 : ROT2 == 1 ? (mb2 * 8) % 14 : ROT2 == 2 ? (mb2 * 2 + ((nb2 & 3) * 7) / 2) % 14 : ROT2 == 3 ? ((nb2 & 3) * 7) / 2 : (((mb2 * ROTN2) / 7) * 14) / ROTN2;
  WStream W1, W2;
  const unsigned region = lds0 + w * REG_W;
  {
    const unsigned r8 = lane >> 3, pc = (lane & 7) ^ (r8 & 6);
    const unsigned q = (lane >> 2) & 3, p = lane & 3, row = 4 * g + q;
    const unsigned t0 = region + row * 128 + ((row >> 1) & 3) * 32 + p * 8;
    W1.srd = mk_srd(w1, S_ * F_ * 2);
    W2.srd = mk_srd(w2, g2 ? F_ * S_ * 2 : 0);
    W1.beg = w * (KW1 * 32) * F_ * 2 + nb1 * 128; W1.end = W1.beg + KW1 * 32 * F_ * 2; W1.cur = W1.beg + rot1 * (32 * F_ * 2);
    W2.beg = w * (KW2 * 32) * S_ * 2 + nb2 * 128; W2.end = W2.beg + KW2 * 32 * S_ * 2; W2.cur = W2.beg + rot2 * (32 * S_ * 2);
    W1.vo = r8 * F_ * 2 + pc * 16;
    W2.vo = r8 * S_ * 2 + pc * 16;
    W1.ring = W2.ring = region;
#pragma unroll
    for (int t = 0; t < 4; ++t) { W1.ta[t] = W2.ta[t] = t0 ^ (t << 5); }
  }
  constexpr int NT = 3;
  {
    const srd_t srdO = mk_srd(out, S_ * D_ * 2), srdB1 = mk_srd(b1, F_ * 2), srdB2 = mk_srd(b2, S_ * 2);
    asm volatile("buffer_load_dword v252, %0, %1, %2 offen" ::"v"(lane < 16 ? lane * (D_ * 2) : OOB), "s"(srdO), "s"((nb2 * 64 + 16 * w) * (D_ * 2) + mb2 * 128) : "memory");
    asm volatile("buffer_load_dword v254, %0, %1, %2 offen" ::"v"(lane == 0 ? 0 : OOB), "s"(srdB1), "s"(nb1 * 128u) : "memory");
    asm volatile("buffer_load_dword v255, %0, %1, %2 offen" ::"v"(lane == 0 ? 0 : OOB), "s"(srdB2), "s"(nb2 * 128u) : "memory");
  }
  if (g1) glds_slots<true, 0>(W1, make_iseq<PRE>{}); else waitv<0>();
  asm volatile("s_waitcnt vmcnt(%3)" : "+v"(ep), "+v"(xv[0]), "+v"(xv[1]) : "i"(4 * PRE + NT) : "memory");
  GAP();
  STAMP(1);
  if (g1) glds_slots<true, PRE>(W1, make_iseq<MID>{});
  GAP();
  unsigned tlo, thi;
  {
    const unsigned long long e = (((unsigned long long)ep[1] << 32) | ep[0]) + 1ull;
    tlo = rfl((unsigned)e); thi = rfl((unsigned)(e >> 32));
  }
  {
    float f[2][8], m[2], qs[2];
#pragma unroll
    for (int r = 0; r < 2; ++r) {
#pragma unroll
      for (int i = 0; i < 4; ++i) { f[r][2 * i] = bflo(xv[r][i]); f[r][2 * i + 1] = bfhi(xv[r][i]); }
      m[r] = ((f[r][0] + f[r][1]) + (f[r][2] + f[r][3])) + ((f[r][4] + f[r][5]) + (f[r][6] + f[r][7]));
    }
    wave_sum2(m[0], m[1]);
#pragma unroll
    for (int r = 0; r < 2; ++r) {
      const float mu = m[r] * (1.0f / D_);
#pragma unroll
      for (int i = 0; i < 8; ++i) f[r][i] -= mu;
      qs[r] = ((f[r][0] * f[r][0] + f[r][1] * f[r][1]) + (f[r][2] * f[r][2] + f[r][3] * f[r][3])) + ((f[r][4] * f[r][4] + f[r][5] * f[r][5]) + (f[r][6] * f[r][6] + f[r][7] * f[r][7]));
      qs[r] = lane < 56 ? qs[r] : 0.f;
    }
    wave_sum2(qs[0], qs[1]);
    v4u o[2];
#pragma unroll
    for (int r = 0; r < 2; ++r) {
      const float rs = __builtin_amdgcn_rsqf(qs[r] * (1.0f / D_) + 1e-5f);
      o[r] = v4u{pack2(f[r][0] * rs, f[r][1] * rs), pack2(f[r][2] * rs, f[r][3] * rs), pack2(f[r][4] * rs, f[r][5] * rs), pack2(f[r][6] * rs, f[r][7] * rs)};
    }
    if (lane < 56) {
      const unsigned ro = w * REG_W + LN_OFF + (w & 1) * 128 + lane * 16;
      *(LAS v4u*)(smem + ro) = o[0];
      *(LAS v4u*)(smem + ro + LNP) = o[1];
    }
  }
  wg_barrier();
  {
    v2u lo[2], hi[2];
    const unsigned q = (lane >> 2) & 3;
    const unsigned tlo_a = lds0 + (q >> 1) * (REG_W + 128) + LN_OFF + (q & 1) * LNP + g * 32 + (lane & 3) * 8 + w * 128, thi_a = tlo_a + 2 * REG_W;
    asm volatile("ds_read_b64_tr_b16 %0, %1 offset:0" : "=v"(lo[0]) : "v"(tlo_a) : "memory");
    asm volatile("ds_read_b64_tr_b16 %0, %1 offset:0" : "=v"(hi[0]) : "v"(thi_a) : "memory");
    asm volatile("ds_read_b64_tr_b16 %0, %1 offset:512" : "=v"(lo[1]) : "v"(tlo_a) : "memory");
    asm volatile("ds_read_b64_tr_b16 %0, %1 offset:512" : "=v"(hi[1]) : "v"(thi_a) : "memory");
    asm volatile("s_waitcnt lgkmcnt(0)" : "+v"(lo[0]), "+v"(lo[1]), "+v"(hi[0]), "+v"(hi[1])::"memory");
    const unsigned vo = g * HT + c * 16 + (b & 3) * 256;
    const unsigned so = H_OFF + (b >> 2) * KSTRIDE + w * 4 * HT;
    store16_sc1(v4u{lo[0][0], lo[0][1], hi[0][0], hi[0][1]}, vo, srdWS, so);
    store16_sc1(v4u{lo[1][0], lo[1][1], hi[1][0], hi[1][1]}, w < 3 ? vo : OOB, srdWS, so + 16 * HT);
    const unsigned vf = lane < R1 ? lane * 8192 + w * 2048 + b * 8 : OOB;
    if (!g1) {
      waitv<0>();
      store8_sc1(v2u{tlo, thi}, vf, srdWS, HF_OFF);
      STAMP(2);
    } else if (w == 0) {
      glds_slots<true, NB1>(W1, make_iseq<NA0>{});
      waitv<4 * NA0>();
      store8_sc1(v2u{tlo, thi}, vf, srdWS, HF_OFF);
      STAMP(2);
      glds_slots<true, NB1 + NA0>(W1, make_iseq<NS1 - NB1 - NA0>{});
      wpipe_stepsx<true, 0, 2, NA0>(W1, make_iseq<7>{});
    } else {
      glds_slots<true, NB1>(W1, make_iseq<NA>{});
      waitv<4 * NA>();
      store8_sc1(v2u{tlo, thi}, vf, srdWS, HF_OFF);
      STAMP(2);
      if (WSLEEP1) __builtin_amdgcn_s_sleep(WSLEEP1);
      glds_slots<true, NB1 + NA>(W1, make_iseq<NS1 - NB1 - NA>{});
      wpipe_stepsx<true, 0, 2, NA>(W1, make_iseq<7>{});
    }
  }
  Exch X;
  X.mine = (LAS unsigned char*)smem + w * REG_W + lane * 16;
#pragma unroll
  for (int j = 0; j < 3; ++j) X.from[j] = (const LAS unsigned char*)smem + ((w - 1 - j) & 3) * REG_W + j * 4096 + lane * 16;
  if (g1) {
    v2u bv[4];
    {
      const srd_t srdB1 = mk_srd(b1, F_ * 2);
      asm volatile("buffer_load_dwordx2 %0, %1, %2, %3 offen" : "=v"(bv[0]) : "v"(g * 8), "s"(srdB1), "s"(nb1 * 128u) : "memory");
      asm volatile("buffer_load_dwordx2 %0, %1, %2, %3 offen offset:32" : "=v"(bv[1]) : "v"(g * 8), "s"(srdB1), "s"(nb1 * 128u) : "memory");
      asm volatile("buffer_load_dwordx2 %0, %1, %2, %3 offen offset:64" : "=v"(bv[2]) : "v"(g * 8), "s"(srdB1), "s"(nb1 * 128u) : "memory");
      asm volatile("buffer_load_dwordx2 %0, %1, %2, %3 offen offset:96" : "=v"(bv[3]) : "v"(g * 8), "s"(srdB1), "s"(nb1 * 128u) : "memory");
    }
    GAP();
    if (w == 0) {
      const unsigned so = HF_OFF + (b % R1) * 8192 + (mb1 & 3) * 2048;
      v4u f[2];
      poll2_issue<2>(f, lane * 16, srdWS, so);
      wtail_steps<true, 7, 6, NA0>(W1, make_iseq<KW1 - 7>{});
      STAMPX0(3);
      if (!poll2_check<2>(f, true, tlo, thi)) poll_flags2<PSLEEP, 2>(lane * 16, true, srdWS, so, tlo, thi);
    }
    asm volatile("s_barrier" ::: "memory");
    STAMPA(4);
    AStream A;
    A.srd = srdWS; A.beg = H_OFF + 4 * mb1 * HT + w * KW1 * KSTRIDE; A.end = A.beg + KW1 * KSTRIDE; A.cur = A.beg + rot1 * KSTRIDE;
#pragma unroll
    for (int m = 0; m < 4; ++m) A.voff[m] = lane * 16 + ((m + w) & 3) * HT;
    gemm_prologue<true>(A, W2, make_iseq<DF1>{});
    if (w != 0) { wtail_steps<true, 7, 4 + DF1, NA>(W1, make_iseq<KW1 - 7>{}); STAMPX1(3); }
    GAP();
    asm volatile("s_waitcnt vmcnt(%4) lgkmcnt(0)" : "+v"(bv[0]), "+v"(bv[1]), "+v"(bv[2]), "+v"(bv[3]) : "i"(DF1) : "memory");
    f32x4 s[4];
#pragma unroll
    for (int t = 0; t < 4; ++t) { const v2u q = bv[t]; const unsigned q0 = q[0], q1 = q[1]; s[t] = f32x4{bflo(q0), bfhi(q0), bflo(q1), bfhi(q1)}; }
    GAP();
    gemm_body<true>(s, A, W2, X);
    STAMPA(5);
    if (!W2POS) glds_slots<false, TPF>(W2, make_iseq<NS2 - TPF>{});
    GAP();
#pragma unroll
    for (int kk = 0; kk < 2; ++kk) {
      const f32x4 s0 = s[2 * kk], s1 = s[2 * kk + 1];
      const v4u o = {pack2(gelu(s0[0]), gelu(s0[1])), pack2(gelu(s0[2]), gelu(s0[3])), pack2(gelu(s1[0]), gelu(s1[1])), pack2(gelu(s1[2]), gelu(s1[3]))};
      store16_sc1(o, lane * 16, srdWS, U_OFF + (4 * mb1 + w) * UT + (2 * nb1 + kk) * KSTRIDE);
    }
    GAP();
    if (W2POS) glds_slots<false, TPF>(W2, make_iseq<NS2 - TPF>{});
    wpipe_steps<false, 0, 1>(W2, make_iseq<POST2>{});
    waitv<4 * POST2 + (W2POS ? 4 * (NS2 - TPF) : 0)>();
    store8_sc1(v2u{tlo, thi}, lane < R2 ? lane * UF_REP + (mb1 * 4 + nb1 / 7) * 256 + ((nb1 % 7) * 4 + w) * 8 : OOB, srdWS, UF_OFF);
    STAMP(6);
    if (!g2) {
      if (b == EPOCH_WG && w == 1) store8_sc1(v2u{tlo, thi}, lane == 0 ? 0 : OOB, srdWS, EP_OFF);
      waitv<0>();
      goto done;
    }
    if (w != 0) __builtin_amdgcn_s_sleep(WSLEEP2);
    wpipe_steps<false, POST2, 2>(W2, make_iseq<NS2 - POST2>{});
  } else {
    wg_barrier();
    if (!g2) goto done;
#pragma unroll 1
    for (int i = 0; i < IDLE_SLEEP; ++i) __builtin_amdgcn_s_sleep(16);
    glds_slots<false, 0>(W2, make_iseq<NS2>{});
    wpipe_steps<false, 0, 0>(W2, make_iseq<KW2>{});
    if (w == 0) poll_flags<8>(0, srdWS, EP_OFF, tlo, thi);
    wg_barrier();
  }
  {
    unsigned bv[4];
    {
      const srd_t srdB2 = mk_srd(b2, S_ * 2);
      asm volatile("buffer_load_ushort %0, %1, %2, %3 offen" : "=v"(bv[0]) : "v"(c * 2), "s"(srdB2), "s"(nb2 * 128u) : "memory");
      asm volatile("buffer_load_ushort %0, %1, %2, %3 offen offset:32" : "=v"(bv[1]) : "v"(c * 2), "s"(srdB2), "s"(nb2 * 128u) : "memory");
      asm volatile("buffer_load_ushort %0, %1, %2, %3 offen offset:64" : "=v"(bv[2]) : "v"(c * 2), "s"(srdB2), "s"(nb2 * 128u) : "memory");
      asm volatile("buffer_load_ushort %0, %1, %2, %3 offen offset:96" : "=v"(bv[3]) : "v"(c * 2), "s"(srdB2), "s"(nb2 * 128u) : "memory");
    }
    GAP();
    if (w == 0) {
      const unsigned so = UF_OFF + (nb2 % R2) * UF_REP + mb2 * 1024;
      v4u f[2];
      poll2_issue<1>(f, lane * 16, srdWS, so);
      if (g1) wtail_steps<false, NS2, 5, NA>(W2, make_iseq<KW2 - NS2>{});
      if (!poll2_check<1>(f, c < 14, tlo, thi)) poll_flags2<PSLEEP, 1>(lane * 16, c < 14, srdWS, so, tlo, thi);
    }
    asm volatile("s_barrier" ::: "memory");
    STAMPB(4);
    AStream A;
    A.srd = srdWS; A.beg = U_OFF + 4 * mb2 * UT + w * KW2 * KSTRIDE; A.end = A.beg + KW2 * KSTRIDE; A.cur = A.beg + rot2 * KSTRIDE;
#pragma unroll
    for (int m = 0; m < 4; ++m) A.voff[m] = lane * 16 + ((m + w) & 3) * UT;
    gemm_prologue<false>(A, W2, make_iseq<DF2>{});
    if (w != 0 && g1) wtail_steps<false, NS2, 4 + DF2, NA>(W2, make_iseq<KW2 - NS2>{});
    STAMPB(3);
    GAP();
    asm volatile("s_waitcnt vmcnt(%4) lgkmcnt(0)" : "+v"(bv[0]), "+v"(bv[1]), "+v"(bv[2]), "+v"(bv[3]) : "i"(DF2) : "memory");
    f32x4 s[4];
#pragma unroll
    for (int t = 0; t < 4; ++t) { const float bb = bflo(bv[t]); s[t] = f32x4{bb, bb, bb, bb}; }
    GAP();
    gemm_body<false>(s, A, W2, X);
    STAMPB(5);
    unsigned char* stage = smem + W2_OFF;
#pragma unroll
    for (int t = 0; t < 4; ++t) *(LAS v2u*)((LAS unsigned char*)stage + (16 * t + c) * 144 + 32 * w + 8 * g) = v2u{pack2(s[t][0], s[t][1]), pack2(s[t][2], s[t][3])};
    wg_barrier();
    const srd_t srdO = mk_srd(out, S_ * D_ * 2);
    const unsigned row = 16 * w + (lane >> 3), col = (lane & 7) * 16;
#pragma unroll
    for (int mp = 0; mp < 2; ++mp) {
      const v4u o = *(const LAS v4u*)((LAS unsigned char*)stage + (row + 8 * mp) * 144 + col);
      asm volatile("buffer_store_dwordx4 %0, %1, %2, %3 offen\n\ts_nop 1" ::"v"(o), "v"((row + 8 * mp) * (D_ * 2) + col), "s"(srdO), "s"(nb2 * 64 * (D_ * 2) + mb2 * 128) : "memory");
    }
  }
done:
  STAMP(7);
}

extern "C" void kernel_launch(void* const* d_in, const int* in_sizes, int n_in, void* d_out, int out_size, void* d_ws, size_t ws_size, hipStream_t stream) {
  mixer_kernel<<<256, 256, 0, stream>>>((const u16*)d_in[0], (const u16*)d_in[1], (const u16*)d_in[2], (const u16*)d_in[3], (const u16*)d_in[4],
                                       (u16*)d_out, (unsigned char*)d_ws);
}
```

```cpp
#include <hip/hip_runtime.h>
#include <stdint.h>
#include <utility>

typedef unsigned v4u __attribute__((ext_vector_type(4)));
typedef unsigned v2u __attribute__((ext_vector_type(2)));
typedef float f32x4 __attribute__((ext_vector_type(4)));
typedef unsigned short u16;
typedef __amdgpu_buffer_rsrc_t srd_t;
#define DI __device__ __forceinline__
#define LAS __attribute__((address_space(3)))
#define GAP() __builtin_amdgcn_sched_barrier(0)
template <int... Is> using iseq = std::integer_sequence<int, Is...>;
template <int N> using make_iseq = std::make_integer_sequence<int, N>;
#define ACLOB "a0","a1","a2","a3","a4","a5","a6","a7","a8","a9","a10","a11","a12","a13","a14","a15","a16","a17","a18","a19","a20","a21","a22","a23","a24","a25","a26","a27","a28","a29","a30","a31","a32","a33","a34","a35","a36","a37","a38","a39","a40","a41","a42","a43","a44","a45","a46","a47","a48","a49","a50","a51","a52","a53","a54","a55","a56","a57","a58","a59","a60","a61","a62","a63","a64","a65","a66","a67","a68","a69","a70","a71","a72","a73","a74","a75","a76","a77","a78","a79","a80","a81","a82","a83","a84","a85","a86","a87","a88","a89","a90","a91","a92","a93","a94","a95","a96","a97","a98","a99","a100","a101","a102","a103","a104","a105","a106","a107","a108","a109","a110","a111","a112","a113","a114","a115","a116","a117","a118","a119","a120","a121","a122","a123","a124","a125","a126","a127","a128","a129","a130","a131","a132","a133","a134","a135","a136","a137","a138","a139","a140","a141","a142","a143","a144","a145","a146","a147","a148","a149","a150","a151","a152","a153","a154","a155","a156","a157","a158","a159","a160","a161","a162","a163","a164","a165","a166","a167","a168","a169","a170","a171","a172","a173","a174","a175","a176","a177","a178","a179","a180","a181","a182","a183","a184","a185","a186","a187","a188","a189","a190","a191","a192","a193","a194","a195","a196","a197","a198","a199","a200","a201","a202","a203","a204","a205","a206","a207","a208","a209","a210","a211","a212","a213","a214","a215","a216","a217","a218","a219","a220","a221","a222","a223","a224","a225","a226","a227","a228","a229","a230","a231","a232","a233","a234","a235","a236","a237","a238","a239","a240","a241","a242","a243","a244","a245","a246","a247","a248","a249","a250","a251","a252","a253","a254","a255"

#define TOUCH 5
#define MAPPING 3
#define DF1 16
#define DF2 16
#define HB 5
#define W2POS 1
#define PRE 3
#define MID 0
#define NA 3
#define NA0 3
#define EARLYPOLL 1
#define POST2 0
#define ROT1 6
#define ROT2 1
#define ROTN1 2
#define ROTN2 2
#define IDLE_SLEEP 0
#define PSLEEP 2
#define QUIET 1
#define WSLEEP1 16
#define WSLEEP2 16
#define R1 4
#define R2 4

constexpr int S_ = 2048, D_ = 448, F_ = 1792;
constexpr int KW1 = 16, KW2 = 14;
constexpr int NS1 = 9, NS2 = 7;
#define TPFMAX 6
constexpr int TPF = DF1 / 4 < TPFMAX ? DF1 / 4 : TPFMAX;
constexpr unsigned KSTRIDE = 1024u;
constexpr unsigned HT = 64u * KSTRIDE, UT = 56u * KSTRIDE;
constexpr unsigned H_OFF = 0, U_OFF = 28u * HT;
constexpr unsigned EP_OFF = 4u << 20, HF_OFF = EP_OFF + 4096u, UF_OFF = HF_OFF + 16u * 8192u;
constexpr unsigned UF_REP = 28u * 256u;
constexpr unsigned WS_USED = UF_OFF + 16u * UF_REP;
constexpr unsigned REG_W = 40960, LN_OFF = 36864, LNP = 960, W2_OFF = 12288, LDS_BYTES = 4 * REG_W;
constexpr unsigned OOB = 0x7ffffff0u;
constexpr int SPIN_MAX = 1 << 15;
constexpr unsigned EPOCH_WG = 225;

DI unsigned rfl(unsigned x) { return __builtin_amdgcn_readfirstlane(x); }
DI float bflo(unsigned w) { return __builtin_bit_cast(float, w << 16); }
DI float bfhi(unsigned w) { return __builtin_bit_cast(float, w & 0xffff0000u); }
DI unsigned pack2(float lo, float hi) {
  typedef __bf16 bf2 __attribute__((ext_vector_type(2)));
  typedef float f2 __attribute__((ext_vector_type(2)));
  f2 v = {lo, hi};
  return __builtin_bit_cast(unsigned, __builtin_convertvector(v, bf2));
}
DI srd_t mk_srd(const void* p, unsigned bytes) { return __builtin_amdgcn_make_buffer_rsrc((void*)p, 0, bytes, 0x00020000); }
template <int C> DI float dpp_add(float v) {
  return v + __builtin_bit_cast(float, __builtin_amdgcn_update_dpp(0, __builtin_bit_cast(int, v), C, 0xf, 0xf, true));
}
DI void wave_sum2(float& a, float& b) {
  a = dpp_add<0xB1>(a); b = dpp_add<0xB1>(b); a = dpp_add<0x4E>(a); b = dpp_add<0x4E>(b);
  a = dpp_add<0x141>(a); b = dpp_add<0x141>(b); a = dpp_add<0x140>(a); b = dpp_add<0x140>(b);
  auto p = __builtin_amdgcn_permlane16_swap(__builtin_bit_cast(unsigned, a), __builtin_bit_cast(unsigned, b), false, false);
  const unsigned p0 = p[0], p1 = p[1];
  float s = __builtin_bit_cast(float, p0) + __builtin_bit_cast(float, p1);
  auto q = __builtin_amdgcn_permlane32_swap(__builtin_bit_cast(unsigned, s), __builtin_bit_cast(unsigned, s), false, false);
  const unsigned q0 = q[0], q1 = q[1];
  s = __builtin_bit_cast(float, q0) + __builtin_bit_cast(float, q1);
  a = __builtin_bit_cast(float, __builtin_amdgcn_readlane(__builtin_bit_cast(int, s), 0));
  b = __builtin_bit_cast(float, __builtin_amdgcn_readlane(__builtin_bit_cast(int, s), 16));
}
template <int N> DI void waitv() { static_assert(N >= 0 && N < 64, "vmcnt"); asm volatile("s_waitcnt vmcnt(%0)" ::"i"(N) : "memory"); }
DI void wait_lds() { asm volatile("s_waitcnt lgkmcnt(0)" ::: "memory"); }
DI void wg_barrier() { asm volatile("s_waitcnt lgkmcnt(0)\n\ts_barrier" ::: "memory"); }

struct WStream {
  srd_t srd;
  unsigned cur, beg, end;
  unsigned vo;
  unsigned ring;
  unsigned ta[4];
};
DI void cyc_add(unsigned& cur, unsigned inc, unsigned beg, unsigned end) {
  asm volatile("s_add_u32 %0, %0, %1\n\ts_cmp_eq_u32 %0, %3\n\ts_cselect_b32 %0, %2, %0" : "+s"(cur) : "s"(inc), "s"(beg), "s"(end) : "scc");
}
template <bool W1> struct WP { static constexpr int LDW = W1 ? F_ : S_, KW = W1 ? KW1 : KW2, NS = W1 ? NS1 : NS2, OFF = W1 ? 0 : (int)W2_OFF; };
template <bool W1, int KSTEP, int I> DI void glds_piece(WStream& s) {
  using P = WP<W1>;
  unsigned t;
  asm volatile("s_add_u32 m0, %1, %2\n\ts_add_u32 %0, %3, %4\n\tbuffer_load_dwordx4 %5, %6, %0 offen lds"
               : "=&s"(t)
               : "s"(s.ring), "i"(P::OFF + (KSTEP % P::NS) * 4096 + I * 1024), "s"(s.cur), "i"(I * 8 * P::LDW * 2), "v"(s.vo), "s"(s.srd)
               : "scc", "memory");
  if constexpr (I == 3) cyc_add(s.cur, 32 * P::LDW * 2, s.beg, s.end);
}
template <bool W1, int KSTEP> DI void glds_slot(WStream& s) {
  glds_piece<W1, KSTEP, 0>(s); glds_piece<W1, KSTEP, 1>(s); glds_piece<W1, KSTEP, 2>(s); glds_piece<W1, KSTEP, 3>(s);
}
template <bool W1, int KSTEP, int T, int H> DI void tr_piece(const WStream& s) {
  using P = WP<W1>;
  asm volatile("ds_read_b64_tr_b16 a[%c1:%c2], %0 offset:%c3" ::"v"(s.ta[T]), "i"(16 * KSTEP + 4 * T + 2 * H), "i"(16 * KSTEP + 4 * T + 2 * H + 1),
               "i"(P::OFF + (KSTEP % P::NS) * 4096 + H * 2048) : "memory", ACLOB);
}
template <bool W1, int KSTEP> DI void tr_slot(const WStream& s) {
  tr_piece<W1, KSTEP, 0, 0>(s); tr_piece<W1, KSTEP, 0, 1>(s); tr_piece<W1, KSTEP, 1, 0>(s); tr_piece<W1, KSTEP, 1, 1>(s);
  tr_piece<W1, KSTEP, 2, 0>(s); tr_piece<W1, KSTEP, 2, 1>(s); tr_piece<W1, KSTEP, 3, 0>(s); tr_piece<W1, KSTEP, 3, 1>(s);
}
constexpr int NB1 = PRE + MID;
static_assert(NB1 + NA <= NS1 && NB1 + NA0 <= NS1 && POST2 <= NS2, "schedule");
template <bool W1, int NAX> constexpr int younger(int j, bool flagged) {
  constexpr int KW = W1 ? KW1 : KW2, NS = W1 ? NS1 : NS2;
  const int before_stores = W1 ? NB1 : (W2POS ? TPF : NS2), before_flag = W1 ? NB1 + NAX : NS2 + POST2;
  const int last = j + NS - 1 < KW - 1 ? j + NS - 1 : KW - 1;
  return 4 * (last - j) + (j < before_stores ? 2 : 0) + (flagged && j < before_flag ? 1 : 0);
}
template <bool W1, int J, int MODE, int EXTRA, int NAX> DI void wpipe_step(WStream& s) {
  using P = WP<W1>;
  constexpr int rem = P::KW - 1 - J;
  waitv<(MODE == 0 ? 4 * (rem < P::NS - 1 ? rem : P::NS - 1) : younger<W1, NAX>(J, MODE == 2)) + EXTRA>();
  tr_slot<W1, J>(s);
  if constexpr (J + P::NS < P::KW) { wait_lds(); glds_slot<W1, J + P::NS>(s); }
  GAP();
}
template <bool W1, int J0, int MODE, int NAX, int... Js> DI void wpipe_stepsx(WStream& s, iseq<Js...>) { (wpipe_step<W1, J0 + Js, MODE, 0, NAX>(s), ...); }
template <bool W1, int J0, int MODE, int... Js> DI void wpipe_steps(WStream& s, iseq<Js...>) { (wpipe_step<W1, J0 + Js, MODE, 0, NA>(s), ...); }
template <bool W1, int J0, int EXTRA, int NAX, int... Js> DI void wtail_steps(WStream& s, iseq<Js...>) { (wpipe_step<W1, J0 + Js, 2, EXTRA, NAX>(s), ...); }
template <bool W1, int J0, int... Js> DI void glds_slots(WStream& s, iseq<Js...>) { (glds_slot<W1, J0 + Js>(s), ...); }

constexpr int RB = 160;
static_assert(RB + 4 * DF1 <= 256 && RB + 4 * DF2 <= 256, "ring");
template <int REG> DI void act_load(unsigned voff, srd_t srd, unsigned soff) {
  asm volatile("buffer_load_dwordx4 v[%c0:%c1], %2, %3, %4 offen sc1" ::"i"(REG), "i"(REG + 3), "v"(voff), "s"(srd), "s"(soff) : "memory");
}
struct AStream { srd_t srd; unsigned voff[4], cur, beg, end; };
template <bool WA, bool ZERO, int AR, int VR> DI void mfma(f32x4& acc) {
  if constexpr (WA) {
    if constexpr (ZERO) asm volatile("v_mfma_f32_16x16x32_bf16 %0, a[%c1:%c2], v[%c3:%c4], 0" : "=&v"(acc) : "i"(AR), "i"(AR + 3), "i"(VR), "i"(VR + 3) : ACLOB);
    else asm volatile("v_mfma_f32_16x16x32_bf16 %0, a[%c1:%c2], v[%c3:%c4], %0" : "+v"(acc) : "i"(AR), "i"(AR + 3), "i"(VR), "i"(VR + 3) : ACLOB);
  } else {
    if constexpr (ZERO) asm volatile("v_mfma_f32_16x16x32_bf16 %0, v[%c3:%c4], a[%c1:%c2], 0" : "=&v"(acc) : "i"(AR), "i"(AR + 3), "i"(VR), "i"(VR + 3) : ACLOB);
    else asm volatile("v_mfma_f32_16x16x32_bf16 %0, v[%c3:%c4], a[%c1:%c2], %0" : "+v"(acc) : "i"(AR), "i"(AR + 3), "i"(VR), "i"(VR + 3) : ACLOB);
  }
}
template <bool G1> constexpr int vop(int v) { return v < 4 * (G1 ? KW1 : KW2) ? 1 : (G1 && v - 4 * KW1 < 4 * TPF ? 1 : 0); }
template <bool G1> constexpr int vop_range(int a, int b) { int n = 0; for (int v = a; v <= b; ++v) n += vop<G1>(v); return n; }
template <bool G1, int V> DI void stream_op(AStream& a, WStream& w2) {
  constexpr int KW = G1 ? KW1 : KW2, DF = G1 ? DF1 : DF2;
  if constexpr (V < 4 * KW) { act_load<RB + 4 * (V % DF)>(a.voff[(V / KW + 1) & 3], a.srd, a.cur); cyc_add(a.cur, KSTRIDE, a.beg, a.end); }
  else if constexpr (vop<G1>(V) != 0) glds_piece<false, (V - 4 * KW) / 4, (V - 4 * KW) % 4>(w2);
}
struct Exch { LAS unsigned char* mine; const LAS unsigned char* from[3]; };
template <bool G1, int I> DI void gemm_frag(f32x4 (&acc)[4][4], f32x4 (&pre)[4], f32x4 (&r)[3][4], AStream& a, WStream& w2, const Exch& x) {
  constexpr int KW = G1 ? KW1 : KW2, DF = G1 ? DF1 : DF2, K = I % KW, M = (I / KW + 1) & 3, VR = RB + 4 * (I % DF);
  waitv<I == 0 ? vop_range<G1>(1, DF - 1) : vop_range<G1>(I + 1, I - 2 + DF)>();
  mfma<G1, K == 0, 16 * K + 0, VR>(acc[M][0]);
  if constexpr (I >= 1) stream_op<G1, I - 1 + DF>(a, w2);
  mfma<G1, K == 0, 16 * K + 4, VR>(acc[M][1]);
  mfma<G1, K == 0, 16 * K + 8, VR>(acc[M][2]);
  mfma<G1, K == 0, 16 * K + 12, VR>(acc[M][3]);
  if constexpr (I == 4 * KW - 1) stream_op<G1, I + DF>(a, w2);
  if constexpr (I % KW == 1 && I > KW) {
    constexpr int m = I / KW;
    asm volatile("" : "+v"(acc[m][0]), "+v"(acc[m][1]), "+v"(acc[m][2]), "+v"(acc[m][3]));
#pragma unroll
    for (int t = 0; t < 4; ++t) *(LAS f32x4*)(x.mine + (m - 1) * 4096 + t * 1024) = acc[m][t];
  }
  if constexpr (I == 3 * KW + HB) {
    wg_barrier();
#pragma unroll
    for (int j = 0; j < 3; ++j)
#pragma unroll
      for (int t = 0; t < 4; ++t) r[j][t] = *(const LAS f32x4*)(x.from[j] + t * 1024);
  }
  if constexpr (I == 3 * KW + HB + 2) {
#pragma unroll
    for (int t = 0; t < 4; ++t) pre[t] += r[0][t];
  }
  if constexpr (I == 3 * KW + HB + 3) {
#pragma unroll
    for (int t = 0; t < 4; ++t) pre[t] += r[1][t];
  }
  if constexpr (I == 3 * KW + HB + 4) {
#pragma unroll
    for (int t = 0; t < 4; ++t) pre[t] += r[2][t];
  }
  GAP();
}
template <bool G1, int... Is> DI void gemm_frags(f32x4 (&acc)[4][4], f32x4 (&pre)[4], f32x4 (&r)[3][4], AStream& a, WStream& w2, const Exch& x, iseq<Is...>) {
  (gemm_frag<G1, Is>(acc, pre, r, a, w2, x), ...);
}
template <bool G1, int... Vs> DI void gemm_prologue(AStream& a, WStream& w2, iseq<Vs...>) { (stream_op<G1, Vs>(a, w2), ...); GAP(); }
template <bool G1> DI void gemm_body(f32x4 (&pre)[4], AStream& a, WStream& w2, const Exch& x) {
  f32x4 acc[4][4], r[3][4];
  gemm_frags<G1>(acc, pre, r, a, w2, x, make_iseq<4 * (G1 ? KW1 : KW2)>{});
  asm volatile("s_nop 7" : "+v"(acc[0][0]), "+v"(acc[0][1]), "+v"(acc[0][2]), "+v"(acc[0][3]));
#pragma unroll
  for (int t = 0; t < 4; ++t) pre[t] += acc[0][t];
}
template <int SLEEP> DI void poll_flags(unsigned voff, srd_t srd, unsigned soff, unsigned tlo, unsigned thi) {
#pragma unroll 1
  for (int spin = 0; spin < SPIN_MAX; ++spin) {
    v2u f;
    asm volatile("buffer_load_dwordx2 %0, %1, %2, %3 offen sc1\n\ts_waitcnt vmcnt(0)" : "=v"(f) : "v"(voff), "s"(srd), "s"(soff) : "memory");
    if (__builtin_amdgcn_ballot_w64(f[0] != tlo || f[1] != thi) == 0) break;
    __builtin_amdgcn_s_sleep(SLEEP);
  }
}
template <int N> DI void poll2_issue(v4u (&f)[2], unsigned voff, srd_t srd, unsigned soff) {
  asm volatile("buffer_load_dwordx4 %0, %1, %2, %3 offen sc1" : "=v"(f[0]) : "v"(voff), "s"(srd), "s"(soff) : "memory");
  if constexpr (N == 2) asm volatile("buffer_load_dwordx4 %0, %1, %2, %3 offen offset:1024 sc1" : "=v"(f[1]) : "v"(voff), "s"(srd), "s"(soff) : "memory");
}
template <int N> DI bool poll2_check(v4u (&f)[2], bool use, unsigned tlo, unsigned thi) {
  if constexpr (N == 2) asm volatile("s_waitcnt vmcnt(0)" : "+v"(f[0]), "+v"(f[1])::"memory");
  else asm volatile("s_waitcnt vmcnt(0)" : "+v"(f[0])::"memory");
  const v4u a = f[0];
  bool bad = a[0] != tlo || a[1] != thi || a[2] != tlo || a[3] != thi;
  if constexpr (N == 2) { const v4u c = f[1]; bad = bad || c[0] != tlo || c[1] != thi || c[2] != tlo || c[3] != thi; }
  return __builtin_amdgcn_ballot_w64(bad && use) == 0;
}
template <int SLEEP, int N> DI void poll_flags2(unsigned voff, bool use, srd_t srd, unsigned soff, unsigned tlo, unsigned thi) {
#pragma unroll 1
  for (int spin = 0; spin < SPIN_MAX; ++spin) {
    v4u f[2];
    poll2_issue<N>(f, voff, srd, soff);
    if (poll2_check<N>(f, use, tlo, thi)) break;
    __builtin_amdgcn_s_sleep(SLEEP);
  }
}
DI void store16_sc1(v4u d, unsigned voff, srd_t srd, unsigned soff) {
  asm volatile("buffer_store_dwordx4 %0, %1, %2, %3 offen sc1\n\ts_nop 1" ::"v"(d), "v"(voff), "s"(srd), "s"(soff) : "memory");
}
DI void store8_sc1(v2u d, unsigned voff, srd_t srd, unsigned soff) {
  asm volatile("buffer_store_dwordx2 %0, %1, %2, %3 offen sc1\n\ts_nop 1" ::"v"(d), "v"(voff), "s"(srd), "s"(soff) : "memory");
}
DI float gelu(float u) {
  const float e = __builtin_amdgcn_exp2f(u * (-2.3022082f - 0.1029432f * u * u));
  return u * __builtin_amdgcn_rcpf(1.0f + e);
}
#define PA(i)
#define PB(i)
#define STAMP(i)
#define STAMPA(i)
#define STAMPB(i)
#define STAMPX0(i)
#define STAMPX1(i)

__global__ void __launch_bounds__(256, 1) __attribute__((amdgpu_num_vgpr(RB)))
mixer_kernel(const u16* __restrict__ x, const u16* __restrict__ w1, const u16* __restrict__ b1, const u16* __restrict__ w2, const u16* __restrict__ b2,
             u16* __restrict__ out, unsigned char* __restrict__ ws) {
  __shared__ __attribute__((aligned(1024))) unsigned char smem[LDS_BYTES];
  STAMP(0);
  asm volatile("" ::: ACLOB);
  asm volatile("" ::: "v160", "v255");
  const unsigned tid = threadIdx.x, lane = tid & 63, w = rfl(tid >> 6), b = blockIdx.x;
  const unsigned lds0 = (unsigned)(uintptr_t)(LAS unsigned char*)smem;
  const srd_t srdWS = mk_srd(ws, WS_USED);
  const unsigned g = lane >> 4, c = lane & 15;

  v2u ep; v4u xv[2];
  {
    const srd_t srdX = mk_srd(x, S_ * D_ * 2);
    const unsigned zero = 0, vx = lane < 56 ? lane * 16 : OOB, row = 32 * (b >> 2) + 4 * (b & 3) + 2 * (w & 1) + 16 * (w >> 1);
    asm volatile("buffer_load_dwordx4 %0, %1, %2, %3 offen" : "=v"(xv[0]) : "v"(vx), "s"(srdX), "s"(row * 896u) : "memory");
    asm volatile("buffer_load_dwordx4 %0, %1, %2, %3 offen offset:896" : "=v"(xv[1]) : "v"(vx), "s"(srdX), "s"(row * 896u) : "memory");
    asm volatile("buffer_load_dwordx2 %0, %1, %2, %3 offen sc1" : "=v"(ep) : "v"(zero), "s"(srdWS), "s"(EP_OFF) : "memory");
  }
  GAP();
  const unsigned xcd = b & 7, li = b >> 3;
  bool g1, g2;
  unsigned mb1, nb1, mb2, nb2;
  {
    const unsigned j = xcd >> 1, odd = xcd & 1, i0 = 7 + odd;
    g1 = li >= i0;
    const unsigned idx = g1 ? li - i0 + (odd ? 25 : 0) : 0;
    nb1 = 7 * j + idx / 7; mb1 = idx % 7;
    g2 = li < 28; nb2 = 4 * xcd + (li & 3); mb2 = g2 ? li >> 2 : 0;
  }
  const unsigned rot1 = ROT1 == 6 ? (nb1 % 7 != 3 ? (mb1 * 16) / 7 : mb1 < 4 ? 4 * mb1 : ((mb1 - 4) * 16) / 3) : ROT1 == 7 ? (nb1 % 7 != 3 ? (mb1 * 16) / 7 : mb1 < 4 ? 4 * mb1 + 1 : ((mb1 - 4) * 16) / 3 + 3) : ROT1 == 0 ? 0 : ROT1 == 1 ? (mb1 * 16) / 7 : ROT1 == 2 ? ((mb1 * 16) / 7 + 4 * (nb1 & 3)) & 15 : ROT1 == 3 ? (4 * (nb1 & 3)) & 15 : (((mb1 * ROTN1) / 7) * 16) / ROTN1;
  const unsigned rot2 = ROT2 == 0 ? 0 : ROT2 == 1 ? (mb2 * 8) % 14 : ROT2 == 2 ? (mb2 * 2 + ((nb2 & 3) * 7) / 2) % 14 : ROT2 == 3 ? ((nb2 & 3) * 7) / 2 : (((mb2 * ROTN2) / 7) * 14) / ROTN2;
  WStream W1, W2;
  const unsigned region = lds0 + w * REG_W;
  {
    const unsigned r8 = lane >> 3, pc = (lane & 7) ^ (r8 & 6);
    const unsigned q = (lane >> 2) & 3, p = lane & 3, row = 4 * g + q;
    const unsigned t0 = region + row * 128 + ((row >> 1) & 3) * 32 + p * 8;
    W1.srd = mk_srd(w1, S_ * F_ * 2);
    W2.srd = mk_srd(w2, g2 ? F_ * S_ * 2 : 0);
    W1.beg = w * (KW1 * 32) * F_ * 2 + nb1 * 128; W1.end = W1.beg + KW1 * 32 * F_ * 2; W1.cur = W1.beg + rot1 * (32 * F_ * 2);
    W2.beg = w * (KW2 * 32) * S_ * 2 + nb2 * 128; W2.end = W2.beg + KW2 * 32 * S_ * 2; W2.cur = W2.beg + rot2 * (32 * S_ * 2);
    W1.vo = r8 * F_ * 2 + pc * 16;
    W2.vo = r8 * S_ * 2 + pc * 16;
    W1.ring = W2.ring = region;
#pragma unroll
    for (int t = 0; t < 4; ++t) { W1.ta[t] = W2.ta[t] = t0 ^ (t << 5); }
  }
  constexpr int NT = 1;
  {
    const srd_t srdO = mk_srd(out, S_ * D_ * 2);
    asm volatile("buffer_load_dword v252, %0, %1, %2 offen" ::"v"(lane == 0 ? 0 : OOB), "s"(srdO), "s"((nb2 * 64 + 16 * w) * (D_ * 2) + mb2 * 128) : "memory");
  }
  if (g1) glds_slots<true, 0>(W1, make_iseq<PRE>{}); else waitv<0>();
  asm volatile("s_waitcnt vmcnt(%3)" : "+v"(ep), "+v"(xv[0]), "+v"(xv[1]) : "i"(4 * PRE + NT) : "memory");
  GAP();
  STAMP(1);
  if (g1) glds_slots<true, PRE>(W1, make_iseq<MID>{});
  GAP();
  unsigned tlo, thi;
  {
    const unsigned long long e = (((unsigned long long)ep[1] << 32) | ep[0]) + 1ull;
    tlo = rfl((unsigned)e); thi = rfl((unsigned)(e >> 32));
  }
  {
    float f[2][8], m[2], qs[2];
#pragma unroll
    for (int r = 0; r < 2; ++r) {
#pragma unroll
      for (int i = 0; i < 4; ++i) { f[r][2 * i] = bflo(xv[r][i]); f[r][2 * i + 1] = bfhi(xv[r][i]); }
      m[r] = ((f[r][0] + f[r][1]) + (f[r][2] + f[r][3])) + ((f[r][4] + f[r][5]) + (f[r][6] + f[r][7]));
    }
    wave_sum2(m[0], m[1]);
#pragma unroll
    for (int r = 0; r < 2; ++r) {
      const float mu = m[r] * (1.0f / D_);
#pragma unroll
      for (int i = 0; i < 8; ++i) f[r][i] -= mu;
      qs[r] = ((f[r][0] * f[r][0] + f[r][1] * f[r][1]) + (f[r][2] * f[r][2] + f[r][3] * f[r][3])) + ((f[r][4] * f[r][4] + f[r][5] * f[r][5]) + (f[r][6] * f[r][6] + f[r][7] * f[r][7]));
      qs[r] = lane < 56 ? qs[r] : 0.f;
    }
    wave_sum2(qs[0], qs[1]);
    v4u o[2];
#pragma unroll
    for (int r = 0; r < 2; ++r) {
      const float rs = __builtin_amdgcn_rsqf(qs[r] * (1.0f / D_) + 1e-5f);
      o[r] = v4u{pack2(f[r][0] * rs, f[r][1] * rs), pack2(f[r][2] * rs, f[r][3] * rs), pack2(f[r][4] * rs, f[r][5] * rs), pack2(f[r][6] * rs, f[r][7] * rs)};
    }
    if (lane < 56) {
      const unsigned ro = w * REG_W + LN_OFF + (w & 1) * 128 + lane * 16;
      *(LAS v4u*)(smem + ro) = o[0];
      *(LAS v4u*)(smem + ro + LNP) = o[1];
    }
  }
  wg_barrier();
  {
    v2u lo[2], hi[2];
    const unsigned q = (lane >> 2) & 3;
    const unsigned tlo_a = lds0 + (q >> 1) * (REG_W + 128) + LN_OFF + (q & 1) * LNP + g * 32 + (lane & 3) * 8 + w * 128, thi_a = tlo_a + 2 * REG_W;
    asm volatile("ds_read_b64_tr_b16 %0, %1 offset:0" : "=v"(lo[0]) : "v"(tlo_a) : "memory");
    asm volatile("ds_read_b64_tr_b16 %0, %1 offset:0" : "=v"(hi[0]) : "v"(thi_a) : "memory");
    asm volatile("ds_read_b64_tr_b16 %0, %1 offset:512" : "=v"(lo[1]) : "v"(tlo_a) : "memory");
    asm volatile("ds_read_b64_tr_b16 %0, %1 offset:512" : "=v"(hi[1]) : "v"(thi_a) : "memory");
    asm volatile("s_waitcnt lgkmcnt(0)" : "+v"(lo[0]), "+v"(lo[1]), "+v"(hi[0]), "+v"(hi[1])::"memory");
    const unsigned vo = g * HT + c * 16 + (b & 3) * 256;
    const unsigned so = H_OFF + (b >> 2) * KSTRIDE + w * 4 * HT;
    store16_sc1(v4u{lo[0][0], lo[0][1], hi[0][0], hi[0][1]}, vo, srdWS, so);
    store16_sc1(v4u{lo[1][0], lo[1][1], hi[1][0], hi[1][1]}, w < 3 ? vo : OOB, srdWS, so + 16 * HT);
    const unsigned vf = lane < R1 ? lane * 8192 + w * 2048 + b * 8 : OOB;
    if (!g1) {
      waitv<0>();
      store8_sc1(v2u{tlo, thi}, vf, srdWS, HF_OFF);
      STAMP(2);
    } else if (w == 0) {
      glds_slots<true, NB1>(W1, make_iseq<NA0>{});
      waitv<4 * NA0>();
      store8_sc1(v2u{tlo, thi}, vf, srdWS, HF_OFF);
      STAMP(2);
      glds_slots<true, NB1 + NA0>(W1, make_iseq<NS1 - NB1 - NA0>{});
      wpipe_stepsx<true, 0, 2, NA0>(W1, make_iseq<7>{});
    } else {
      glds_slots<true, NB1>(W1, make_iseq<NA>{});
      waitv<4 * NA>();
      store8_sc1(v2u{tlo, thi}, vf, srdWS, HF_OFF);
      STAMP(2);
      if (WSLEEP1) __builtin_amdgcn_s_sleep(WSLEEP1);
      glds_slots<true, NB1 + NA>(W1, make_iseq<NS1 - NB1 - NA>{});
      wpipe_stepsx<true, 0, 2, NA>(W1, make_iseq<7>{});
    }
  }
  Exch X;
  X.mine = (LAS unsigned char*)smem + w * REG_W + lane * 16;
#pragma unroll
  for (int j = 0; j < 3; ++j) X.from[j] = (const LAS unsigned char*)smem + ((w - 1 - j) & 3) * REG_W + j * 4096 + lane * 16;
  if (g1) {
    v2u bv[4];
    {
      const srd_t srdB1 = mk_srd(b1, F_ * 2);
      asm volatile("buffer_load_dwordx2 %0, %1, %2, %3 offen" : "=v"(bv[0]) : "v"(g * 8), "s"(srdB1), "s"(nb1 * 128u) : "memory");
      asm volatile("buffer_load_dwordx2 %0, %1, %2, %3 offen offset:32" : "=v"(bv[1]) : "v"(g * 8), "s"(srdB1), "s"(nb1 * 128u) : "memory");
      asm volatile("buffer_load_dwordx2 %0, %1, %2, %3 offen offset:64" : "=v"(bv[2]) : "v"(g * 8), "s"(srdB1), "s"(nb1 * 128u) : "memory");
      asm volatile("buffer_load_dwordx2 %0, %1, %2, %3 offen offset:96" : "=v"(bv[3]) : "v"(g * 8), "s"(srdB1), "s"(nb1 * 128u) : "memory");
    }
    GAP();
    if (w == 0) {
      const unsigned so = HF_OFF + (b % R1) * 8192 + (mb1 & 3) * 2048;
      v4u f[2];
      poll2_issue<2>(f, lane * 16, srdWS, so);
      wtail_steps<true, 7, 6, NA0>(W1, make_iseq<KW1 - 7>{});
      STAMPX0(3);
      if (!poll2_check<2>(f, true, tlo, thi)) poll_flags2<PSLEEP, 2>(lane * 16, true, srdWS, so, tlo, thi);
    }
    asm volatile("s_barrier" ::: "memory");
    STAMPA(4);
    AStream A;
    A.srd = srdWS; A.beg = H_OFF + 4 * mb1 * HT + w * KW1 * KSTRIDE; A.end = A.beg + KW1 * KSTRIDE; A.cur = A.beg + rot1 * KSTRIDE;
#pragma unroll
    for (int m = 0; m < 4; ++m) A.voff[m] = lane * 16 + ((m + w) & 3) * HT;
    gemm_prologue<true>(A, W2, make_iseq<DF1>{});
    if (w != 0) { wtail_steps<true, 7, 4 + DF1, NA>(W1, make_iseq<KW1 - 7>{}); STAMPX1(3); }
    GAP();
    asm volatile("s_waitcnt vmcnt(%4) lgkmcnt(0)" : "+v"(bv[0]), "+v"(bv[1]), "+v"(bv[2]), "+v"(bv[3]) : "i"(DF1) : "memory");
    f32x4 s[4];
#pragma unroll
    for (int t = 0; t < 4; ++t) { const v2u q = bv[t]; const unsigned q0 = q[0], q1 = q[1]; s[t] = f32x4{bflo(q0), bfhi(q0), bflo(q1), bfhi(q1)}; }
    GAP();
    gemm_body<true>(s, A, W2, X);
    STAMPA(5);
    if (!W2POS) glds_slots<false, TPF>(W2, make_iseq<NS2 - TPF>{});
    GAP();
#pragma unroll
    for (int kk = 0; kk < 2; ++kk) {
      const f32x4 s0 = s[2 * kk], s1 = s[2 * kk + 1];
      const v4u o = {pack2(gelu(s0[0]), gelu(s0[1])), pack2(gelu(s0[2]), gelu(s0[3])), pack2(gelu(s1[0]), gelu(s1[1])), pack2(gelu(s1[2]), gelu(s1[3]))};
      store16_sc1(o, lane * 16, srdWS, U_OFF + (4 * mb1 + w) * UT + (2 * nb1 + kk) * KSTRIDE);
    }
    GAP();
    if (W2POS) glds_slots<false, TPF>(W2, make_iseq<NS2 - TPF>{});
    wpipe_steps<false, 0, 1>(W2, make_iseq<POST2>{});
    waitv<4 * POST2 + (W2POS ? 4 * (NS2 - TPF) : 0)>();
    store8_sc1(v2u{tlo, thi}, lane < R2 ? lane * UF_REP + (mb1 * 4 + nb1 / 7) * 256 + ((nb1 % 7) * 4 + w) * 8 : OOB, srdWS, UF_OFF);
    STAMP(6);
    if (!g2) {
      if (b == EPOCH_WG && w == 1) store8_sc1(v2u{tlo, thi}, lane == 0 ? 0 : OOB, srdWS, EP_OFF);
      waitv<0>();
      goto done;
    }
    if (w != 0) __builtin_amdgcn_s_sleep(WSLEEP2);
    wpipe_steps<false, POST2, 2>(W2, make_iseq<NS2 - POST2>{});
  } else {
    wg_barrier();
    if (!g2) goto done;
#pragma unroll 1
    for (int i = 0; i < IDLE_SLEEP; ++i) __builtin_amdgcn_s_sleep(16);
    glds_slots<false, 0>(W2, make_iseq<NS2>{});
    wpipe_steps<false, 0, 0>(W2, make_iseq<KW2>{});
    if (w == 0) poll_flags<8>(0, srdWS, EP_OFF, tlo, thi);
    wg_barrier();
  }
  {
    unsigned bv[4];
    {
      const srd_t srdB2 = mk_srd(b2, S_ * 2);
      asm volatile("buffer_load_ushort %0, %1, %2, %3 offen" : "=v"(bv[0]) : "v"(c * 2), "s"(srdB2), "s"(nb2 * 128u) : "memory");
      asm volatile("buffer_load_ushort %0, %1, %2, %3 offen offset:32" : "=v"(bv[1]) : "v"(c * 2), "s"(srdB2), "s"(nb2 * 128u) : "memory");
      asm volatile("buffer_load_ushort %0, %1, %2, %3 offen offset:64" : "=v"(bv[2]) : "v"(c * 2), "s"(srdB2), "s"(nb2 * 128u) : "memory");
      asm volatile("buffer_load_ushort %0, %1, %2, %3 offen offset:96" : "=v"(bv[3]) : "v"(c * 2), "s"(srdB2), "s"(nb2 * 128u) : "memory");
    }
    GAP();
    if (w == 0) {
      const unsigned so = UF_OFF + (nb2 % R2) * UF_REP + mb2 * 1024;
      v4u f[2];
      poll2_issue<1>(f, lane * 16, srdWS, so);
      if (g1) wtail_steps<false, NS2, 5, NA>(W2, make_iseq<KW2 - NS2>{});
      if (!poll2_check<1>(f, c < 14, tlo, thi)) poll_flags2<PSLEEP, 1>(lane * 16, c < 14, srdWS, so, tlo, thi);
    }
    asm volatile("s_barrier" ::: "memory");
    STAMPB(4);
    AStream A;
    A.srd = srdWS; A.beg = U_OFF + 4 * mb2 * UT + w * KW2 * KSTRIDE; A.end = A.beg + KW2 * KSTRIDE; A.cur = A.beg + rot2 * KSTRIDE;
#pragma unroll
    for (int m = 0; m < 4; ++m) A.voff[m] = lane * 16 + ((m + w) & 3) * UT;
    gemm_prologue<false>(A, W2, make_iseq<DF2>{});
    if (w != 0 && g1) wtail_steps<false, NS2, 4 + DF2, NA>(W2, make_iseq<KW2 - NS2>{});
    STAMPB(3);
    GAP();
    asm volatile("s_waitcnt vmcnt(%4) lgkmcnt(0)" : "+v"(bv[0]), "+v"(bv[1]), "+v"(bv[2]), "+v"(bv[3]) : "i"(DF2) : "memory");
    f32x4 s[4];
#pragma unroll
    for (int t = 0; t < 4; ++t) { const float bb = bflo(bv[t]); s[t] = f32x4{bb, bb, bb, bb}; }
    GAP();
    gemm_body<false>(s, A, W2, X);
    STAMPB(5);
    unsigned char* stage = smem + W2_OFF;
#pragma unroll
    for (int t = 0; t < 4; ++t) *(LAS v2u*)((LAS unsigned char*)stage + (16 * t + c) * 144 + 32 * w + 8 * g) = v2u{pack2(s[t][0], s[t][1]), pack2(s[t][2], s[t][3])};
    wg_barrier();
    const srd_t srdO = mk_srd(out, S_ * D_ * 2);
    const unsigned row = 16 * w + (lane >> 3), col = (lane & 7) * 16;
#pragma unroll
    for (int mp = 0; mp < 2; ++mp) {
      const v4u o = *(const LAS v4u*)((LAS unsigned char*)stage + (row + 8 * mp) * 144 + col);
      asm volatile("buffer_store_dwordx4 %0, %1, %2, %3 offen\n\ts_nop 1" ::"v"(o), "v"((row + 8 * mp) * (D_ * 2) + col), "s"(srdO), "s"(nb2 * 64 * (D_ * 2) + mb2 * 128) : "memory");
    }
  }
done:
  STAMP(7);
}

extern "C" void kernel_launch(void* const* d_in, const int* in_sizes, int n_in, void* d_out, int out_size, void* d_ws, size_t ws_size, hipStream_t stream) {
  mixer_kernel<<<256, 256, 0, stream>>>((const u16*)d_in[0], (const u16*)d_in[1], (const u16*)d_in[2], (const u16*)d_in[3], (const u16*)d_in[4],
                                       (u16*)d_out, (unsigned char*)d_ws);
}
```

```cpp
#include <hip/hip_runtime.h>
#include <stdint.h>
#include <utility>

typedef unsigned v4u __attribute__((ext_vector_type(4)));
typedef unsigned v2u __attribute__((ext_vector_type(2)));
typedef float f32x4 __attribute__((ext_vector_type(4)));
typedef unsigned short u16;
typedef __amdgpu_buffer_rsrc_t srd_t;
#define DI __device__ __forceinline__
#define LAS __attribute__((address_space(3)))
#define GAP() __builtin_amdgcn_sched_barrier(0)
template <int... Is> using iseq = std::integer_sequence<int, Is...>;
template <int N> using make_iseq = std::make_integer_sequence<int, N>;
#define ACLOB "a0","a1","a2","a3","a4","a5","a6","a7","a8","a9","a10","a11","a12","a13","a14","a15","a16","a17","a18","a19","a20","a21","a22","a23","a24","a25","a26","a27","a28","a29","a30","a31","a32","a33","a34","a35","a36","a37","a38","a39","a40","a41","a42","a43","a44","a45","a46","a47","a48","a49","a50","a51","a52","a53","a54","a55","a56","a57","a58","a59","a60","a61","a62","a63","a64","a65","a66","a67","a68","a69","a70","a71","a72","a73","a74","a75","a76","a77","a78","a79","a80","a81","a82","a83","a84","a85","a86","a87","a88","a89","a90","a91","a92","a93","a94","a95","a96","a97","a98","a99","a100","a101","a102","a103","a104","a105","a106","a107","a108","a109","a110","a111","a112","a113","a114","a115","a116","a117","a118","a119","a120","a121","a122","a123","a124","a125","a126","a127","a128","a129","a130","a131","a132","a133","a134","a135","a136","a137","a138","a139","a140","a141","a142","a143","a144","a145","a146","a147","a148","a149","a150","a151","a152","a153","a154","a155","a156","a157","a158","a159","a160","a161","a162","a163","a164","a165","a166","a167","a168","a169","a170","a171","a172","a173","a174","a175","a176","a177","a178","a179","a180","a181","a182","a183","a184","a185","a186","a187","a188","a189","a190","a191","a192","a193","a194","a195","a196","a197","a198","a199","a200","a201","a202","a203","a204","a205","a206","a207","a208","a209","a210","a211","a212","a213","a214","a215","a216","a217","a218","a219","a220","a221","a222","a223","a224","a225","a226","a227","a228","a229","a230","a231","a232","a233","a234","a235","a236","a237","a238","a239","a240","a241","a242","a243","a244","a245","a246","a247","a248","a249","a250","a251","a252","a253","a254","a255"

#define TOUCH 5
#define LT 15
#define MAPPING 3
#define DF1 16
#define DF2 16
#define HB 5
#define W2POS 1
#define PRE 3
#define MID 0
#define NA 3
#define NA0 3
#define EARLYPOLL 1
#define POST2 0
#define ROT1 6
#define ROT2 1
#define ROTN1 2
#define ROTN2 2
#define IDLE_SLEEP 0
#define PSLEEP 2
#define QUIET 1
#define WSLEEP1 16
#define WSLEEP2 16
#define R1 4
#define R2 4

constexpr int S_ = 2048, D_ = 448, F_ = 1792;
constexpr int KW1 = 16, KW2 = 14;
constexpr int NS1 = 9, NS2 = 7;
#define TPFMAX 6
constexpr int TPF = DF1 / 4 < TPFMAX ? DF1 / 4 : TPFMAX;
constexpr unsigned KSTRIDE = 1024u;
constexpr unsigned HT = 64u * KSTRIDE, UT = 56u * KSTRIDE;
constexpr unsigned H_OFF = 0, U_OFF = 28u * HT;
constexpr unsigned EP_OFF = 4u << 20, HF_OFF = EP_OFF + 4096u, UF_OFF = HF_OFF + 16u * 8192u;
constexpr unsigned UF_REP = 28u * 256u;
constexpr unsigned WS_USED = UF_OFF + 16u * UF_REP;
constexpr unsigned REG_W = 40960, LN_OFF = 36864, LNP = 960, W2_OFF = 12288, LDS_BYTES = 4 * REG_W;
constexpr unsigned OOB = 0x7ffffff0u;
constexpr int SPIN_MAX = 1 << 15;
constexpr unsigned EPOCH_WG = 225;

DI unsigned rfl(unsigned x) { return __builtin_amdgcn_readfirstlane(x); }
DI float bflo(unsigned w) { return __builtin_bit_cast(float, w << 16); }
DI float bfhi(unsigned w) { return __builtin_bit_cast(float, w & 0xffff0000u); }
DI unsigned pack2(float lo, float hi) {
  typedef __bf16 bf2 __attribute__((ext_vector_type(2)));
  typedef float f2 __attribute__((ext_vector_type(2)));
  f2 v = {lo, hi};
  return __builtin_bit_cast(unsigned, __builtin_convertvector(v, bf2));
}
DI srd_t mk_srd(const void* p, unsigned bytes) { return __builtin_amdgcn_make_buffer_rsrc((void*)p, 0, bytes, 0x00020000); }
template <int C> DI float dpp_add(float v) {
  return v + __builtin_bit_cast(float, __builtin_amdgcn_update_dpp(0, __builtin_bit_cast(int, v), C, 0xf, 0xf, true));
}
DI void wave_sum2(float& a, float& b) {
  a = dpp_add<0xB1>(a); b = dpp_add<0xB1>(b); a = dpp_add<0x4E>(a); b = dpp_add<0x4E>(b);
  a = dpp_add<0x141>(a); b = dpp_add<0x141>(b); a = dpp_add<0x140>(a); b = dpp_add<0x140>(b);
  auto p = __builtin_amdgcn_permlane16_swap(__builtin_bit_cast(unsigned, a), __builtin_bit_cast(unsigned, b), false, false);
  const unsigned p0 = p[0], p1 = p[1];
  float s = __builtin_bit_cast(float, p0) + __builtin_bit_cast(float, p1);
  auto q = __builtin_amdgcn_permlane32_swap(__builtin_bit_cast(unsigned, s), __builtin_bit_cast(unsigned, s), false, false);
  const unsigned q0 = q[0], q1 = q[1];
  s = __builtin_bit_cast(float, q0) + __builtin_bit_cast(float, q1);
  a = __builtin_bit_cast(float, __builtin_amdgcn_readlane(__builtin_bit_cast(int, s), 0));
  b = __builtin_bit_cast(float, __builtin_amdgcn_readlane(__builtin_bit_cast(int, s), 16));
}
template <int N> DI void waitv() { static_assert(N >= 0 && N < 64, "vmcnt"); asm volatile("s_waitcnt vmcnt(%0)" ::"i"(N) : "memory"); }
DI void wait_lds() { asm volatile("s_waitcnt lgkmcnt(0)" ::: "memory"); }
DI void wg_barrier() { asm volatile("s_waitcnt lgkmcnt(0)\n\ts_barrier" ::: "memory"); }

struct WStream {
  srd_t srd;
  unsigned cur, beg, end;
  unsigned vo;
  unsigned ring;
  unsigned ta[4];
};
DI void cyc_add(unsigned& cur, unsigned inc, unsigned beg, unsigned end) {
  asm volatile("s_add_u32 %0, %0, %1\n\ts_cmp_eq_u32 %0, %3\n\ts_cselect_b32 %0, %2, %0" : "+s"(cur) : "s"(inc), "s"(beg), "s"(end) : "scc");
}
template <bool W1> struct WP { static constexpr int LDW = W1 ? F_ : S_, KW = W1 ? KW1 : KW2, NS = W1 ? NS1 : NS2, OFF = W1 ? 0 : (int)W2_OFF; };
template <bool W1, int KSTEP, int I> DI void glds_piece(WStream& s) {
  using P = WP<W1>;
  unsigned t;
  asm volatile("s_add_u32 m0, %1, %2\n\ts_add_u32 %0, %3, %4\n\tbuffer_load_dwordx4 %5, %6, %0 offen lds"
               : "=&s"(t)
               : "s"(s.ring), "i"(P::OFF + (KSTEP % P::NS) * 4096 + I * 1024), "s"(s.cur), "i"(I * 8 * P::LDW * 2), "v"(s.vo), "s"(s.srd)
               : "scc", "memory");
  if constexpr (I == 3) cyc_add(s.cur, 32 * P::LDW * 2, s.beg, s.end);
}
template <bool W1, int KSTEP> DI void glds_slot(WStream& s) {
  glds_piece<W1, KSTEP, 0>(s); glds_piece<W1, KSTEP, 1>(s); glds_piece<W1, KSTEP, 2>(s); glds_piece<W1, KSTEP, 3>(s);
}
template <bool W1, int KSTEP, int T, int H> DI void tr_piece(const WStream& s) {
  using P = WP<W1>;
  asm volatile("ds_read_b64_tr_b16 a[%c1:%c2], %0 offset:%c3" ::"v"(s.ta[T]), "i"(16 * KSTEP + 4 * T + 2 * H), "i"(16 * KSTEP + 4 * T + 2 * H + 1),
               "i"(P::OFF + (KSTEP % P::NS) * 4096 + H * 2048) : "memory", ACLOB);
}
template <bool W1, int KSTEP> DI void tr_slot(const WStream& s) {
  tr_piece<W1, KSTEP, 0, 0>(s); tr_piece<W1, KSTEP, 0, 1>(s); tr_piece<W1, KSTEP, 1, 0>(s); tr_piece<W1, KSTEP, 1, 1>(s);
  tr_piece<W1, KSTEP, 2, 0>(s); tr_piece<W1, KSTEP, 2, 1>(s); tr_piece<W1, KSTEP, 3, 0>(s); tr_piece<W1, KSTEP, 3, 1>(s);
}
constexpr int NB1 = PRE + MID;
static_assert(NB1 + NA <= NS1 && NB1 + NA0 <= NS1 && POST2 <= NS2, "schedule");
constexpr int NT2 = (LT & 1) + ((LT >> 1) & 1) + ((LT >> 2) & 1) + ((LT >> 3) & 1);
template <bool W1, int NAX> constexpr int younger(int j, bool flagged) {
  constexpr int KW = W1 ? KW1 : KW2, NS = W1 ? NS1 : NS2;
  const int before_stores = W1 ? NB1 : (W2POS ? TPF : NS2), before_flag = W1 ? NB1 + NAX % 100 : NS2 + POST2;
  const int last = j + NS - 1 < KW - 1 ? j + NS - 1 : KW - 1;
  return 4 * (last - j) + (j < before_stores ? 2 : 0) + (flagged && j < before_flag ? 1 + NAX / 100 : 0);
}
template <bool W1, int J, int MODE, int EXTRA, int NAX> DI void wpipe_step(WStream& s) {
  using P = WP<W1>;
  constexpr int rem = P::KW - 1 - J;
  waitv<(MODE == 0 ? 4 * (rem < P::NS - 1 ? rem : P::NS - 1) : younger<W1, NAX>(J, MODE == 2)) + EXTRA>();
  tr_slot<W1, J>(s);
  if constexpr (J + P::NS < P::KW) { wait_lds(); glds_slot<W1, J + P::NS>(s); }
  GAP();
}
template <bool W1, int J0, int MODE, int NAX, int... Js> DI void wpipe_stepsx(WStream& s, iseq<Js...>) { (wpipe_step<W1, J0 + Js, MODE, 0, NAX>(s), ...); }
template <bool W1, int J0, int MODE, int... Js> DI void wpipe_steps(WStream& s, iseq<Js...>) { (wpipe_step<W1, J0 + Js, MODE, 0, NA>(s), ...); }
template <bool W1, int J0, int EXTRA, int NAX, int... Js> DI void wtail_steps(WStream& s, iseq<Js...>) { (wpipe_step<W1, J0 + Js, 2, EXTRA, NAX>(s), ...); }
template <bool W1, int J0, int... Js> DI void glds_slots(WStream& s, iseq<Js...>) { (glds_slot<W1, J0 + Js>(s), ...); }

constexpr int RB = 160;
static_assert(RB + 4 * DF1 <= 256 && RB + 4 * DF2 <= 256, "ring");
template <int REG> DI void act_load(unsigned voff, srd_t srd, unsigned soff) {
  asm volatile("buffer_load_dwordx4 v[%c0:%c1], %2, %3, %4 offen sc1" ::"i"(REG), "i"(REG + 3), "v"(voff), "s"(srd), "s"(soff) : "memory");
}
struct AStream { srd_t srd; unsigned voff[4], cur, beg, end; };
template <bool WA, bool ZERO, int AR, int VR> DI void mfma(f32x4& acc) {
  if constexpr (WA) {
    if constexpr (ZERO) asm volatile("v_mfma_f32_16x16x32_bf16 %0, a[%c1:%c2], v[%c3:%c4], 0" : "=&v"(acc) : "i"(AR), "i"(AR + 3), "i"(VR), "i"(VR + 3) : ACLOB);
    else asm volatile("v_mfma_f32_16x16x32_bf16 %0, a[%c1:%c2], v[%c3:%c4], %0" : "+v"(acc) : "i"(AR), "i"(AR + 3), "i"(VR), "i"(VR + 3) : ACLOB);
  } else {
    if constexpr (ZERO) asm volatile("v_mfma_f32_16x16x32_bf16 %0, v[%c3:%c4], a[%c1:%c2], 0" : "=&v"(acc) : "i"(AR), "i"(AR + 3), "i"(VR), "i"(VR + 3) : ACLOB);
    else asm volatile("v_mfma_f32_16x16x32_bf16 %0, v[%c3:%c4], a[%c1:%c2], %0" : "+v"(acc) : "i"(AR), "i"(AR + 3), "i"(VR), "i"(VR + 3) : ACLOB);
  }
}
template <bool G1> constexpr int vop(int v) { return v < 4 * (G1 ? KW1 : KW2) ? 1 : (G1 && v - 4 * KW1 < 4 * TPF ? 1 : 0); }
template <bool G1> constexpr int vop_range(int a, int b) { int n = 0; for (int v = a; v <= b; ++v) n += vop<G1>(v); return n; }
template <bool G1, int V> DI void stream_op(AStream& a, WStream& w2) {
  constexpr int KW = G1 ? KW1 : KW2, DF = G1 ? DF1 : DF2;
  if constexpr (V < 4 * KW) { act_load<RB + 4 * (V % DF)>(a.voff[(V / KW + 1) & 3], a.srd, a.cur); cyc_add(a.cur, KSTRIDE, a.beg, a.end); }
  else if constexpr (vop<G1>(V) != 0) glds_piece<false, (V - 4 * KW) / 4, (V - 4 * KW) % 4>(w2);
}
struct Exch { LAS unsigned char* mine; const LAS unsigned char* from[3]; };
template <bool G1, int I> DI void gemm_frag(f32x4 (&acc)[4][4], f32x4 (&pre)[4], f32x4 (&r)[3][4], AStream& a, WStream& w2, const Exch& x) {
  constexpr int KW = G1 ? KW1 : KW2, DF = G1 ? DF1 : DF2, K = I % KW, M = (I / KW + 1) & 3, VR = RB + 4 * (I % DF);
  waitv<I == 0 ? vop_range<G1>(1, DF - 1) : vop_range<G1>(I + 1, I - 2 + DF)>();
  mfma<G1, K == 0, 16 * K + 0, VR>(acc[M][0]);
  if constexpr (I >= 1) stream_op<G1, I - 1 + DF>(a, w2);
  mfma<G1, K == 0, 16 * K + 4, VR>(acc[M][1]);
  mfma<G1, K == 0, 16 * K + 8, VR>(acc[M][2]);
  mfma<G1, K == 0, 16 * K + 12, VR>(acc[M][3]);
  if constexpr (I == 4 * KW - 1) stream_op<G1, I + DF>(a, w2);
  if constexpr (I % KW == 1 && I > KW) {
    constexpr int m = I / KW;
    asm volatile("" : "+v"(acc[m][0]), "+v"(acc[m][1]), "+v"(acc[m][2]), "+v"(acc[m][3]));
#pragma unroll
    for (int t = 0; t < 4; ++t) *(LAS f32x4*)(x.mine + (m - 1) * 4096 + t * 1024) = acc[m][t];
  }
  if constexpr (I == 3 * KW + HB) {
    wg_barrier();
#pragma unroll
    for (int j = 0; j < 3; ++j)
#pragma unroll
      for (int t = 0; t < 4; ++t) r[j][t] = *(const LAS f32x4*)(x.from[j] + t * 1024);
  }
  if constexpr (I == 3 * KW + HB + 2) {
#pragma unroll
    for (int t = 0; t < 4; ++t) pre[t] += r[0][t];
  }
  if constexpr (I == 3 * KW + HB + 3) {
#pragma unroll
    for (int t = 0; t < 4; ++t) pre[t] += r[1][t];
  }
  if constexpr (I == 3 * KW + HB + 4) {
#pragma unroll
    for (int t = 0; t < 4; ++t) pre[t] += r[2][t];
  }
  GAP();
}
template <bool G1, int... Is> DI void gemm_frags(f32x4 (&acc)[4][4], f32x4 (&pre)[4], f32x4 (&r)[3][4], AStream& a, WStream& w2, const Exch& x, iseq<Is...>) {
  (gemm_frag<G1, Is>(acc, pre, r, a, w2, x), ...);
}
template <bool G1, int... Vs> DI void gemm_prologue(AStream& a, WStream& w2, iseq<Vs...>) { (stream_op<G1, Vs>(a, w2), ...); GAP(); }
template <bool G1> DI void gemm_body(f32x4 (&pre)[4], AStream& a, WStream& w2, const Exch& x) {
  f32x4 acc[4][4], r[3][4];
  gemm_frags<G1>(acc, pre, r, a, w2, x, make_iseq<4 * (G1 ? KW1 : KW2)>{});
  asm volatile("s_nop 7" : "+v"(acc[0][0]), "+v"(acc[0][1]), "+v"(acc[0][2]), "+v"(acc[0][3]));
#pragma unroll
  for (int t = 0; t < 4; ++t) pre[t] += acc[0][t];
}
template <int SLEEP> DI void poll_flags(unsigned voff, srd_t srd, unsigned soff, unsigned tlo, unsigned thi) {
#pragma unroll 1
  for (int spin = 0; spin < SPIN_MAX; ++spin) {
    v2u f;
    asm volatile("buffer_load_dwordx2 %0, %1, %2, %3 offen sc1\n\ts_waitcnt vmcnt(0)" : "=v"(f) : "v"(voff), "s"(srd), "s"(soff) : "memory");
    if (__builtin_amdgcn_ballot_w64(f[0] != tlo || f[1] != thi) == 0) break;
    __builtin_amdgcn_s_sleep(SLEEP);
  }
}
template <int N> DI void poll2_issue(v4u (&f)[2], unsigned voff, srd_t srd, unsigned soff) {
  asm volatile("buffer_load_dwordx4 %0, %1, %2, %3 offen sc1" : "=v"(f[0]) : "v"(voff), "s"(srd), "s"(soff) : "memory");
  if constexpr (N == 2) asm volatile("buffer_load_dwordx4 %0, %1, %2, %3 offen offset:1024 sc1" : "=v"(f[1]) : "v"(voff), "s"(srd), "s"(soff) : "memory");
}
template <int N> DI bool poll2_check(v4u (&f)[2], bool use, unsigned tlo, unsigned thi) {
  if constexpr (N == 2) asm volatile("s_waitcnt vmcnt(0)" : "+v"(f[0]), "+v"(f[1])::"memory");
  else asm volatile("s_waitcnt vmcnt(0)" : "+v"(f[0])::"memory");
  const v4u a = f[0];
  bool bad = a[0] != tlo || a[1] != thi || a[2] != tlo || a[3] != thi;
  if constexpr (N == 2) { const v4u c = f[1]; bad = bad || c[0] != tlo || c[1] != thi || c[2] != tlo || c[3] != thi; }
  return __builtin_amdgcn_ballot_w64(bad && use) == 0;
}
template <int SLEEP, int N> DI void poll_flags2(unsigned voff, bool use, srd_t srd, unsigned soff, unsigned tlo, unsigned thi) {
#pragma unroll 1
  for (int spin = 0; spin < SPIN_MAX; ++spin) {
    v4u f[2];
    poll2_issue<N>(f, voff, srd, soff);
    if (poll2_check<N>(f, use, tlo, thi)) break;
    __builtin_amdgcn_s_sleep(SLEEP);
  }
}
DI void store16_sc1(v4u d, unsigned voff, srd_t srd, unsigned soff) {
  asm volatile("buffer_store_dwordx4 %0, %1, %2, %3 offen sc1\n\ts_nop 1" ::"v"(d), "v"(voff), "s"(srd), "s"(soff) : "memory");
}
DI void store8_sc1(v2u d, unsigned voff, srd_t srd, unsigned soff) {
  asm volatile("buffer_store_dwordx2 %0, %1, %2, %3 offen sc1\n\ts_nop 1" ::"v"(d), "v"(voff), "s"(srd), "s"(soff) : "memory");
}
DI float gelu(float u) {
  const float e = __builtin_amdgcn_exp2f(u * (-2.3022082f - 0.1029432f * u * u));
  return u * __builtin_amdgcn_rcpf(1.0f + e);
}
#define PA(i)
#define PB(i)
#define STAMP(i)
#define STAMPA(i)
#define STAMPB(i)
#define STAMPX0(i)
#define STAMPX1(i)

__global__ void __launch_bounds__(256, 1) __attribute__((amdgpu_num_vgpr(RB)))
mixer_kernel(const u16* __restrict__ x, const u16* __restrict__ w1, const u16* __restrict__ b1, const u16* __restrict__ w2, const u16* __restrict__ b2,
             u16* __restrict__ out, unsigned char* __restrict__ ws) {
  __shared__ __attribute__((aligned(1024))) unsigned char smem[LDS_BYTES];
  STAMP(0);
  asm volatile("" ::: ACLOB);
  asm volatile("" ::: "v160", "v255");
  const unsigned tid = threadIdx.x, lane = tid & 63, w = rfl(tid >> 6), b = blockIdx.x;
  const unsigned lds0 = (unsigned)(uintptr_t)(LAS unsigned char*)smem;
  const srd_t srdWS = mk_srd(ws, WS_USED);
  const unsigned g = lane >> 4, c = lane & 15;

  v2u ep; v4u xv[2];
  {
    const srd_t srdX = mk_srd(x, S_ * D_ * 2);
    const unsigned zero = 0, vx = lane < 56 ? lane * 16 : OOB, row = 32 * (b >> 2) + 4 * (b & 3) + 2 * (w & 1) + 16 * (w >> 1);
    asm volatile("buffer_load_dwordx4 %0, %1, %2, %3 offen" : "=v"(xv[0]) : "v"(vx), "s"(srdX), "s"(row * 896u) : "memory");
    asm volatile("buffer_load_dwordx4 %0, %1, %2, %3 offen offset:896" : "=v"(xv[1]) : "v"(vx), "s"(srdX), "s"(row * 896u) : "memory");
    asm volatile("buffer_load_dwordx2 %0, %1, %2, %3 offen sc1" : "=v"(ep) : "v"(zero), "s"(srdWS), "s"(EP_OFF) : "memory");
  }
  GAP();
  const unsigned xcd = b & 7, li = b >> 3;
  bool g1, g2;
  unsigned mb1, nb1, mb2, nb2;
  {
    const unsigned j = xcd >> 1, odd = xcd & 1, i0 = 7 + odd;
    g1 = li >= i0;
    const unsigned idx = g1 ? li - i0 + (odd ? 25 : 0) : 0;
    nb1 = 7 * j + idx / 7; mb1 = idx % 7;
    g2 = li < 28; nb2 = 4 * xcd + (li & 3); mb2 = g2 ? li >> 2 : 0;
  }
  const unsigned rot1 = ROT1 == 6 ? (nb1 % 7 != 3 ? (mb1 * 16) / 7 : mb1 < 4 ? 4 * mb1 : ((mb1 - 4) * 16) / 3) : ROT1 == 7 ? (nb1 % 7 != 3 ? (mb1 * 16) / 7 : mb1 < 4 ? 4 * mb1 + 1 : ((mb1 - 4) * 16) / 3 + 3) : ROT1 == 0 ? 0 : ROT1 == 1 ? (mb1 * 16) / 7 : ROT1 == 2 ? ((mb1 * 16) / 7 + 4 * (nb1 & 3)) & 15 : ROT1 == 3 ? (4 * (nb1 & 3)) & 15 : (((mb1 * ROTN1) / 7) * 16) / ROTN1;
  const unsigned rot2 = ROT2 == 0 ? 0 : ROT2 == 1 ? (mb2 * 8) % 14 : ROT2 == 2 ? (mb2 * 2 + ((nb2 & 3) * 7) / 2) % 14 : ROT2 == 3 ? ((nb2 & 3) * 7) / 2 : (((mb2 * ROTN2) / 7) * 14) / ROTN2;
  WStream W1, W2;
  const unsigned region = lds0 + w * REG_W;
  {
    const unsigned r8 = lane >> 3, pc = (lane & 7) ^ (r8 & 6);
    const unsigned q = (lane >> 2) & 3, p = lane & 3, row = 4 * g + q;
    const unsigned t0 = region + row * 128 + ((row >> 1) & 3) * 32 + p * 8;
    W1.srd = mk_srd(w1, S_ * F_ * 2);
    W2.srd = mk_srd(w2, g2 ? F_ * S_ * 2 : 0);
    W1.beg = w * (KW1 * 32) * F_ * 2 + nb1 * 128; W1.end = W1.beg + KW1 * 32 * F_ * 2; W1.cur = W1.beg + rot1 * (32 * F_ * 2);
    W2.beg = w * (KW2 * 32) * S_ * 2 + nb2 * 128; W2.end = W2.beg + KW2 * 32 * S_ * 2; W2.cur = W2.beg + rot2 * (32 * S_ * 2);
    W1.vo = r8 * F_ * 2 + pc * 16;
    W2.vo = r8 * S_ * 2 + pc * 16;
    W1.ring = W2.ring = region;
#pragma unroll
    for (int t = 0; t < 4; ++t) { W1.ta[t] = W2.ta[t] = t0 ^ (t << 5); }
  }
  constexpr int NT = 1;
  {
    const srd_t srdO = mk_srd(out, S_ * D_ * 2);
    asm volatile("buffer_load_dword v252, %0, %1, %2 offen" ::"v"(lane == 0 ? 0 : OOB), "s"(srdO), "s"((nb2 * 64 + 16 * w) * (D_ * 2) + mb2 * 128) : "memory");
  }
  if (g1) glds_slots<true, 0>(W1, make_iseq<PRE>{}); else waitv<0>();
  asm volatile("s_waitcnt vmcnt(%3)" : "+v"(ep), "+v"(xv[0]), "+v"(xv[1]) : "i"(4 * PRE + NT) : "memory");
  GAP();
  STAMP(1);
  if (g1) glds_slots<true, PRE>(W1, make_iseq<MID>{});
  GAP();
  unsigned tlo, thi;
  {
    const unsigned long long e = (((unsigned long long)ep[1] << 32) | ep[0]) + 1ull;
    tlo = rfl((unsigned)e); thi = rfl((unsigned)(e >> 32));
  }
  {
    float f[2][8], m[2], qs[2];
#pragma unroll
    for (int r = 0; r < 2; ++r) {
#pragma unroll
      for (int i = 0; i < 4; ++i) { f[r][2 * i] = bflo(xv[r][i]); f[r][2 * i + 1] = bfhi(xv[r][i]); }
      m[r] = ((f[r][0] + f[r][1]) + (f[r][2] + f[r][3])) + ((f[r][4] + f[r][5]) + (f[r][6] + f[r][7]));
    }
    wave_sum2(m[0], m[1]);
#pragma unroll
    for (int r = 0; r < 2; ++r) {
      const float mu = m[r] * (1.0f / D_);
#pragma unroll
      for (int i = 0; i < 8; ++i) f[r][i] -= mu;
      qs[r] = ((f[r][0] * f[r][0] + f[r][1] * f[r][1]) + (f[r][2] * f[r][2] + f[r][3] * f[r][3])) + ((f[r][4] * f[r][4] + f[r][5] * f[r][5]) + (f[r][6] * f[r][6] + f[r][7] * f[r][7]));
      qs[r] = lane < 56 ? qs[r] : 0.f;
    }
    wave_sum2(qs[0], qs[1]);
    v4u o[2];
#pragma unroll
    for (int r = 0; r < 2; ++r) {
      const float rs = __builtin_amdgcn_rsqf(qs[r] * (1.0f / D_) + 1e-5f);
      o[r] = v4u{pack2(f[r][0] * rs, f[r][1] * rs), pack2(f[r][2] * rs, f[r][3] * rs), pack2(f[r][4] * rs, f[r][5] * rs), pack2(f[r][6] * rs, f[r][7] * rs)};
    }
    if (lane < 56) {
      const unsigned ro = w * REG_W + LN_OFF + (w & 1) * 128 + lane * 16;
      *(LAS v4u*)(smem + ro) = o[0];
      *(LAS v4u*)(smem + ro + LNP) = o[1];
    }
  }
  wg_barrier();
  {
    v2u lo[2], hi[2];
    const unsigned q = (lane >> 2) & 3;
    const unsigned tlo_a = lds0 + (q >> 1) * (REG_W + 128) + LN_OFF + (q & 1) * LNP + g * 32 + (lane & 3) * 8 + w * 128, thi_a = tlo_a + 2 * REG_W;
    asm volatile("ds_read_b64_tr_b16 %0, %1 offset:0" : "=v"(lo[0]) : "v"(tlo_a) : "memory");
    asm volatile("ds_read_b64_tr_b16 %0, %1 offset:0" : "=v"(hi[0]) : "v"(thi_a) : "memory");
    asm volatile("ds_read_b64_tr_b16 %0, %1 offset:512" : "=v"(lo[1]) : "v"(tlo_a) : "memory");
    asm volatile("ds_read_b64_tr_b16 %0, %1 offset:512" : "=v"(hi[1]) : "v"(thi_a) : "memory");
    asm volatile("s_waitcnt lgkmcnt(0)" : "+v"(lo[0]), "+v"(lo[1]), "+v"(hi[0]), "+v"(hi[1])::"memory");
    const unsigned vo = g * HT + c * 16 + (b & 3) * 256;
    const unsigned so = H_OFF + (b >> 2) * KSTRIDE + w * 4 * HT;
    store16_sc1(v4u{lo[0][0], lo[0][1], hi[0][0], hi[0][1]}, vo, srdWS, so);
    store16_sc1(v4u{lo[1][0], lo[1][1], hi[1][0], hi[1][1]}, w < 3 ? vo : OOB, srdWS, so + 16 * HT);
    const unsigned vf = lane < R1 ? lane * 8192 + w * 2048 + b * 8 : OOB;
    auto late_touch = [&]() {
      const unsigned wo = (lane & 2) ? 0 : w;
      const bool on = lane < (w == 1 ? 4u : 2u);
      if constexpr (LT & 1) {
        const unsigned v = on ? wo * (KW2 * 32) * S_ * 2 + nb2 * 128 + ((lane & 1) ? (KW2 * 32 - 1) * S_ * 2 : rot2 * (32 * S_ * 2)) : OOB;
        asm volatile("buffer_load_dword v248, %0, %1, 0 offen" ::"v"(v), "s"(W2.srd) : "memory");
      }
      if constexpr (LT & 2) {
        const unsigned v = !on ? OOB : (lane & 1) ? U_OFF + 4 * mb2 * UT + wo * KW2 * KSTRIDE : U_OFF + (4 * mb1 + wo) * UT + 2 * nb1 * KSTRIDE;
        asm volatile("buffer_load_dword v249, %0, %1, 0 offen" ::"v"(v), "s"(srdWS) : "memory");
      }
      if constexpr (LT & 4) {
        const srd_t srdB1 = mk_srd(b1, F_ * 2);
        asm volatile("buffer_load_dword v250, %0, %1, %2 offen" ::"v"(lane == 0 && w == 1 ? 0 : OOB), "s"(srdB1), "s"(nb1 * 128u) : "memory");
      }
      if constexpr (LT & 8) {
        const srd_t srdB2 = mk_srd(b2, S_ * 2);
        asm volatile("buffer_load_dword v251, %0, %1, %2 offen" ::"v"(lane == 0 && w == 1 ? 0 : OOB), "s"(srdB2), "s"(nb2 * 128u) : "memory");
      }
    };
    if (!g1) {
      waitv<0>();
      store8_sc1(v2u{tlo, thi}, vf, srdWS, HF_OFF);
      STAMP(2);
      if (w != 0) late_touch();
    } else if (w == 0) {
      glds_slots<true, NB1>(W1, make_iseq<NA0>{});
      waitv<4 * NA0>();
      store8_sc1(v2u{tlo, thi}, vf, srdWS, HF_OFF);
      STAMP(2);
      glds_slots<true, NB1 + NA0>(W1, make_iseq<NS1 - NB1 - NA0>{});
      wpipe_stepsx<true, 0, 2, NA0>(W1, make_iseq<7>{});
    } else {
      glds_slots<true, NB1>(W1, make_iseq<NA>{});
      waitv<4 * NA>();
      store8_sc1(v2u{tlo, thi}, vf, srdWS, HF_OFF);
      STAMP(2);
      late_touch();
      if (WSLEEP1) __builtin_amdgcn_s_sleep(WSLEEP1);
      glds_slots<true, NB1 + NA>(W1, make_iseq<NS1 - NB1 - NA>{});
      wpipe_stepsx<true, 0, 2, NA + 100 * NT2>(W1, make_iseq<7>{});
    }
  }
  Exch X;
  X.mine = (LAS unsigned char*)smem + w * REG_W + lane * 16;
#pragma unroll
  for (int j = 0; j < 3; ++j) X.from[j] = (const LAS unsigned char*)smem + ((w - 1 - j) & 3) * REG_W + j * 4096 + lane * 16;
  if (g1) {
    v2u bv[4];
    {
      const srd_t srdB1 = mk_srd(b1, F_ * 2);
      asm volatile("buffer_load_dwordx2 %0, %1, %2, %3 offen" : "=v"(bv[0]) : "v"(g * 8), "s"(srdB1), "s"(nb1 * 128u) : "memory");
      asm volatile("buffer_load_dwordx2 %0, %1, %2, %3 offen offset:32" : "=v"(bv[1]) : "v"(g * 8), "s"(srdB1), "s"(nb1 * 128u) : "memory");
      asm volatile("buffer_load_dwordx2 %0, %1, %2, %3 offen offset:64" : "=v"(bv[2]) : "v"(g * 8), "s"(srdB1), "s"(nb1 * 128u) : "memory");
      asm volatile("buffer_load_dwordx2 %0, %1, %2, %3 offen offset:96" : "=v"(bv[3]) : "v"(g * 8), "s"(srdB1), "s"(nb1 * 128u) : "memory");
    }
    GAP();
    if (w == 0) {
      const unsigned so = HF_OFF + (b % R1) * 8192 + (mb1 & 3) * 2048;
      v4u f[2];
      poll2_issue<2>(f, lane * 16, srdWS, so);
      wtail_steps<true, 7, 6, NA0>(W1, make_iseq<KW1 - 7>{});
      STAMPX0(3);
      if (!poll2_check<2>(f, true, tlo, thi)) poll_flags2<PSLEEP, 2>(lane * 16, true, srdWS, so, tlo, thi);
    }
    asm volatile("s_barrier" ::: "memory");
    STAMPA(4);
    AStream A;
    A.srd = srdWS; A.beg = H_OFF + 4 * mb1 * HT + w * KW1 * KSTRIDE; A.end = A.beg + KW1 * KSTRIDE; A.cur = A.beg + rot1 * KSTRIDE;
#pragma unroll
    for (int m = 0; m < 4; ++m) A.voff[m] = lane * 16 + ((m + w) & 3) * HT;
    gemm_prologue<true>(A, W2, make_iseq<DF1>{});
    if (w != 0) { wtail_steps<true, 7, 4 + DF1, NA>(W1, make_iseq<KW1 - 7>{}); STAMPX1(3); }
    GAP();
    asm volatile("s_waitcnt vmcnt(%4) lgkmcnt(0)" : "+v"(bv[0]), "+v"(bv[1]), "+v"(bv[2]), "+v"(bv[3]) : "i"(DF1) : "memory");
    f32x4 s[4];
#pragma unroll
    for (int t = 0; t < 4; ++t) { const v2u q = bv[t]; const unsigned q0 = q[0], q1 = q[1]; s[t] = f32x4{bflo(q0), bfhi(q0), bflo(q1), bfhi(q1)}; }
    GAP();
    gemm_body<true>(s, A, W2, X);
    STAMPA(5);
    if (!W2POS) glds_slots<false, TPF>(W2, make_iseq<NS2 - TPF>{});
    GAP();
#pragma unroll
    for (int kk = 0; kk < 2; ++kk) {
      const f32x4 s0 = s[2 * kk], s1 = s[2 * kk + 1];
      const v4u o = {pack2(gelu(s0[0]), gelu(s0[1])), pack2(gelu(s0[2]), gelu(s0[3])), pack2(gelu(s1[0]), gelu(s1[1])), pack2(gelu(s1[2]), gelu(s1[3]))};
      store16_sc1(o, lane * 16, srdWS, U_OFF + (4 * mb1 + w) * UT + (2 * nb1 + kk) * KSTRIDE);
    }
    GAP();
    if (W2POS) glds_slots<false, TPF>(W2, make_iseq<NS2 - TPF>{});
    wpipe_steps<false, 0, 1>(W2, make_iseq<POST2>{});
    waitv<4 * POST2 + (W2POS ? 4 * (NS2 - TPF) : 0)>();
    store8_sc1(v2u{tlo, thi}, lane < R2 ? lane * UF_REP + (mb1 * 4 + nb1 / 7) * 256 + ((nb1 % 7) * 4 + w) * 8 : OOB, srdWS, UF_OFF);
    STAMP(6);
    if (!g2) {
      if (b == EPOCH_WG && w == 1) store8_sc1(v2u{tlo, thi}, lane == 0 ? 0 : OOB, srdWS, EP_OFF);
      waitv<0>();
      goto done;
    }
    if (w != 0) __builtin_amdgcn_s_sleep(WSLEEP2);
    wpipe_steps<false, POST2, 2>(W2, make_iseq<NS2 - POST2>{});
  } else {
    wg_barrier();
    if (!g2) goto done;
#pragma unroll 1
    for (int i = 0; i < IDLE_SLEEP; ++i) __builtin_amdgcn_s_sleep(16);
    glds_slots<false, 0>(W2, make_iseq<NS2>{});
    wpipe_steps<false, 0, 0>(W2, make_iseq<KW2>{});
    if (w == 0) poll_flags<8>(0, srdWS, EP_OFF, tlo, thi);
    wg_barrier();
  }
  {
    unsigned bv[4];
    {
      const srd_t srdB2 = mk_srd(b2, S_ * 2);
      asm volatile("buffer_load_ushort %0, %1, %2, %3 offen" : "=v"(bv[0]) : "v"(c * 2), "s"(srdB2), "s"(nb2 * 128u) : "memory");
      asm volatile("buffer_load_ushort %0, %1, %2, %3 offen offset:32" : "=v"(bv[1]) : "v"(c * 2), "s"(srdB2), "s"(nb2 * 128u) : "memory");
      asm volatile("buffer_load_ushort %0, %1, %2, %3 offen offset:64" : "=v"(bv[2]) : "v"(c * 2), "s"(srdB2), "s"(nb2 * 128u) : "memory");
      asm volatile("buffer_load_ushort %0, %1, %2, %3 offen offset:96" : "=v"(bv[3]) : "v"(c * 2), "s"(srdB2), "s"(nb2 * 128u) : "memory");
    }
    GAP();
    if (w == 0) {
      const unsigned so = UF_OFF + (nb2 % R2) * UF_REP + mb2 * 1024;
      v4u f[2];
      poll2_issue<1>(f, lane * 16, srdWS, so);
      if (g1) wtail_steps<false, NS2, 5, NA>(W2, make_iseq<KW2 - NS2>{});
      if (!poll2_check<1>(f, c < 14, tlo, thi)) poll_flags2<PSLEEP, 1>(lane * 16, c < 14, srdWS, so, tlo, thi);
    }
    asm volatile("s_barrier" ::: "memory");
    STAMPB(4);
    AStream A;
    A.srd = srdWS; A.beg = U_OFF + 4 * mb2 * UT + w * KW2 * KSTRIDE; A.end = A.beg + KW2 * KSTRIDE; A.cur = A.beg + rot2 * KSTRIDE;
#pragma unroll
    for (int m = 0; m < 4; ++m) A.voff[m] = lane * 16 + ((m + w) & 3) * UT;
    gemm_prologue<false>(A, W2, make_iseq<DF2>{});
    if (w != 0 && g1) wtail_steps<false, NS2, 4 + DF2, NA>(W2, make_iseq<KW2 - NS2>{});
    STAMPB(3);
    GAP();
    asm volatile("s_waitcnt vmcnt(%4) lgkmcnt(0)" : "+v"(bv[0]), "+v"(bv[1]), "+v"(bv[2]), "+v"(bv[3]) : "i"(DF2) : "memory");
    f32x4 s[4];
#pragma unroll
    for (int t = 0; t < 4; ++t) { const float bb = bflo(bv[t]); s[t] = f32x4{bb, bb, bb, bb}; }
    GAP();
    gemm_body<false>(s, A, W2, X);
    STAMPB(5);
    unsigned char* stage = smem + W2_OFF;
#pragma unroll
    for (int t = 0; t < 4; ++t) *(LAS v2u*)((LAS unsigned char*)stage + (16 * t + c) * 144 + 32 * w + 8 * g) = v2u{pack2(s[t][0], s[t][1]), pack2(s[t][2], s[t][3])};
    wg_barrier();
    const srd_t srdO = mk_srd(out, S_ * D_ * 2);
    const unsigned row = 16 * w + (lane >> 3), col = (lane & 7) * 16;
#pragma unroll
    for (int mp = 0; mp < 2; ++mp) {
      const v4u o = *(const LAS v4u*)((LAS unsigned char*)stage + (row + 8 * mp) * 144 + col);
      asm volatile("buffer_store_dwordx4 %0, %1, %2, %3 offen\n\ts_nop 1" ::"v"(o), "v"((row + 8 * mp) * (D_ * 2) + col), "s"(srdO), "s"(nb2 * 64 * (D_ * 2) + mb2 * 128) : "memory");
    }
  }
done:
  STAMP(7);
}

extern "C" void kernel_launch(void* const* d_in, const int* in_sizes, int n_in, void* d_out, int out_size, void* d_ws, size_t ws_size, hipStream_t stream) {
  mixer_kernel<<<256, 256, 0, stream>>>((const u16*)d_in[0], (const u16*)d_in[1], (const u16*)d_in[2], (const u16*)d_in[3], (const u16*)d_in[4],
                                       (u16*)d_out, (unsigned char*)d_ws);
}
```

```cpp
#include <hip/hip_runtime.h>
#include <stdint.h>
#include <utility>

typedef unsigned v4u __attribute__((ext_vector_type(4)));
typedef unsigned v2u __attribute__((ext_vector_type(2)));
typedef float f32x4 __attribute__((ext_vector_type(4)));
typedef unsigned short u16;
typedef __amdgpu_buffer_rsrc_t srd_t;
#define DI __device__ __forceinline__
#define LAS __attribute__((address_space(3)))
#define GAP() __builtin_amdgcn_sched_barrier(0)
template <int... Is> using iseq = std::integer_sequence<int, Is...>;
template <int N> using make_iseq = std::make_integer_sequence<int, N>;
#define ACLOB "a0","a1","a2","a3","a4","a5","a6","a7","a8","a9","a10","a11","a12","a13","a14","a15","a16","a17","a18","a19","a20","a21","a22","a23","a24","a25","a26","a27","a28","a29","a30","a31","a32","a33","a34","a35","a36","a37","a38","a39","a40","a41","a42","a43","a44","a45","a46","a47","a48","a49","a50","a51","a52","a53","a54","a55","a56","a57","a58","a59","a60","a61","a62","a63","a64","a65","a66","a67","a68","a69","a70","a71","a72","a73","a74","a75","a76","a77","a78","a79","a80","a81","a82","a83","a84","a85","a86","a87","a88","a89","a90","a91","a92","a93","a94","a95","a96","a97","a98","a99","a100","a101","a102","a103","a104","a105","a106","a107","a108","a109","a110","a111","a112","a113","a114","a115","a116","a117","a118","a119","a120","a121","a122","a123","a124","a125","a126","a127","a128","a129","a130","a131","a132","a133","a134","a135","a136","a137","a138","a139","a140","a141","a142","a143","a144","a145","a146","a147","a148","a149","a150","a151","a152","a153","a154","a155","a156","a157","a158","a159","a160","a161","a162","a163","a164","a165","a166","a167","a168","a169","a170","a171","a172","a173","a174","a175","a176","a177","a178","a179","a180","a181","a182","a183","a184","a185","a186","a187","a188","a189","a190","a191","a192","a193","a194","a195","a196","a197","a198","a199","a200","a201","a202","a203","a204","a205","a206","a207","a208","a209","a210","a211","a212","a213","a214","a215","a216","a217","a218","a219","a220","a221","a222","a223","a224","a225","a226","a227","a228","a229","a230","a231","a232","a233","a234","a235","a236","a237","a238","a239","a240","a241","a242","a243","a244","a245","a246","a247","a248","a249","a250","a251","a252","a253","a254","a255"

#define ACL0 "a0","a1","a2","a3","a4","a5","a6","a7","a8","a9","a10","a11","a12","a13","a14","a15","a16","a17","a18","a19","a20","a21","a22","a23","a24","a25","a26","a27","a28","a29","a30","a31","a32","a33","a34","a35","a36","a37","a38","a39","a40","a41","a42","a43","a44","a45","a46","a47","a48","a49","a50","a51","a52","a53","a54","a55","a56","a57","a58","a59","a60","a61","a62","a63"
#define ACL1 "a64","a65","a66","a67","a68","a69","a70","a71","a72","a73","a74","a75","a76","a77","a78","a79","a80","a81","a82","a83","a84","a85","a86","a87","a88","a89","a90","a91","a92","a93","a94","a95","a96","a97","a98","a99","a100","a101","a102","a103","a104","a105","a106","a107","a108","a109","a110","a111","a112","a113","a114","a115","a116","a117","a118","a119","a120","a121","a122","a123","a124","a125","a126","a127"
#define ACL2 "a128","a129","a130","a131","a132","a133","a134","a135","a136","a137","a138","a139","a140","a141","a142","a143","a144","a145","a146","a147","a148","a149","a150","a151","a152","a153","a154","a155","a156","a157","a158","a159","a160","a161","a162","a163","a164","a165","a166","a167","a168","a169","a170","a171","a172","a173","a174","a175","a176","a177","a178","a179","a180","a181","a182","a183","a184","a185","a186","a187","a188","a189","a190","a191"
#define ACL3 "a192","a193","a194","a195","a196","a197","a198","a199","a200","a201","a202","a203","a204","a205","a206","a207","a208","a209","a210","a211","a212","a213","a214","a215","a216","a217","a218","a219","a220","a221","a222","a223","a224","a225","a226","a227","a228","a229","a230","a231","a232","a233","a234","a235","a236","a237","a238","a239","a240","a241","a242","a243","a244","a245","a246","a247","a248","a249","a250","a251","a252","a253","a254","a255"

#define TOUCH 5
#define MAPPING 3
#define DF1 16
#define DF2 16
#define HB 5
#define W2POS 1
#define PRE 3
#define MID 0
#define NA 3
#define NA0 3
#define EARLYPOLL 1
#define POST2 0
#define ROT1 6
#define ROT2 1
#define ROTN1 2
#define ROTN2 2
#define IDLE_SLEEP 0
#define PSLEEP 2
#define QUIET 1
#define WSLEEP1 16
#define WSLEEP2 16
#define R1 4
#define R2 4

constexpr int S_ = 2048, D_ = 448, F_ = 1792;
constexpr int KW1 = 16, KW2 = 14;
constexpr int NS1 = 9, NS2 = 7;
#define TPFMAX 6
constexpr int TPF = DF1 / 4 < TPFMAX ? DF1 / 4 : TPFMAX;
constexpr unsigned KSTRIDE = 1024u;
constexpr unsigned HT = 64u * KSTRIDE, UT = 56u * KSTRIDE;
constexpr unsigned H_OFF = 0, U_OFF = 28u * HT;
constexpr unsigned EP_OFF = 4u << 20, HF_OFF = EP_OFF + 4096u, UF_OFF = HF_OFF + 16u * 8192u;
constexpr unsigned UF_REP = 28u * 256u;
constexpr unsigned WS_USED = UF_OFF + 16u * UF_REP;
constexpr unsigned REG_W = 40960, LN_OFF = 36864, LNP = 960, W2_OFF = 12288, LDS_BYTES = 4 * REG_W;
constexpr unsigned OOB = 0x7ffffff0u;
constexpr int SPIN_MAX = 1 << 15;
constexpr unsigned EPOCH_WG = 225;

DI unsigned rfl(unsigned x) { return __builtin_amdgcn_readfirstlane(x); }
DI float bflo(unsigned w) { return __builtin_bit_cast(float, w << 16); }
DI float bfhi(unsigned w) { return __builtin_bit_cast(float, w & 0xffff0000u); }
DI unsigned pack2(float lo, float hi) {
  typedef __bf16 bf2 __attribute__((ext_vector_type(2)));
  typedef float f2 __attribute__((ext_vector_type(2)));
  f2 v = {lo, hi};
  return __builtin_bit_cast(unsigned, __builtin_convertvector(v, bf2));
}
DI srd_t mk_srd(const void* p, unsigned bytes) { return __builtin_amdgcn_make_buffer_rsrc((void*)p, 0, bytes, 0x00020000); }
template <int C> DI float dpp_add(float v) {
  return v + __builtin_bit_cast(float, __builtin_amdgcn_update_dpp(0, __builtin_bit_cast(int, v), C, 0xf, 0xf, true));
}
DI void wave_sum2(float& a, float& b) {
  a = dpp_add<0xB1>(a); b = dpp_add<0xB1>(b); a = dpp_add<0x4E>(a); b = dpp_add<0x4E>(b);
  a = dpp_add<0x141>(a); b = dpp_add<0x141>(b); a = dpp_add<0x140>(a); b = dpp_add<0x140>(b);
  auto p = __builtin_amdgcn_permlane16_swap(__builtin_bit_cast(unsigned, a), __builtin_bit_cast(unsigned, b), false, false);
  const unsigned p0 = p[0], p1 = p[1];
  float s = __builtin_bit_cast(float, p0) + __builtin_bit_cast(float, p1);
  auto q = __builtin_amdgcn_permlane32_swap(__builtin_bit_cast(unsigned, s), __builtin_bit_cast(unsigned, s), false, false);
  const unsigned q0 = q[0], q1 = q[1];
  s = __builtin_bit_cast(float, q0) + __builtin_bit_cast(float, q1);
  a = __builtin_bit_cast(float, __builtin_amdgcn_readlane(__builtin_bit_cast(int, s), 0));
  b = __builtin_bit_cast(float, __builtin_amdgcn_readlane(__builtin_bit_cast(int, s), 16));
}
template <int N> DI void waitv() { static_assert(N >= 0 && N < 64, "vmcnt"); asm volatile("s_waitcnt vmcnt(%0)" ::"i"(N) : "memory"); }
DI void wait_lds() { asm volatile("s_waitcnt lgkmcnt(0)" ::: "memory"); }
DI void wg_barrier() { asm volatile("s_waitcnt lgkmcnt(0)\n\ts_barrier" ::: "memory"); }

struct WStream {
  srd_t srd;
  unsigned cur, beg, end;
  unsigned vo;
  unsigned ring;
  unsigned ta[4];
};
DI void cyc_add(unsigned& cur, unsigned inc, unsigned beg, unsigned end) {
  asm volatile("s_add_u32 %0, %0, %1\n\ts_cmp_eq_u32 %0, %3\n\ts_cselect_b32 %0, %2, %0" : "+s"(cur) : "s"(inc), "s"(beg), "s"(end) : "scc");
}
template <bool W1> struct WP { static constexpr int LDW = W1 ? F_ : S_, KW = W1 ? KW1 : KW2, NS = W1 ? NS1 : NS2, OFF = W1 ? 0 : (int)W2_OFF; };
template <bool W1, int KSTEP, int I> DI void glds_piece(WStream& s) {
  using P = WP<W1>;
  unsigned t;
  asm volatile("s_add_u32 m0, %1, %2\n\ts_add_u32 %0, %3, %4\n\tbuffer_load_dwordx4 %5, %6, %0 offen lds"
               : "=&s"(t)
               : "s"(s.ring), "i"(P::OFF + (KSTEP % P::NS) * 4096 + I * 1024), "s"(s.cur), "i"(I * 8 * P::LDW * 2), "v"(s.vo), "s"(s.srd)
               : "scc", "memory");
  if constexpr (I == 3) cyc_add(s.cur, 32 * P::LDW * 2, s.beg, s.end);
}
template <bool W1, int KSTEP> DI void glds_slot(WStream& s) {
  glds_piece<W1, KSTEP, 0>(s); glds_piece<W1, KSTEP, 1>(s); glds_piece<W1, KSTEP, 2>(s); glds_piece<W1, KSTEP, 3>(s);
}
template <bool W1, int KSTEP, int T, int H> DI void tr_piece(const WStream& s) {
  using P = WP<W1>;
  asm volatile("ds_read_b64_tr_b16 a[%c1:%c2], %0 offset:%c3" ::"v"(s.ta[T]), "i"(16 * KSTEP + 4 * T + 2 * H), "i"(16 * KSTEP + 4 * T + 2 * H + 1),
               "i"(P::OFF + (KSTEP % P::NS) * 4096 + H * 2048) : "memory", ACLOB);
}
template <bool W1, int KSTEP> DI void tr_slot(const WStream& s) {
  tr_piece<W1, KSTEP, 0, 0>(s); tr_piece<W1, KSTEP, 0, 1>(s); tr_piece<W1, KSTEP, 1, 0>(s); tr_piece<W1, KSTEP, 1, 1>(s);
  tr_piece<W1, KSTEP, 2, 0>(s); tr_piece<W1, KSTEP, 2, 1>(s); tr_piece<W1, KSTEP, 3, 0>(s); tr_piece<W1, KSTEP, 3, 1>(s);
}
constexpr int NB1 = PRE + MID;
static_assert(NB1 + NA <= NS1 && NB1 + NA0 <= NS1 && POST2 <= NS2, "schedule");
template <bool W1, int NAX> constexpr int younger(int j, bool flagged) {
  constexpr int KW = W1 ? KW1 : KW2, NS = W1 ? NS1 : NS2;
  const int before_stores = W1 ? NB1 : (W2POS ? TPF : NS2), before_flag = W1 ? NB1 + NAX : NS2 + POST2;
  const int last = j + NS - 1 < KW - 1 ? j + NS - 1 : KW - 1;
  return 4 * (last - j) + (j < before_stores ? 2 : 0) + (flagged && j < before_flag ? 1 : 0);
}
template <bool W1, int J, int MODE, int EXTRA, int NAX> DI void wpipe_step(WStream& s) {
  using P = WP<W1>;
  constexpr int rem = P::KW - 1 - J;
  waitv<(MODE == 0 ? 4 * (rem < P::NS - 1 ? rem : P::NS - 1) : younger<W1, NAX>(J, MODE == 2)) + EXTRA>();
  tr_slot<W1, J>(s);
  if constexpr (J + P::NS < P::KW) { wait_lds(); glds_slot<W1, J + P::NS>(s); }
  GAP();
}
template <bool W1, int J0, int MODE, int NAX, int... Js> DI void wpipe_stepsx(WStream& s, iseq<Js...>) { (wpipe_step<W1, J0 + Js, MODE, 0, NAX>(s), ...); }
template <bool W1, int J0, int MODE, int... Js> DI void wpipe_steps(WStream& s, iseq<Js...>) { (wpipe_step<W1, J0 + Js, MODE, 0, NA>(s), ...); }
template <bool W1, int J0, int EXTRA, int NAX, int... Js> DI void wtail_steps(WStream& s, iseq<Js...>) { (wpipe_step<W1, J0 + Js, 2, EXTRA, NAX>(s), ...); }
template <bool W1, int J0, int... Js> DI void glds_slots(WStream& s, iseq<Js...>) { (glds_slot<W1, J0 + Js>(s), ...); }

constexpr int RB = 160;
static_assert(RB + 4 * DF1 <= 256 && RB + 4 * DF2 <= 256, "ring");
template <int REG> DI void act_load(unsigned voff, srd_t srd, unsigned soff) {
  asm volatile("buffer_load_dwordx4 v[%c0:%c1], %2, %3, %4 offen sc1" ::"i"(REG), "i"(REG + 3), "v"(voff), "s"(srd), "s"(soff) : "memory");
}
struct AStream { srd_t srd; unsigned voff[4], so[16]; };
template <int KW> DI void astream_init(AStream& a, unsigned beg, unsigned rot) {
  unsigned cur = beg + rot * KSTRIDE;
#pragma unroll
  for (int k = 0; k < KW; ++k) { a.so[k] = cur; cyc_add(cur, KSTRIDE, beg, beg + KW * KSTRIDE); }
}
struct W2Head { unsigned vo[4], cur[8], ring; srd_t srd; };
#define MFMA_ASM(CL)                                                                                                                                       \
  if constexpr (WA) {                                                                                                                                      \
    if constexpr (ZERO) asm volatile("v_mfma_f32_16x16x32_bf16 %0, a[%c1:%c2], v[%c3:%c4], 0" : "=&v"(acc) : "i"(AR), "i"(AR + 3), "i"(VR), "i"(VR + 3) : CL); \
    else asm volatile("v_mfma_f32_16x16x32_bf16 %0, a[%c1:%c2], v[%c3:%c4], %0" : "+v"(acc) : "i"(AR), "i"(AR + 3), "i"(VR), "i"(VR + 3) : CL);               \
  } else {                                                                                                                                                 \
    if constexpr (ZERO) asm volatile("v_mfma_f32_16x16x32_bf16 %0, v[%c3:%c4], a[%c1:%c2], 0" : "=&v"(acc) : "i"(AR), "i"(AR + 3), "i"(VR), "i"(VR + 3) : CL); \
    else asm volatile("v_mfma_f32_16x16x32_bf16 %0, v[%c3:%c4], a[%c1:%c2], %0" : "+v"(acc) : "i"(AR), "i"(AR + 3), "i"(VR), "i"(VR + 3) : CL);               \
  }
template <bool WA, bool ZERO, int AR, int VR, int Q> DI void mfma(f32x4& acc) {
  if constexpr (Q == 0) { MFMA_ASM(ACL0) } else if constexpr (Q == 1) { MFMA_ASM(ACL1) } else if constexpr (Q == 2) { MFMA_ASM(ACL2) } else { MFMA_ASM(ACL3) }
}
template <int N> DI void waitv_b() { static_assert(N >= 0 && N < 64, "vmcnt"); __builtin_amdgcn_s_waitcnt((N & 15) | ((N >> 4) << 14) | 0x0F70); }
template <bool G1> constexpr int vop(int v) { return v < 4 * (G1 ? KW1 : KW2) ? 1 : (G1 && v - 4 * KW1 < 4 * TPF ? 1 : 0); }
template <bool G1> constexpr int vop_range(int a, int b) { int n = 0; for (int v = a; v <= b; ++v) n += vop<G1>(v); return n; }
template <bool G1, int V> DI void act_op(const AStream& a) {
  constexpr int KW = G1 ? KW1 : KW2, DF = G1 ? DF1 : DF2;
  act_load<RB + 4 * (V % DF)>(a.voff[(V / KW + 1) & 3], a.srd, a.so[V % KW]);
}
template <int P> DI void w2_m0(const W2Head& h) {
  asm volatile("s_add_u32 m0, %0, %1" ::"s"(h.ring), "i"((int)W2_OFF + ((P / 4) % NS2) * 4096 + (P % 4) * 1024) : "scc", "memory");
}
template <int P> DI void w2_load(const W2Head& h) {
  asm volatile("buffer_load_dwordx4 %0, %1, %2 offen lds" ::"v"(h.vo[P % 4]), "s"(h.srd), "s"(h.cur[P / 4]) : "memory");
}
struct Exch { LAS unsigned char* mine; const LAS unsigned char* from[3]; };
template <bool G1> constexpr bool has_piece(int i) { constexpr int KW = G1 ? KW1 : KW2, DF = G1 ? DF1 : DF2; return i >= 1 && i - 1 + DF >= 4 * KW && vop<G1>(i - 1 + DF) != 0; }
template <bool G1> constexpr int xcap(int i, int g) {
  constexpr int KW = G1 ? KW1 : KW2;
  const int j = i - 3 * KW;
  if (j < HB || (j == HB && g == 0)) return 0;
  return 2 - (g == 3 ? 1 : 0) - (has_piece<G1>(i) && g < 2 ? 1 : 0);
}
template <bool G1> constexpr int xfirst(int i, int g) {
  constexpr int KW = G1 ? KW1 : KW2;
  int n = 0;
  for (int ii = 3 * KW; ii <= i; ++ii)
    for (int gg = 0; gg < 4 && (ii < i || gg < g); ++gg) n += xcap<G1>(ii, gg);
  return n;
}
constexpr int NX = 12 + 1 + 32;
static_assert(xfirst<true>(4 * KW1, 0) >= NX && xfirst<false>(4 * KW2, 0) >= NX, "exchange does not fit");
struct XRegs { f32x4 r[3][4]; float a[4][4], s[4][4]; };
DI float addf(float a, float b) { float d; asm volatile("v_add_f32 %0, %1, %2" : "=v"(d) : "v"(a), "v"(b)); return d; }
template <int N> DI void xop(XRegs& r, const Exch& x) {
  if constexpr (N < 12) r.r[N / 4][N % 4] = *(const LAS f32x4*)(x.from[N / 4] + (N % 4) * 1024);
  else if constexpr (N == 12) __builtin_amdgcn_s_waitcnt(0xC07F);
  else if constexpr (N < 29) { constexpr int t = (N - 13) / 4, e = (N - 13) % 4; const float p = r.r[0][t][e], q = r.r[1][t][e]; r.a[t][e] = addf(p, q); }
  else if constexpr (N < NX) { constexpr int t = (N - 29) / 4, e = (N - 29) % 4; const float p = r.r[2][t][e]; r.s[t][e] = addf(r.a[t][e], p); }
}
template <bool G1, int I, int G> DI void slot(f32x4 (&acc)[4][4], XRegs& r, const AStream& a, const W2Head& h, const Exch& x) {
  constexpr int KW = G1 ? KW1 : KW2, DF = G1 ? DF1 : DF2, V = I - 1 + DF;
  GAP();
  if constexpr (I >= 1 && V < 4 * KW) { if constexpr (G == 0) act_op<G1, V>(a); }
  else if constexpr (has_piece<G1>(I)) {
    if constexpr (G == 0) w2_m0<V - 4 * KW>(h);
    if constexpr (G == 1) w2_load<V - 4 * KW>(h);
  }
  if constexpr (I % KW == 1 && I > KW) {
    constexpr int m = I / KW;
    asm volatile("" : "+v"(acc[m][G]));
    *(LAS f32x4*)(x.mine + (m - 1) * 4096 + G * 1024) = acc[m][G];
  }
  if constexpr (I == 3 * KW + HB && G == 0) wg_barrier();
  constexpr int n0 = xfirst<G1>(I, G), cap = xcap<G1>(I, G);
  if constexpr (cap >= 1 && n0 < NX) xop<n0>(r, x);
  if constexpr (cap >= 2 && n0 + 1 < NX) xop<n0 + 1>(r, x);
  GAP();
}
template <bool G1, int I> DI void gemm_frag(f32x4 (&acc)[4][4], XRegs& r, const AStream& a, const W2Head& h, const Exch& x) {
  constexpr int KW = G1 ? KW1 : KW2, DF = G1 ? DF1 : DF2, K = I % KW, M = (I / KW + 1) & 3, VR = RB + 4 * (I % DF);
  constexpr bool Z = K == 0 && M != 0;
  waitv_b<I == 0 ? vop_range<G1>(1, DF - 1) : vop_range<G1>(I + 1, I - 2 + DF)>();
  GAP();
  mfma<G1, Z, 16 * K + 0, VR, 0>(acc[M][0]);
  slot<G1, I, 0>(acc, r, a, h, x);
  mfma<G1, Z, 16 * K + 4, VR, 1>(acc[M][1]);
  slot<G1, I, 1>(acc, r, a, h, x);
  mfma<G1, Z, 16 * K + 8, VR, 2>(acc[M][2]);
  slot<G1, I, 2>(acc, r, a, h, x);
  mfma<G1, Z, 16 * K + 12, VR, 3>(acc[M][3]);
  slot<G1, I, 3>(acc, r, a, h, x);
}
template <bool G1, int... Is> DI void gemm_frags(f32x4 (&acc)[4][4], XRegs& r, const AStream& a, const W2Head& h, const Exch& x, iseq<Is...>) {
  (gemm_frag<G1, Is>(acc, r, a, h, x), ...);
}
template <bool G1, int... Vs> DI void gemm_prologue(const AStream& a, iseq<Vs...>) { (act_op<G1, Vs>(a), ...); GAP(); }
template <bool G1> DI void gemm_body(f32x4 (&pre)[4], const AStream& a, const W2Head& h, const Exch& x) {
  constexpr int KW = G1 ? KW1 : KW2, DF = G1 ? DF1 : DF2, VL = 4 * KW - 1 + DF;
  f32x4 acc[4][4];
  XRegs r;
#pragma unroll
  for (int t = 0; t < 4; ++t) acc[0][t] = pre[t];
  asm volatile("" : "+v"(acc[0][0]), "+v"(acc[0][1]), "+v"(acc[0][2]), "+v"(acc[0][3]));
  GAP();
  gemm_frags<G1>(acc, r, a, h, x, make_iseq<4 * KW>{});
  if constexpr (vop<G1>(VL) != 0) {
    w2_m0<VL - 4 * KW>(h);
    asm volatile("s_nop 0");
    w2_load<VL - 4 * KW>(h);
  }
  asm volatile("s_nop 7" : "+v"(acc[0][0]), "+v"(acc[0][1]), "+v"(acc[0][2]), "+v"(acc[0][3]));
#pragma unroll
  for (int t = 0; t < 4; ++t) pre[t] = acc[0][t] + f32x4{r.s[t][0], r.s[t][1], r.s[t][2], r.s[t][3]};
}
template <int SLEEP> DI void poll_flags(unsigned voff, srd_t srd, unsigned soff, unsigned tlo, unsigned thi) {
#pragma unroll 1
  for (int spin = 0; spin < SPIN_MAX; ++spin) {
    v2u f;
    asm volatile("buffer_load_dwordx2 %0, %1, %2, %3 offen sc1\n\ts_waitcnt vmcnt(0)" : "=v"(f) : "v"(voff), "s"(srd), "s"(soff) : "memory");
    if (__builtin_amdgcn_ballot_w64(f[0] != tlo || f[1] != thi) == 0) break;
    __builtin_amdgcn_s_sleep(SLEEP);
  }
}
template <int N> DI void poll2_issue(v4u (&f)[2], unsigned voff, srd_t srd, unsigned soff) {
  asm volatile("buffer_load_dwordx4 %0, %1, %2, %3 offen sc1" : "=v"(f[0]) : "v"(voff), "s"(srd), "s"(soff) : "memory");
  if constexpr (N == 2) asm volatile("buffer_load_dwordx4 %0, %1, %2, %3 offen offset:1024 sc1" : "=v"(f[1]) : "v"(voff), "s"(srd), "s"(soff) : "memory");
}
template <int N> DI bool poll2_check(v4u (&f)[2], bool use, unsigned tlo, unsigned thi) {
  if constexpr (N == 2) asm volatile("s_waitcnt vmcnt(0)" : "+v"(f[0]), "+v"(f[1])::"memory");
  else asm volatile("s_waitcnt vmcnt(0)" : "+v"(f[0])::"memory");
  const v4u a = f[0];
  bool bad = a[0] != tlo || a[1] != thi || a[2] != tlo || a[3] != thi;
  if constexpr (N == 2) { const v4u c = f[1]; bad = bad || c[0] != tlo || c[1] != thi || c[2] != tlo || c[3] != thi; }
  return __builtin_amdgcn_ballot_w64(bad && use) == 0;
}
template <int SLEEP, int N> DI void poll_flags2(unsigned voff, bool use, srd_t srd, unsigned soff, unsigned tlo, unsigned thi) {
#pragma unroll 1
  for (int spin = 0; spin < SPIN_MAX; ++spin) {
    v4u f[2];
    poll2_issue<N>(f, voff, srd, soff);
    if (poll2_check<N>(f, use, tlo, thi)) break;
    __builtin_amdgcn_s_sleep(SLEEP);
  }
}
DI void store16_sc1(v4u d, unsigned voff, srd_t srd, unsigned soff) {
  asm volatile("buffer_store_dwordx4 %0, %1, %2, %3 offen sc1\n\ts_nop 1" ::"v"(d), "v"(voff), "s"(srd), "s"(soff) : "memory");
}
DI void store8_sc1(v2u d, unsigned voff, srd_t srd, unsigned soff) {
  asm volatile("buffer_store_dwordx2 %0, %1, %2, %3 offen sc1\n\ts_nop 1" ::"v"(d), "v"(voff), "s"(srd), "s"(soff) : "memory");
}
DI float gelu(float u) {
  const float e = __builtin_amdgcn_exp2f(u * (-2.3022082f - 0.1029432f * u * u));
  return u * __builtin_amdgcn_rcpf(1.0f + e);
}
#define PA(i)
#define PB(i)
#define STAMP(i)
#define STAMPA(i)
#define STAMPB(i)
#define STAMPX0(i)
#define STAMPX1(i)

__global__ void __launch_bounds__(256, 1) __attribute__((amdgpu_num_vgpr(RB)))
mixer_kernel(const u16* __restrict__ x, const u16* __restrict__ w1, const u16* __restrict__ b1, const u16* __restrict__ w2, const u16* __restrict__ b2,
             u16* __restrict__ out, unsigned char* __restrict__ ws) {
  __shared__ __attribute__((aligned(1024))) unsigned char smem[LDS_BYTES];
  STAMP(0);
  asm volatile("" ::: ACLOB);
  asm volatile("" ::: "v160", "v255");
  const unsigned tid = threadIdx.x, lane = tid & 63, w = rfl(tid >> 6), b = blockIdx.x;
  const unsigned lds0 = (unsigned)(uintptr_t)(LAS unsigned char*)smem;
  const srd_t srdWS = mk_srd(ws, WS_USED);
  const unsigned g = lane >> 4, c = lane & 15;

  v2u ep; v4u xv[2];
  {
    const srd_t srdX = mk_srd(x, S_ * D_ * 2);
    const unsigned zero = 0, vx = lane < 56 ? lane * 16 : OOB, row = 32 * (b >> 2) + 4 * (b & 3) + 2 * (w & 1) + 16 * (w >> 1);
    asm volatile("buffer_load_dwordx4 %0, %1, %2, %3 offen" : "=v"(xv[0]) : "v"(vx), "s"(srdX), "s"(row * 896u) : "memory");
    asm volatile("buffer_load_dwordx4 %0, %1, %2, %3 offen offset:896" : "=v"(xv[1]) : "v"(vx), "s"(srdX), "s"(row * 896u) : "memory");
    asm volatile("buffer_load_dwordx2 %0, %1, %2, %3 offen sc1" : "=v"(ep) : "v"(zero), "s"(srdWS), "s"(EP_OFF) : "memory");
  }
  GAP();
  const unsigned xcd = b & 7, li = b >> 3;
  bool g1, g2;
  unsigned mb1, nb1, mb2, nb2;
  {
    const unsigned j = xcd >> 1, odd = xcd & 1, i0 = 7 + odd;
    g1 = li >= i0;
    const unsigned idx = g1 ? li - i0 + (odd ? 25 : 0) : 0;
    nb1 = 7 * j + idx / 7; mb1 = idx % 7;
    g2 = li < 28; nb2 = 4 * xcd + (li & 3); mb2 = g2 ? li >> 2 : 0;
  }
  const unsigned rot1 = ROT1 == 6 ? (nb1 % 7 != 3 ? (mb1 * 16) / 7 : mb1 < 4 ? 4 * mb1 : ((mb1 - 4) * 16) / 3) : ROT1 == 7 ? (nb1 % 7 != 3 ? (mb1 * 16) / 7 : mb1 < 4 ? 4 * mb1 + 1 : ((mb1 - 4) * 16) / 3 + 3) : ROT1 == 0 ? 0 : ROT1 == 1 ? (mb1 * 16) / 7 : ROT1 == 2 ? ((mb1 * 16) / 7 + 4 * (nb1 & 3)) & 15 : ROT1 == 3 ? (4 * (nb1 & 3)) & 15 : (((mb1 * ROTN1) / 7) * 16) / ROTN1;
  const unsigned rot2 = ROT2 == 0 ? 0 : ROT2 == 1 ? (mb2 * 8) % 14 : ROT2 == 2 ? (mb2 * 2 + ((nb2 & 3) * 7) / 2) % 14 : ROT2 == 3 ? ((nb2 & 3) * 7) / 2 : (((mb2 * ROTN2) / 7) * 14) / ROTN2;
  WStream W1, W2;
  const unsigned region = lds0 + w * REG_W;
  {
    const unsigned r8 = lane >> 3, pc = (lane & 7) ^ (r8 & 6);
    const unsigned q = (lane >> 2) & 3, p = lane & 3, row = 4 * g + q;
    const unsigned t0 = region + row * 128 + ((row >> 1) & 3) * 32 + p * 8;
    W1.srd = mk_srd(w1, S_ * F_ * 2);
    W2.srd = mk_srd(w2, g2 ? F_ * S_ * 2 : 0);
    W1.beg = w * (KW1 * 32) * F_ * 2 + nb1 * 128; W1.end = W1.beg + KW1 * 32 * F_ * 2; W1.cur = W1.beg + rot1 * (32 * F_ * 2);
    W2.beg = w * (KW2 * 32) * S_ * 2 + nb2 * 128; W2.end = W2.beg + KW2 * 32 * S_ * 2; W2.cur = W2.beg + rot2 * (32 * S_ * 2);
    W1.vo = r8 * F_ * 2 + pc * 16;
    W2.vo = r8 * S_ * 2 + pc * 16;
    W1.ring = W2.ring = region;
#pragma unroll
    for (int t = 0; t < 4; ++t) { W1.ta[t] = W2.ta[t] = t0 ^ (t << 5); }
  }
  constexpr int NT = 1;
  {
    const srd_t srdO = mk_srd(out, S_ * D_ * 2);
    asm volatile("buffer_load_dword v252, %0, %1, %2 offen" ::"v"(lane == 0 ? 0 : OOB), "s"(srdO), "s"((nb2 * 64 + 16 * w) * (D_ * 2) + mb2 * 128) : "memory");
  }
  if (g1) glds_slots<true, 0>(W1, make_iseq<PRE>{}); else waitv<0>();
  asm volatile("s_waitcnt vmcnt(%3)" : "+v"(ep), "+v"(xv[0]), "+v"(xv[1]) : "i"(4 * PRE + NT) : "memory");
  GAP();
  STAMP(1);
  if (g1) glds_slots<true, PRE>(W1, make_iseq<MID>{});
  GAP();
  unsigned tlo, thi;
  {
    const unsigned long long e = (((unsigned long long)ep[1] << 32) | ep[0]) + 1ull;
    tlo = rfl((unsigned)e); thi = rfl((unsigned)(e >> 32));
  }
  {
    float f[2][8], m[2], qs[2];
#pragma unroll
    for (int r = 0; r < 2; ++r) {
#pragma unroll
      for (int i = 0; i < 4; ++i) { f[r][2 * i] = bflo(xv[r][i]); f[r][2 * i + 1] = bfhi(xv[r][i]); }
      m[r] = ((f[r][0] + f[r][1]) + (f[r][2] + f[r][3])) + ((f[r][4] + f[r][5]) + (f[r][6] + f[r][7]));
    }
    wave_sum2(m[0], m[1]);
#pragma unroll
    for (int r = 0; r < 2; ++r) {
      const float mu = m[r] * (1.0f / D_);
#pragma unroll
      for (int i = 0; i < 8; ++i) f[r][i] -= mu;
      qs[r] = ((f[r][0] * f[r][0] + f[r][1] * f[r][1]) + (f[r][2] * f[r][2] + f[r][3] * f[r][3])) + ((f[r][4] * f[r][4] + f[r][5] * f[r][5]) + (f[r][6] * f[r][6] + f[r][7] * f[r][7]));
      qs[r] = lane < 56 ? qs[r] : 0.f;
    }
    wave_sum2(qs[0], qs[1]);
    v4u o[2];
#pragma unroll
    for (int r = 0; r < 2; ++r) {
      const float rs = __builtin_amdgcn_rsqf(qs[r] * (1.0f / D_) + 1e-5f);
      o[r] = v4u{pack2(f[r][0] * rs, f[r][1] * rs), pack2(f[r][2] * rs, f[r][3] * rs), pack2(f[r][4] * rs, f[r][5] * rs), pack2(f[r][6] * rs, f[r][7] * rs)};
    }
    if (lane < 56) {
      const unsigned ro = w * REG_W + LN_OFF + (w & 1) * 128 + lane * 16;
      *(LAS v4u*)(smem + ro) = o[0];
      *(LAS v4u*)(smem + ro + LNP) = o[1];
    }
  }
  wg_barrier();
  {
    v2u lo[2], hi[2];
    const unsigned q = (lane >> 2) & 3;
    const unsigned tlo_a = lds0 + (q >> 1) * (REG_W + 128) + LN_OFF + (q & 1) * LNP + g * 32 + (lane & 3) * 8 + w * 128, thi_a = tlo_a + 2 * REG_W;
    asm volatile("ds_read_b64_tr_b16 %0, %1 offset:0" : "=v"(lo[0]) : "v"(tlo_a) : "memory");
    asm volatile("ds_read_b64_tr_b16 %0, %1 offset:0" : "=v"(hi[0]) : "v"(thi_a) : "memory");
    asm volatile("ds_read_b64_tr_b16 %0, %1 offset:512" : "=v"(lo[1]) : "v"(tlo_a) : "memory");
    asm volatile("ds_read_b64_tr_b16 %0, %1 offset:512" : "=v"(hi[1]) : "v"(thi_a) : "memory");
    asm volatile("s_waitcnt lgkmcnt(0)" : "+v"(lo[0]), "+v"(lo[1]), "+v"(hi[0]), "+v"(hi[1])::"memory");
    const unsigned vo = g * HT + c * 16 + (b & 3) * 256;
    const unsigned so = H_OFF + (b >> 2) * KSTRIDE + w * 4 * HT;
    store16_sc1(v4u{lo[0][0], lo[0][1], hi[0][0], hi[0][1]}, vo, srdWS, so);
    store16_sc1(v4u{lo[1][0], lo[1][1], hi[1][0], hi[1][1]}, w < 3 ? vo : OOB, srdWS, so + 16 * HT);
    const unsigned vf = lane < R1 ? lane * 8192 + w * 2048 + b * 8 : OOB;
    if (!g1) {
      waitv<0>();
      store8_sc1(v2u{tlo, thi}, vf, srdWS, HF_OFF);
      STAMP(2);
    } else if (w == 0) {
      glds_slots<true, NB1>(W1, make_iseq<NA0>{});
      waitv<4 * NA0>();
      store8_sc1(v2u{tlo, thi}, vf, srdWS, HF_OFF);
      STAMP(2);
      glds_slots<true, NB1 + NA0>(W1, make_iseq<NS1 - NB1 - NA0>{});
      wpipe_stepsx<true, 0, 2, NA0>(W1, make_iseq<7>{});
    } else {
      glds_slots<true, NB1>(W1, make_iseq<NA>{});
      waitv<4 * NA>();
      store8_sc1(v2u{tlo, thi}, vf, srdWS, HF_OFF);
      STAMP(2);
      if (WSLEEP1) __builtin_amdgcn_s_sleep(WSLEEP1);
      glds_slots<true, NB1 + NA>(W1, make_iseq<NS1 - NB1 - NA>{});
      wpipe_stepsx<true, 0, 2, NA>(W1, make_iseq<7>{});
    }
  }
  Exch X;
  X.mine = (LAS unsigned char*)smem + w * REG_W + lane * 16;
#pragma unroll
  for (int j = 0; j < 3; ++j) X.from[j] = (const LAS unsigned char*)smem + ((w - 1 - j) & 3) * REG_W + j * 4096 + lane * 16;
  if (g1) {
    v2u bv[4];
    {
      const srd_t srdB1 = mk_srd(b1, F_ * 2);
      asm volatile("buffer_load_dwordx2 %0, %1, %2, %3 offen" : "=v"(bv[0]) : "v"(g * 8), "s"(srdB1), "s"(nb1 * 128u) : "memory");
      asm volatile("buffer_load_dwordx2 %0, %1, %2, %3 offen offset:32" : "=v"(bv[1]) : "v"(g * 8), "s"(srdB1), "s"(nb1 * 128u) : "memory");
      asm volatile("buffer_load_dwordx2 %0, %1, %2, %3 offen offset:64" : "=v"(bv[2]) : "v"(g * 8), "s"(srdB1), "s"(nb1 * 128u) : "memory");
      asm volatile("buffer_load_dwordx2 %0, %1, %2, %3 offen offset:96" : "=v"(bv[3]) : "v"(g * 8), "s"(srdB1), "s"(nb1 * 128u) : "memory");
    }
    GAP();
    AStream A;
    A.srd = srdWS;
    astream_init<KW1>(A, H_OFF + 4 * mb1 * HT + w * KW1 * KSTRIDE, rot1);
#pragma unroll
    for (int m = 0; m < 4; ++m) A.voff[m] = lane * 16 + ((m + w) & 3) * HT;
    W2Head H2;
    H2.srd = W2.srd; H2.ring = W2.ring;
#pragma unroll
    for (int i = 0; i < 4; ++i) H2.vo[i] = W2.vo + i * 8 * S_ * 2;
#pragma unroll
    for (int i = 0; i < TPF; ++i) { H2.cur[i] = W2.cur; cyc_add(W2.cur, 32 * S_ * 2, W2.beg, W2.end); }
    GAP();
    if (w == 0) {
      const unsigned so = HF_OFF + (b % R1) * 8192 + (mb1 & 3) * 2048;
      v4u f[2];
      poll2_issue<2>(f, lane * 16, srdWS, so);
      wtail_steps<true, 7, 6, NA0>(W1, make_iseq<KW1 - 7>{});
      STAMPX0(3);
      if (!poll2_check<2>(f, true, tlo, thi)) poll_flags2<PSLEEP, 2>(lane * 16, true, srdWS, so, tlo, thi);
    }
    asm volatile("s_barrier" ::: "memory");
    STAMPA(4);
    gemm_prologue<true>(A, make_iseq<DF1>{});
    if (w != 0) { wtail_steps<true, 7, 4 + DF1, NA>(W1, make_iseq<KW1 - 7>{}); STAMPX1(3); }
    GAP();
    asm volatile("s_waitcnt vmcnt(%4) lgkmcnt(0)" : "+v"(bv[0]), "+v"(bv[1]), "+v"(bv[2]), "+v"(bv[3]) : "i"(DF1) : "memory");
    f32x4 s[4];
#pragma unroll
    for (int t = 0; t < 4; ++t) { const v2u q = bv[t]; const unsigned q0 = q[0], q1 = q[1]; s[t] = f32x4{bflo(q0), bfhi(q0), bflo(q1), bfhi(q1)}; }
    GAP();
    gemm_body<true>(s, A, H2, X);
    STAMPA(5);
    if (!W2POS) glds_slots<false, TPF>(W2, make_iseq<NS2 - TPF>{});
    GAP();
#pragma unroll
    for (int kk = 0; kk < 2; ++kk) {
      const f32x4 s0 = s[2 * kk], s1 = s[2 * kk + 1];
      const v4u o = {pack2(gelu(s0[0]), gelu(s0[1])), pack2(gelu(s0[2]), gelu(s0[3])), pack2(gelu(s1[0]), gelu(s1[1])), pack2(gelu(s1[2]), gelu(s1[3]))};
      store16_sc1(o, lane * 16, srdWS, U_OFF + (4 * mb1 + w) * UT + (2 * nb1 + kk) * KSTRIDE);
    }
    GAP();
    if (W2POS) glds_slots<false, TPF>(W2, make_iseq<NS2 - TPF>{});
    wpipe_steps<false, 0, 1>(W2, make_iseq<POST2>{});
    waitv<4 * POST2 + (W2POS ? 4 * (NS2 - TPF) : 0)>();
    store8_sc1(v2u{tlo, thi}, lane < R2 ? lane * UF_REP + (mb1 * 4 + nb1 / 7) * 256 + ((nb1 % 7) * 4 + w) * 8 : OOB, srdWS, UF_OFF);
    STAMP(6);
    if (!g2) {
      if (b == EPOCH_WG && w == 1) store8_sc1(v2u{tlo, thi}, lane == 0 ? 0 : OOB, srdWS, EP_OFF);
      waitv<0>();
      goto done;
    }
    if (w != 0) __builtin_amdgcn_s_sleep(WSLEEP2);
    wpipe_steps<false, POST2, 2>(W2, make_iseq<NS2 - POST2>{});
  } else {
    wg_barrier();
    if (!g2) goto done;
#pragma unroll 1
    for (int i = 0; i < IDLE_SLEEP; ++i) __builtin_amdgcn_s_sleep(16);
    glds_slots<false, 0>(W2, make_iseq<NS2>{});
    wpipe_steps<false, 0, 0>(W2, make_iseq<KW2>{});
    if (w == 0) poll_flags<8>(0, srdWS, EP_OFF, tlo, thi);
    wg_barrier();
  }
  {
    unsigned bv[4];
    {
      const srd_t srdB2 = mk_srd(b2, S_ * 2);
      asm volatile("buffer_load_ushort %0, %1, %2, %3 offen" : "=v"(bv[0]) : "v"(c * 2), "s"(srdB2), "s"(nb2 * 128u) : "memory");
      asm volatile("buffer_load_ushort %0, %1, %2, %3 offen offset:32" : "=v"(bv[1]) : "v"(c * 2), "s"(srdB2), "s"(nb2 * 128u) : "memory");
      asm volatile("buffer_load_ushort %0, %1, %2, %3 offen offset:64" : "=v"(bv[2]) : "v"(c * 2), "s"(srdB2), "s"(nb2 * 128u) : "memory");
      asm volatile("buffer_load_ushort %0, %1, %2, %3 offen offset:96" : "=v"(bv[3]) : "v"(c * 2), "s"(srdB2), "s"(nb2 * 128u) : "memory");
    }
    GAP();
    AStream A;
    A.srd = srdWS;
    astream_init<KW2>(A, U_OFF + 4 * mb2 * UT + w * KW2 * KSTRIDE, rot2);
#pragma unroll
    for (int m = 0; m < 4; ++m) A.voff[m] = lane * 16 + ((m + w) & 3) * UT;
    GAP();
    if (w == 0) {
      const unsigned so = UF_OFF + (nb2 % R2) * UF_REP + mb2 * 1024;
      v4u f[2];
      poll2_issue<1>(f, lane * 16, srdWS, so);
      if (g1) wtail_steps<false, NS2, 5, NA>(W2, make_iseq<KW2 - NS2>{});
      if (!poll2_check<1>(f, c < 14, tlo, thi)) poll_flags2<PSLEEP, 1>(lane * 16, c < 14, srdWS, so, tlo, thi);
    }
    asm volatile("s_barrier" ::: "memory");
    STAMPB(4);
    gemm_prologue<false>(A, make_iseq<DF2>{});
    if (w != 0 && g1) wtail_steps<false, NS2, 4 + DF2, NA>(W2, make_iseq<KW2 - NS2>{});
    STAMPB(3);
    GAP();
    asm volatile("s_waitcnt vmcnt(%4) lgkmcnt(0)" : "+v"(bv[0]), "+v"(bv[1]), "+v"(bv[2]), "+v"(bv[3]) : "i"(DF2) : "memory");
    f32x4 s[4];
#pragma unroll
    for (int t = 0; t < 4; ++t) { const float bb = bflo(bv[t]); s[t] = f32x4{bb, bb, bb, bb}; }
    GAP();
    W2Head H2;
    gemm_body<false>(s, A, H2, X);
    STAMPB(5);
    unsigned char* stage = smem + W2_OFF;
#pragma unroll
    for (int t = 0; t < 4; ++t) *(LAS v2u*)((LAS unsigned char*)stage + (16 * t + c) * 144 + 32 * w + 8 * g) = v2u{pack2(s[t][0], s[t][1]), pack2(s[t][2], s[t][3])};
    wg_barrier();
    const srd_t srdO = mk_srd(out, S_ * D_ * 2);
    const unsigned row = 16 * w + (lane >> 3), col = (lane & 7) * 16;
#pragma unroll
    for (int mp = 0; mp < 2; ++mp) {
      const v4u o = *(const LAS v4u*)((LAS unsigned char*)stage + (row + 8 * mp) * 144 + col);
      asm volatile("buffer_store_dwordx4 %0, %1, %2, %3 offen\n\ts_nop 1" ::"v"(o), "v"((row + 8 * mp) * (D_ * 2) + col), "s"(srdO), "s"(nb2 * 64 * (D_ * 2) + mb2 * 128) : "memory");
    }
  }
done:
  STAMP(7);
}

extern "C" void kernel_launch(void* const* d_in, const int* in_sizes, int n_in, void* d_out, int out_size, void* d_ws, size_t ws_size, hipStream_t stream) {
  mixer_kernel<<<256, 256, 0, stream>>>((const u16*)d_in[0], (const u16*)d_in[1], (const u16*)d_in[2], (const u16*)d_in[3], (const u16*)d_in[4],
                                       (u16*)d_out, (unsigned char*)d_ws);
}
```

```cpp
#include <hip/hip_runtime.h>
#include <stdint.h>
#include <utility>

typedef unsigned v4u __attribute__((ext_vector_type(4)));
typedef unsigned v2u __attribute__((ext_vector_type(2)));
typedef float f32x4 __attribute__((ext_vector_type(4)));
typedef unsigned short u16;
typedef __amdgpu_buffer_rsrc_t srd_t;
#define DI __device__ __forceinline__
#define LAS __attribute__((address_space(3)))
#define GAP() __builtin_amdgcn_sched_barrier(0)
template <int... Is> using iseq = std::integer_sequence<int, Is...>;
template <int N> using make_iseq = std::make_integer_sequence<int, N>;
#define ACLOB "a0","a1","a2","a3","a4","a5","a6","a7","a8","a9","a10","a11","a12","a13","a14","a15","a16","a17","a18","a19","a20","a21","a22","a23","a24","a25","a26","a27","a28","a29","a30","a31","a32","a33","a34","a35","a36","a37","a38","a39","a40","a41","a42","a43","a44","a45","a46","a47","a48","a49","a50","a51","a52","a53","a54","a55","a56","a57","a58","a59","a60","a61","a62","a63","a64","a65","a66","a67","a68","a69","a70","a71","a72","a73","a74","a75","a76","a77","a78","a79","a80","a81","a82","a83","a84","a85","a86","a87","a88","a89","a90","a91","a92","a93","a94","a95","a96","a97","a98","a99","a100","a101","a102","a103","a104","a105","a106","a107","a108","a109","a110","a111","a112","a113","a114","a115","a116","a117","a118","a119","a120","a121","a122","a123","a124","a125","a126","a127","a128","a129","a130","a131","a132","a133","a134","a135","a136","a137","a138","a139","a140","a141","a142","a143","a144","a145","a146","a147","a148","a149","a150","a151","a152","a153","a154","a155","a156","a157","a158","a159","a160","a161","a162","a163","a164","a165","a166","a167","a168","a169","a170","a171","a172","a173","a174","a175","a176","a177","a178","a179","a180","a181","a182","a183","a184","a185","a186","a187","a188","a189","a190","a191","a192","a193","a194","a195","a196","a197","a198","a199","a200","a201","a202","a203","a204","a205","a206","a207","a208","a209","a210","a211","a212","a213","a214","a215","a216","a217","a218","a219","a220","a221","a222","a223","a224","a225","a226","a227","a228","a229","a230","a231","a232","a233","a234","a235","a236","a237","a238","a239","a240","a241","a242","a243","a244","a245","a246","a247","a248","a249","a250","a251","a252","a253","a254","a255"

#define ACL0 "a0","a1","a2","a3","a4","a5","a6","a7","a8","a9","a10","a11","a12","a13","a14","a15","a16","a17","a18","a19","a20","a21","a22","a23","a24","a25","a26","a27","a28","a29","a30","a31","a32","a33","a34","a35","a36","a37","a38","a39","a40","a41","a42","a43","a44","a45","a46","a47","a48","a49","a50","a51","a52","a53","a54","a55","a56","a57","a58","a59","a60","a61","a62","a63"
#define ACL1 "a64","a65","a66","a67","a68","a69","a70","a71","a72","a73","a74","a75","a76","a77","a78","a79","a80","a81","a82","a83","a84","a85","a86","a87","a88","a89","a90","a91","a92","a93","a94","a95","a96","a97","a98","a99","a100","a101","a102","a103","a104","a105","a106","a107","a108","a109","a110","a111","a112","a113","a114","a115","a116","a117","a118","a119","a120","a121","a122","a123","a124","a125","a126","a127"
#define ACL2 "a128","a129","a130","a131","a132","a133","a134","a135","a136","a137","a138","a139","a140","a141","a142","a143","a144","a145","a146","a147","a148","a149","a150","a151","a152","a153","a154","a155","a156","a157","a158","a159","a160","a161","a162","a163","a164","a165","a166","a167","a168","a169","a170","a171","a172","a173","a174","a175","a176","a177","a178","a179","a180","a181","a182","a183","a184","a185","a186","a187","a188","a189","a190","a191"
#define ACL3 "a192","a193","a194","a195","a196","a197","a198","a199","a200","a201","a202","a203","a204","a205","a206","a207","a208","a209","a210","a211","a212","a213","a214","a215","a216","a217","a218","a219","a220","a221","a222","a223","a224","a225","a226","a227","a228","a229","a230","a231","a232","a233","a234","a235","a236","a237","a238","a239","a240","a241","a242","a243","a244","a245","a246","a247","a248","a249","a250","a251","a252","a253","a254","a255"

#define TOUCH 5
#define MAPPING 3
#define DF1 16
#define DF2 16
#define HB 5
#define W2POS 1
#define PRE 3
#define MID 0
#define NA 3
#define NA0 3
#define EARLYPOLL 1
#define POST2 0
#define ROT1 6
#define ROT2 1
#define ROTN1 2
#define ROTN2 2
#define IDLE_SLEEP 0
#define PSLEEP 2
#define QUIET 1
#define WSLEEP1 10
#define WSLEEP2 16
#define R1 4
#define R2 4

constexpr int S_ = 2048, D_ = 448, F_ = 1792;
constexpr int KW1 = 16, KW2 = 14;
constexpr int NS1 = 9, NS2 = 7;
#define TPFMAX 6
constexpr int TPF = DF1 / 4 < TPFMAX ? DF1 / 4 : TPFMAX;
constexpr unsigned KSTRIDE = 1024u;
constexpr unsigned HT = 64u * KSTRIDE, UT = 56u * KSTRIDE;
constexpr unsigned H_OFF = 0, U_OFF = 28u * HT;
constexpr unsigned EP_OFF = 4u << 20, HF_OFF = EP_OFF + 4096u, UF_OFF = HF_OFF + 16u * 8192u;
constexpr unsigned UF_REP = 28u * 256u;
constexpr unsigned WS_USED = UF_OFF + 16u * UF_REP;
constexpr unsigned REG_W = 40960, LN_OFF = 36864, LNP = 960, W2_OFF = 12288, LDS_BYTES = 4 * REG_W;
constexpr unsigned OOB = 0x7ffffff0u;
constexpr int SPIN_MAX = 1 << 15;
constexpr unsigned EPOCH_WG = 225;

DI unsigned rfl(unsigned x) { return __builtin_amdgcn_readfirstlane(x); }
DI float bflo(unsigned w) { return __builtin_bit_cast(float, w << 16); }
DI float bfhi(unsigned w) { return __builtin_bit_cast(float, w & 0xffff0000u); }
DI unsigned pack2(float lo, float hi) {
  typedef __bf16 bf2 __attribute__((ext_vector_type(2)));
  typedef float f2 __attribute__((ext_vector_type(2)));
  f2 v = {lo, hi};
  return __builtin_bit_cast(unsigned, __builtin_convertvector(v, bf2));
}
DI srd_t mk_srd(const void* p, unsigned bytes) { return __builtin_amdgcn_make_buffer_rsrc((void*)p, 0, bytes, 0x00020000); }
template <int C> DI float dpp_add(float v) {
  return v + __builtin_bit_cast(float, __builtin_amdgcn_update_dpp(0, __builtin_bit_cast(int, v), C, 0xf, 0xf, true));
}
DI void wave_sum2(float& a, float& b) {
  a = dpp_add<0xB1>(a); b = dpp_add<0xB1>(b); a = dpp_add<0x4E>(a); b = dpp_add<0x4E>(b);
  a = dpp_add<0x141>(a); b = dpp_add<0x141>(b); a = dpp_add<0x140>(a); b = dpp_add<0x140>(b);
  auto p = __builtin_amdgcn_permlane16_swap(__builtin_bit_cast(unsigned, a), __builtin_bit_cast(unsigned, b), false, false);
  const unsigned p0 = p[0], p1 = p[1];
  float s = __builtin_bit_cast(float, p0) + __builtin_bit_cast(float, p1);
  auto q = __builtin_amdgcn_permlane32_swap(__builtin_bit_cast(unsigned, s), __builtin_bit_cast(unsigned, s), false, false);
  const unsigned q0 = q[0], q1 = q[1];
  s = __builtin_bit_cast(float, q0) + __builtin_bit_cast(float, q1);
  a = __builtin_bit_cast(float, __builtin_amdgcn_readlane(__builtin_bit_cast(int, s), 0));
  b = __builtin_bit_cast(float, __builtin_amdgcn_readlane(__builtin_bit_cast(int, s), 16));
}
template <int N> DI void waitv() { static_assert(N >= 0 && N < 64, "vmcnt"); asm volatile("s_waitcnt vmcnt(%0)" ::"i"(N) : "memory"); }
template <int N> DI void waitv_b() { static_assert(N >= 0 && N < 64, "vmcnt"); __builtin_amdgcn_s_waitcnt((N & 15) | ((N >> 4) << 14) | 0x0F70); }
DI void wait_lds() { asm volatile("s_waitcnt lgkmcnt(0)" ::: "memory"); }
DI void wg_barrier() { asm volatile("s_waitcnt lgkmcnt(0)\n\ts_barrier" ::: "memory"); }

struct WStream {
  srd_t srd;
  unsigned cur, beg, end;
  unsigned vo;
  unsigned ring;
  unsigned ta[4];
};
DI void cyc_add(unsigned& cur, unsigned inc, unsigned beg, unsigned end) {
  asm volatile("s_add_u32 %0, %0, %1\n\ts_cmp_eq_u32 %0, %3\n\ts_cselect_b32 %0, %2, %0" : "+s"(cur) : "s"(inc), "s"(beg), "s"(end) : "scc");
}
template <bool W1> struct WP { static constexpr int LDW = W1 ? F_ : S_, KW = W1 ? KW1 : KW2, NS = W1 ? NS1 : NS2, OFF = W1 ? 0 : (int)W2_OFF; };
template <bool W1, int KSTEP> DI void glds_slot(WStream& s) {
  using P = WP<W1>;
  unsigned t;
  asm volatile("s_add_u32 m0, %2, %3\n\ts_add_u32 %0, %1, 0\n\tbuffer_load_dwordx4 %5, %6, %0 offen lds\n\t"
               "s_add_u32 m0, %2, %3+1024\n\ts_add_u32 %0, %1, %4\n\tbuffer_load_dwordx4 %5, %6, %0 offen lds\n\t"
               "s_add_u32 m0, %2, %3+2048\n\ts_add_u32 %0, %1, 2*%4\n\tbuffer_load_dwordx4 %5, %6, %0 offen lds\n\t"
               "s_add_u32 m0, %2, %3+3072\n\ts_add_u32 %0, %1, 3*%4\n\tbuffer_load_dwordx4 %5, %6, %0 offen lds\n\t"
               "s_add_u32 %1, %1, 4*%4\n\ts_cmp_eq_u32 %1, %8\n\ts_cselect_b32 %1, %7, %1"
               : "=&s"(t), "+s"(s.cur)
               : "s"(s.ring), "i"(P::OFF + (KSTEP % P::NS) * 4096), "i"(8 * P::LDW * 2), "v"(s.vo), "s"(s.srd), "s"(s.beg), "s"(s.end)
               : "scc", "memory");
}
template <bool W1, int KSTEP> DI void tr_slot(const WStream& s) {
  using P = WP<W1>;
  asm volatile("ds_read_b64_tr_b16 a[%c4:%c4+1], %0 offset:%c5\n\tds_read_b64_tr_b16 a[%c4+2:%c4+3], %0 offset:%c5+2048\n\t"
               "ds_read_b64_tr_b16 a[%c4+4:%c4+5], %1 offset:%c5\n\tds_read_b64_tr_b16 a[%c4+6:%c4+7], %1 offset:%c5+2048\n\t"
               "ds_read_b64_tr_b16 a[%c4+8:%c4+9], %2 offset:%c5\n\tds_read_b64_tr_b16 a[%c4+10:%c4+11], %2 offset:%c5+2048\n\t"
               "ds_read_b64_tr_b16 a[%c4+12:%c4+13], %3 offset:%c5\n\tds_read_b64_tr_b16 a[%c4+14:%c4+15], %3 offset:%c5+2048"
               ::"v"(s.ta[0]), "v"(s.ta[1]), "v"(s.ta[2]), "v"(s.ta[3]), "i"(16 * KSTEP), "i"(P::OFF + (KSTEP % P::NS) * 4096) : "memory", ACLOB);
}
constexpr int NB1 = PRE + MID;
static_assert(NB1 + NA <= NS1 && NB1 + NA0 <= NS1 && POST2 <= NS2, "schedule");
template <bool W1, int NAX> constexpr int younger(int j, bool flagged) {
  constexpr int KW = W1 ? KW1 : KW2, NS = W1 ? NS1 : NS2;
  const int before_stores = W1 ? NB1 : (W2POS ? TPF : NS2), before_flag = W1 ? NB1 + NAX : NS2 + POST2;
  const int last = j + NS - 1 < KW - 1 ? j + NS - 1 : KW - 1;
  return 4 * (last - j) + (j < before_stores ? 2 : 0) + (flagged && j < before_flag ? 1 : 0);
}
template <bool W1, int J, int MODE, int EXTRA, int NAX> DI void wpipe_step(WStream& s) {
  using P = WP<W1>;
  constexpr int rem = P::KW - 1 - J;
  GAP();
  waitv_b<(MODE == 0 ? 4 * (rem < P::NS - 1 ? rem : P::NS - 1) : younger<W1, NAX>(J, MODE == 2)) + EXTRA>();
  GAP();
  tr_slot<W1, J>(s);
  if constexpr (J + P::NS < P::KW) { wait_lds(); glds_slot<W1, J + P::NS>(s); }
  GAP();
}
template <bool W1, int J0, int MODE, int NAX, int... Js> DI void wpipe_stepsx(WStream& s, iseq<Js...>) { (wpipe_step<W1, J0 + Js, MODE, 0, NAX>(s), ...); }
template <bool W1, int J0, int MODE, int... Js> DI void wpipe_steps(WStream& s, iseq<Js...>) { (wpipe_step<W1, J0 + Js, MODE, 0, NA>(s), ...); }
template <bool W1, int J0, int EXTRA, int NAX, int... Js> DI void wtail_steps(WStream& s, iseq<Js...>) { (wpipe_step<W1, J0 + Js, 2, EXTRA, NAX>(s), ...); }
template <bool W1, int J0, int... Js> DI void glds_slots(WStream& s, iseq<Js...>) { (glds_slot<W1, J0 + Js>(s), ...); }

constexpr int RB = 160;
static_assert(RB + 4 * DF1 <= 256 && RB + 4 * DF2 <= 256, "ring");
template <int REG> DI void act_load(unsigned voff, srd_t srd, unsigned soff) {
  asm volatile("buffer_load_dwordx4 v[%c0:%c1], %2, %3, %4 offen sc1" ::"i"(REG), "i"(REG + 3), "v"(voff), "s"(srd), "s"(soff) : "memory");
}
struct AStream { srd_t srd; unsigned voff[4], so[16]; };
template <int KW> DI void astream_init(AStream& a, unsigned rot) {
#pragma unroll
  for (int k = 0; k < KW; ++k) {
    const unsigned t = (rot + k) * KSTRIDE;
    if constexpr (KW == 16) a.so[k] = t & (16 * KSTRIDE - 1);
    else { const unsigned t2 = t - KW * KSTRIDE; a.so[k] = t < t2 ? t : t2; }
    asm volatile("" : "+s"(a.so[k]));
  }
}
struct W2Head { unsigned vo[4], cur[8], ring; srd_t srd; };
#define MFMA_ASM(CL)                                                                                                                                       \
  if constexpr (WA) {                                                                                                                                      \
    if constexpr (ZERO) asm volatile("v_mfma_f32_16x16x32_bf16 %0, a[%c1:%c2], v[%c3:%c4], 0" : "=&v"(acc) : "i"(AR), "i"(AR + 3), "i"(VR), "i"(VR + 3) : CL); \
    else asm volatile("v_mfma_f32_16x16x32_bf16 %0, a[%c1:%c2], v[%c3:%c4], %0" : "+v"(acc) : "i"(AR), "i"(AR + 3), "i"(VR), "i"(VR + 3) : CL);               \
  } else {                                                                                                                                                 \
    if constexpr (ZERO) asm volatile("v_mfma_f32_16x16x32_bf16 %0, v[%c3:%c4], a[%c1:%c2], 0" : "=&v"(acc) : "i"(AR), "i"(AR + 3), "i"(VR), "i"(VR + 3) : CL); \
    else asm volatile("v_mfma_f32_16x16x32_bf16 %0, v[%c3:%c4], a[%c1:%c2], %0" : "+v"(acc) : "i"(AR), "i"(AR + 3), "i"(VR), "i"(VR + 3) : CL);               \
  }
template <bool WA, bool ZERO, int AR, int VR, int Q> DI void mfma(f32x4& acc) {
  if constexpr (Q == 0) { MFMA_ASM(ACL0) } else if constexpr (Q == 1) { MFMA_ASM(ACL1) } else if constexpr (Q == 2) { MFMA_ASM(ACL2) } else { MFMA_ASM(ACL3) }
}
template <bool G1> constexpr int vop(int v) { return v < 4 * (G1 ? KW1 : KW2) ? 1 : (G1 && v - 4 * KW1 < 4 * TPF ? 1 : 0); }
template <bool G1> constexpr int vop_range(int a, int b) { int n = 0; for (int v = a; v <= b; ++v) n += vop<G1>(v); return n; }
template <bool G1, int V> DI void act_op(const AStream& a) {
  constexpr int KW = G1 ? KW1 : KW2, DF = G1 ? DF1 : DF2;
  act_load<RB + 4 * (V % DF)>(a.voff[(V / KW + 1) & 3], a.srd, a.so[V % KW]);
}
template <int P> DI void w2_m0(const W2Head& h) {
  asm volatile("s_add_u32 m0, %0, %1" ::"s"(h.ring), "i"((int)W2_OFF + ((P / 4) % NS2) * 4096 + (P % 4) * 1024) : "scc", "memory");
}
template <int P> DI void w2_load(const W2Head& h) {
  asm volatile("buffer_load_dwordx4 %0, %1, %2 offen lds" ::"v"(h.vo[P % 4]), "s"(h.srd), "s"(h.cur[P / 4]) : "memory");
}
struct Exch { LAS unsigned char* mine; const LAS unsigned char* from[3]; };
template <bool G1> constexpr bool has_piece(int i) { constexpr int KW = G1 ? KW1 : KW2, DF = G1 ? DF1 : DF2; return i >= 1 && i - 1 + DF >= 4 * KW && vop<G1>(i - 1 + DF) != 0; }
template <bool G1> constexpr int xcap(int i, int g) {
  constexpr int KW = G1 ? KW1 : KW2;
  const int j = i - 3 * KW;
  if (j < HB || (j == HB && g == 0)) return 0;
  return 2 - (g == 3 ? 1 : 0) - (has_piece<G1>(i) && g < 2 ? 1 : 0);
}
template <bool G1> constexpr int xfirst(int i, int g) {
  constexpr int KW = G1 ? KW1 : KW2;
  int n = 0;
  for (int ii = 3 * KW; ii <= i; ++ii)
    for (int gg = 0; gg < 4 && (ii < i || gg < g); ++gg) n += xcap<G1>(ii, gg);
  return n;
}
constexpr int NX = 12 + 1 + 32;
static_assert(xfirst<true>(4 * KW1, 0) >= NX && xfirst<false>(4 * KW2, 0) >= NX, "exchange does not fit");
struct XRegs { f32x4 r[3][4]; float a[4][4], s[4][4]; };
DI float addf(float a, float b) { float d; asm volatile("v_add_f32 %0, %1, %2" : "=v"(d) : "v"(a), "v"(b)); return d; }
template <int N> DI void xop(XRegs& r, const Exch& x) {
  if constexpr (N < 12) r.r[N / 4][N % 4] = *(const LAS f32x4*)(x.from[N / 4] + (N % 4) * 1024);
  else if constexpr (N == 12) __builtin_amdgcn_s_waitcnt(0xC07F);
  else if constexpr (N < 29) { constexpr int t = (N - 13) / 4, e = (N - 13) % 4; const float p = r.r[0][t][e], q = r.r[1][t][e]; r.a[t][e] = addf(p, q); }
  else if constexpr (N < NX) { constexpr int t = (N - 29) / 4, e = (N - 29) % 4; const float p = r.r[2][t][e]; r.s[t][e] = addf(r.a[t][e], p); }
}
template <bool G1, int I, int G> DI void slot(f32x4 (&acc)[4][4], XRegs& r, const AStream& a, const W2Head& h, const Exch& x) {
  constexpr int KW = G1 ? KW1 : KW2, DF = G1 ? DF1 : DF2, V = I - 1 + DF;
  GAP();
  if constexpr (I >= 1 && V < 4 * KW) { if constexpr (G == 0) act_op<G1, V>(a); }
  else if constexpr (has_piece<G1>(I)) {
    if constexpr (G == 0) w2_m0<V - 4 * KW>(h);
    if constexpr (G == 1) w2_load<V - 4 * KW>(h);
  }
  if constexpr (I % KW == 1 && I > KW) {
    constexpr int m = I / KW;
    asm volatile("" : "+v"(acc[m][G]));
    *(LAS f32x4*)(x.mine + (m - 1) * 4096 + G * 1024) = acc[m][G];
  }
  if constexpr (I == 3 * KW + HB && G == 0) wg_barrier();
  constexpr int n0 = xfirst<G1>(I, G), cap = xcap<G1>(I, G);
  if constexpr (cap >= 1 && n0 < NX) xop<n0>(r, x);
  if constexpr (cap >= 2 && n0 + 1 < NX) xop<n0 + 1>(r, x);
  GAP();
}
template <bool G1, int I> DI void gemm_frag(f32x4 (&acc)[4][4], XRegs& r, const AStream& a, const W2Head& h, const Exch& x) {
  constexpr int KW = G1 ? KW1 : KW2, DF = G1 ? DF1 : DF2, K = I % KW, M = (I / KW + 1) & 3, VR = RB + 4 * (I % DF);
  constexpr bool Z = K == 0 && M != 0;
  waitv_b<I == 0 ? vop_range<G1>(1, DF - 1) : vop_range<G1>(I + 1, I - 2 + DF)>();
  GAP();
  mfma<G1, Z, 16 * K + 0, VR, 0>(acc[M][0]);
  slot<G1, I, 0>(acc, r, a, h, x);
  mfma<G1, Z, 16 * K + 4, VR, 1>(acc[M][1]);
  slot<G1, I, 1>(acc, r, a, h, x);
  mfma<G1, Z, 16 * K + 8, VR, 2>(acc[M][2]);
  slot<G1, I, 2>(acc, r, a, h, x);
  mfma<G1, Z, 16 * K + 12, VR, 3>(acc[M][3]);
  slot<G1, I, 3>(acc, r, a, h, x);
}
template <bool G1, int... Is> DI void gemm_frags(f32x4 (&acc)[4][4], XRegs& r, const AStream& a, const W2Head& h, const Exch& x, iseq<Is...>) {
  (gemm_frag<G1, Is>(acc, r, a, h, x), ...);
}
template <bool G1, int... Vs> DI void gemm_prologue(const AStream& a, iseq<Vs...>) { (act_op<G1, Vs>(a), ...); GAP(); }
template <bool G1> DI void gemm_body(f32x4 (&pre)[4], const AStream& a, const W2Head& h, const Exch& x) {
  constexpr int KW = G1 ? KW1 : KW2, DF = G1 ? DF1 : DF2, VL = 4 * KW - 1 + DF;
  f32x4 acc[4][4];
  XRegs r;
#pragma unroll
  for (int t = 0; t < 4; ++t) acc[0][t] = pre[t];
  asm volatile("" : "+v"(acc[0][0]), "+v"(acc[0][1]), "+v"(acc[0][2]), "+v"(acc[0][3]));
  GAP();
  gemm_frags<G1>(acc, r, a, h, x, make_iseq<4 * KW>{});
  if constexpr (vop<G1>(VL) != 0) {
    w2_m0<VL - 4 * KW>(h);
    asm volatile("s_nop 0");
    w2_load<VL - 4 * KW>(h);
  }
  asm volatile("s_nop 7" : "+v"(acc[0][0]), "+v"(acc[0][1]), "+v"(acc[0][2]), "+v"(acc[0][3]));
#pragma unroll
  for (int t = 0; t < 4; ++t) pre[t] = acc[0][t] + f32x4{r.s[t][0], r.s[t][1], r.s[t][2], r.s[t][3]};
}
template <int SLEEP> DI void poll_flags(unsigned voff, srd_t srd, unsigned soff, unsigned tlo, unsigned thi) {
#pragma unroll 1
  for (int spin = 0; spin < SPIN_MAX; ++spin) {
    v2u f;
    asm volatile("buffer_load_dwordx2 %0, %1, %2, %3 offen sc1\n\ts_waitcnt vmcnt(0)" : "=v"(f) : "v"(voff), "s"(srd), "s"(soff) : "memory");
    if (__builtin_amdgcn_ballot_w64(f[0] != tlo || f[1] != thi) == 0) break;
    __builtin_amdgcn_s_sleep(SLEEP);
  }
}
template <int N> DI void poll2_issue(v4u (&f)[2], unsigned voff, srd_t srd, unsigned soff) {
  asm volatile("buffer_load_dwordx4 %0, %1, %2, %3 offen sc1" : "=v"(f[0]) : "v"(voff), "s"(srd), "s"(soff) : "memory");
  if constexpr (N == 2) asm volatile("buffer_load_dwordx4 %0, %1, %2, %3 offen offset:1024 sc1" : "=v"(f[1]) : "v"(voff), "s"(srd), "s"(soff) : "memory");
}
template <int N> DI bool poll2_check(v4u (&f)[2], bool use, unsigned tlo, unsigned thi) {
  if constexpr (N == 2) asm volatile("s_waitcnt vmcnt(0)" : "+v"(f[0]), "+v"(f[1])::"memory");
  else asm volatile("s_waitcnt vmcnt(0)" : "+v"(f[0])::"memory");
  const v4u a = f[0];
  bool bad = a[0] != tlo || a[1] != thi || a[2] != tlo || a[3] != thi;
  if constexpr (N == 2) { const v4u c = f[1]; bad = bad || c[0] != tlo || c[1] != thi || c[2] != tlo || c[3] != thi; }
  return __builtin_amdgcn_ballot_w64(bad && use) == 0;
}
template <int SLEEP, int N> DI void poll_flags2(unsigned voff, bool use, srd_t srd, unsigned soff, unsigned tlo, unsigned thi) {
#pragma unroll 1
  for (int spin = 0; spin < SPIN_MAX; ++spin) {
    v4u f[2];
    poll2_issue<N>(f, voff, srd, soff);
    if (poll2_check<N>(f, use, tlo, thi)) break;
    __builtin_amdgcn_s_sleep(SLEEP);
  }
}
DI void store16_sc1(v4u d, unsigned voff, srd_t srd, unsigned soff) {
  asm volatile("buffer_store_dwordx4 %0, %1, %2, %3 offen sc1\n\ts_nop 1" ::"v"(d), "v"(voff), "s"(srd), "s"(soff) : "memory");
}
DI void store8_sc1(v2u d, unsigned voff, srd_t srd, unsigned soff) {
  asm volatile("buffer_store_dwordx2 %0, %1, %2, %3 offen sc1\n\ts_nop 1" ::"v"(d), "v"(voff), "s"(srd), "s"(soff) : "memory");
}
DI float gelu(float u) {
  const float e = __builtin_amdgcn_exp2f(u * (-2.3022082f - 0.1029432f * u * u));
  return u * __builtin_amdgcn_rcpf(1.0f + e);
}
#define PA(i)
#define PB(i)
#define STAMP(i)
#define STAMPA(i)
#define STAMPB(i)
#define STAMPX0(i)
#define STAMPX1(i)

__global__ void __launch_bounds__(256, 1) __attribute__((amdgpu_num_vgpr(RB)))
mixer_kernel(const u16* __restrict__ x, const u16* __restrict__ w1, const u16* __restrict__ b1, const u16* __restrict__ w2, const u16* __restrict__ b2,
             u16* __restrict__ out, unsigned char* __restrict__ ws) {
  __shared__ __attribute__((aligned(1024))) unsigned char smem[LDS_BYTES];
  STAMP(0);
  asm volatile("" ::: ACLOB);
  asm volatile("" ::: "v160", "v255");
  const unsigned tid = threadIdx.x, lane = tid & 63, w = rfl(tid >> 6), b = blockIdx.x;
  const unsigned lds0 = (unsigned)(uintptr_t)(LAS unsigned char*)smem;
  const srd_t srdWS = mk_srd(ws, WS_USED);
  const unsigned g = lane >> 4, c = lane & 15;

  v2u ep; v4u xv[2];
  {
    const srd_t srdX = mk_srd(x, S_ * D_ * 2);
    const unsigned zero = 0, vx = lane < 56 ? lane * 16 : OOB, row = 32 * (b >> 2) + 4 * (b & 3) + 2 * (w & 1) + 16 * (w >> 1);
    asm volatile("buffer_load_dwordx4 %0, %1, %2, %3 offen" : "=v"(xv[0]) : "v"(vx), "s"(srdX), "s"(row * 896u) : "memory");
    asm volatile("buffer_load_dwordx4 %0, %1, %2, %3 offen offset:896" : "=v"(xv[1]) : "v"(vx), "s"(srdX), "s"(row * 896u) : "memory");
    asm volatile("buffer_load_dwordx2 %0, %1, %2, %3 offen sc1" : "=v"(ep) : "v"(zero), "s"(srdWS), "s"(EP_OFF) : "memory");
  }
  GAP();
  const unsigned xcd = b & 7, li = b >> 3;
  bool g1, g2;
  unsigned mb1, nb1, mb2, nb2, nq1;
  {
    const unsigned j = xcd >> 1, odd = xcd & 1, i0 = 7 + odd;
    g1 = li >= i0;
    const unsigned idx = g1 ? li - i0 + (odd ? 25 : 0) : 0;
    nq1 = (idx * 37) >> 8;
    nb1 = 7 * j + nq1; mb1 = idx - 7 * nq1;
    g2 = li < 28; nb2 = 4 * xcd + (li & 3); mb2 = g2 ? li >> 2 : 0;
  }
  const unsigned rot1 = ((nq1 == 3 ? 0xa50c840u : 0xdb96420u) >> (4 * mb1)) & 15;
  const unsigned rot2 = (0x6c4a280u >> (4 * mb2)) & 15;
  WStream W1, W2;
  const unsigned region = lds0 + w * REG_W;
  {
    const unsigned r8 = lane >> 3, pc = (lane & 7) ^ (r8 & 6);
    const unsigned q = (lane >> 2) & 3, p = lane & 3, row = 4 * g + q;
    const unsigned t0 = region + row * 128 + ((row >> 1) & 3) * 32 + p * 8;
    W1.srd = mk_srd(w1, S_ * F_ * 2);
    W2.srd = mk_srd(w2, g2 ? F_ * S_ * 2 : 0);
    W1.beg = w * (KW1 * 32) * F_ * 2 + nb1 * 128; W1.end = W1.beg + KW1 * 32 * F_ * 2; W1.cur = W1.beg + rot1 * (32 * F_ * 2);
    W2.beg = w * (KW2 * 32) * S_ * 2 + nb2 * 128; W2.end = W2.beg + KW2 * 32 * S_ * 2; W2.cur = W2.beg + rot2 * (32 * S_ * 2);
    W1.vo = r8 * F_ * 2 + pc * 16;
    W2.vo = r8 * S_ * 2 + pc * 16;
    W1.ring = W2.ring = region;
#pragma unroll
    for (int t = 0; t < 4; ++t) { W1.ta[t] = W2.ta[t] = t0 ^ (t << 5); }
  }
  constexpr int NT = 1;
  {
    const srd_t srdO = mk_srd(out, S_ * D_ * 2);
    asm volatile("buffer_load_dword v252, %0, %1, %2 offen" ::"v"(lane == 0 ? 0 : OOB), "s"(srdO), "s"((nb2 * 64 + 16 * w) * (D_ * 2) + mb2 * 128) : "memory");
  }
  if (g1) glds_slots<true, 0>(W1, make_iseq<PRE>{}); else waitv<0>();
  asm volatile("s_waitcnt vmcnt(%3)" : "+v"(ep), "+v"(xv[0]), "+v"(xv[1]) : "i"(4 * PRE + NT) : "memory");
  GAP();
  STAMP(1);
  if (g1) glds_slots<true, PRE>(W1, make_iseq<MID>{});
  GAP();
  unsigned tlo, thi;
  {
    const unsigned long long e = (((unsigned long long)ep[1] << 32) | ep[0]) + 1ull;
    tlo = rfl((unsigned)e); thi = rfl((unsigned)(e >> 32));
  }
  {
    float f[2][8], m[2], qs[2];
#pragma unroll
    for (int r = 0; r < 2; ++r) {
#pragma unroll
      for (int i = 0; i < 4; ++i) { f[r][2 * i] = bflo(xv[r][i]); f[r][2 * i + 1] = bfhi(xv[r][i]); }
      m[r] = ((f[r][0] + f[r][1]) + (f[r][2] + f[r][3])) + ((f[r][4] + f[r][5]) + (f[r][6] + f[r][7]));
    }
    wave_sum2(m[0], m[1]);
#pragma unroll
    for (int r = 0; r < 2; ++r) {
      const float mu = m[r] * (1.0f / D_);
#pragma unroll
      for (int i = 0; i < 8; ++i) f[r][i] -= mu;
      qs[r] = ((f[r][0] * f[r][0] + f[r][1] * f[r][1]) + (f[r][2] * f[r][2] + f[r][3] * f[r][3])) + ((f[r][4] * f[r][4] + f[r][5] * f[r][5]) + (f[r][6] * f[r][6] + f[r][7] * f[r][7]));
      qs[r] = lane < 56 ? qs[r] : 0.f;
    }
    wave_sum2(qs[0], qs[1]);
    v4u o[2];
#pragma unroll
    for (int r = 0; r < 2; ++r) {
      const float rs = __builtin_amdgcn_rsqf(qs[r] * (1.0f / D_) + 1e-5f);
      o[r] = v4u{pack2(f[r][0] * rs, f[r][1] * rs), pack2(f[r][2] * rs, f[r][3] * rs), pack2(f[r][4] * rs, f[r][5] * rs), pack2(f[r][6] * rs, f[r][7] * rs)};
    }
    if (lane < 56) {
      const unsigned ro = w * REG_W + LN_OFF + (w & 1) * 128 + lane * 16;
      *(LAS v4u*)(smem + ro) = o[0];
      *(LAS v4u*)(smem + ro + LNP) = o[1];
    }
  }
  AStream A1, A2;
  W2Head H2;
  auto setup1 = [&]() {
    GAP();
    A1.srd = srdWS;
    astream_init<KW1>(A1, rot1);
#pragma unroll
    for (int m = 0; m < 4; ++m) A1.voff[m] = lane * 16 + ((m + w) & 3) * HT + (H_OFF + 4 * mb1 * HT + w * KW1 * KSTRIDE);
    H2.srd = W2.srd; H2.ring = W2.ring;
#pragma unroll
    for (int i = 0; i < 4; ++i) H2.vo[i] = W2.vo + i * 8 * S_ * 2;
#pragma unroll
    for (int i = 0; i < TPF; ++i) { H2.cur[i] = W2.cur; cyc_add(W2.cur, 32 * S_ * 2, W2.beg, W2.end); }
    GAP();
  };
  auto setup2 = [&]() {
    GAP();
    A2.srd = srdWS;
    astream_init<KW2>(A2, rot2);
#pragma unroll
    for (int m = 0; m < 4; ++m) A2.voff[m] = lane * 16 + ((m + w) & 3) * UT + (U_OFF + 4 * mb2 * UT + w * KW2 * KSTRIDE);
    GAP();
  };
  wg_barrier();
  {
    v2u lo[2], hi[2];
    const unsigned q = (lane >> 2) & 3;
    const unsigned tlo_a = lds0 + (q >> 1) * (REG_W + 128) + LN_OFF + (q & 1) * LNP + g * 32 + (lane & 3) * 8 + w * 128, thi_a = tlo_a + 2 * REG_W;
    asm volatile("ds_read_b64_tr_b16 %0, %1 offset:0" : "=v"(lo[0]) : "v"(tlo_a) : "memory");
    asm volatile("ds_read_b64_tr_b16 %0, %1 offset:0" : "=v"(hi[0]) : "v"(thi_a) : "memory");
    asm volatile("ds_read_b64_tr_b16 %0, %1 offset:512" : "=v"(lo[1]) : "v"(tlo_a) : "memory");
    asm volatile("ds_read_b64_tr_b16 %0, %1 offset:512" : "=v"(hi[1]) : "v"(thi_a) : "memory");
    asm volatile("s_waitcnt lgkmcnt(0)" : "+v"(lo[0]), "+v"(lo[1]), "+v"(hi[0]), "+v"(hi[1])::"memory");
    const unsigned vo = g * HT + c * 16 + (b & 3) * 256;
    const unsigned so = H_OFF + (b >> 2) * KSTRIDE + w * 4 * HT;
    store16_sc1(v4u{lo[0][0], lo[0][1], hi[0][0], hi[0][1]}, vo, srdWS, so);
    store16_sc1(v4u{lo[1][0], lo[1][1], hi[1][0], hi[1][1]}, w < 3 ? vo : OOB, srdWS, so + 16 * HT);
  }
  Exch X;
  X.mine = (LAS unsigned char*)smem + w * REG_W + lane * 16;
#pragma unroll
  for (int j = 0; j < 3; ++j) X.from[j] = (const LAS unsigned char*)smem + ((w - 1 - j) & 3) * REG_W + j * 4096 + lane * 16;
  const unsigned vf = lane < R1 ? lane * 8192 + w * 2048 + b * 8 : OOB;
  if (g1) {
    static_assert(NA == NA0, "common head");
    glds_slots<true, NB1>(W1, make_iseq<NA>{});
    waitv<4 * NA>();
    store8_sc1(v2u{tlo, thi}, vf, srdWS, HF_OFF);
    STAMP(2);
    if (w != 0) {
      setup1();
      if (WSLEEP1) __builtin_amdgcn_s_sleep(WSLEEP1);
      glds_slots<true, NB1 + NA>(W1, make_iseq<NS1 - NB1 - NA>{});
    } else {
      glds_slots<true, NB1 + NA>(W1, make_iseq<NS1 - NB1 - NA>{});
      setup1();
    }
    wpipe_stepsx<true, 0, 2, NA>(W1, make_iseq<7>{});
    v2u bv[4];
    {
      const srd_t srdB1 = mk_srd(b1, F_ * 2);
      asm volatile("buffer_load_dwordx2 %0, %1, %2, %3 offen" : "=v"(bv[0]) : "v"(g * 8), "s"(srdB1), "s"(nb1 * 128u) : "memory");
      asm volatile("buffer_load_dwordx2 %0, %1, %2, %3 offen offset:32" : "=v"(bv[1]) : "v"(g * 8), "s"(srdB1), "s"(nb1 * 128u) : "memory");
      asm volatile("buffer_load_dwordx2 %0, %1, %2, %3 offen offset:64" : "=v"(bv[2]) : "v"(g * 8), "s"(srdB1), "s"(nb1 * 128u) : "memory");
      asm volatile("buffer_load_dwordx2 %0, %1, %2, %3 offen offset:96" : "=v"(bv[3]) : "v"(g * 8), "s"(srdB1), "s"(nb1 * 128u) : "memory");
    }
    GAP();
    if (w == 0) {
      const unsigned so = HF_OFF + (b % R1) * 8192 + (mb1 & 3) * 2048;
      v4u f[2];
      poll2_issue<2>(f, lane * 16, srdWS, so);
      wtail_steps<true, 7, 6, NA0>(W1, make_iseq<KW1 - 7>{});
      STAMPX0(3);
      if (!poll2_check<2>(f, true, tlo, thi)) poll_flags2<PSLEEP, 2>(lane * 16, true, srdWS, so, tlo, thi);
    }
    asm volatile("s_barrier" ::: "memory");
    STAMPA(4);
    gemm_prologue<true>(A1, make_iseq<DF1>{});
    if (w != 0) { wtail_steps<true, 7, 4 + DF1, NA>(W1, make_iseq<KW1 - 7>{}); STAMPX1(3); }
    GAP();
    asm volatile("s_waitcnt vmcnt(%4) lgkmcnt(0)" : "+v"(bv[0]), "+v"(bv[1]), "+v"(bv[2]), "+v"(bv[3]) : "i"(DF1) : "memory");
    f32x4 s[4];
#pragma unroll
    for (int t = 0; t < 4; ++t) { const v2u q = bv[t]; const unsigned q0 = q[0], q1 = q[1]; s[t] = f32x4{bflo(q0), bfhi(q0), bflo(q1), bfhi(q1)}; }
    GAP();
    gemm_body<true>(s, A1, H2, X);
    STAMPA(5);
    if (!W2POS) glds_slots<false, TPF>(W2, make_iseq<NS2 - TPF>{});
    GAP();
#pragma unroll
    for (int kk = 0; kk < 2; ++kk) {
      const f32x4 s0 = s[2 * kk], s1 = s[2 * kk + 1];
      const v4u o = {pack2(gelu(s0[0]), gelu(s0[1])), pack2(gelu(s0[2]), gelu(s0[3])), pack2(gelu(s1[0]), gelu(s1[1])), pack2(gelu(s1[2]), gelu(s1[3]))};
      store16_sc1(o, lane * 16, srdWS, U_OFF + (4 * mb1 + w) * UT + (2 * nb1 + kk) * KSTRIDE);
    }
    GAP();
    if (W2POS) glds_slots<false, TPF>(W2, make_iseq<NS2 - TPF>{});
    wpipe_steps<false, 0, 1>(W2, make_iseq<POST2>{});
    setup2();
    waitv<4 * POST2 + (W2POS ? 4 * (NS2 - TPF) : 0)>();
    store8_sc1(v2u{tlo, thi}, lane < R2 ? lane * UF_REP + (mb1 * 4 + (xcd >> 1)) * 256 + (nq1 * 4 + w) * 8 : OOB, srdWS, UF_OFF);
    STAMP(6);
    if (!g2) {
      if (b == EPOCH_WG && w == 1) store8_sc1(v2u{tlo, thi}, lane == 0 ? 0 : OOB, srdWS, EP_OFF);
      waitv<0>();
      goto done;
    }
    if (w != 0) __builtin_amdgcn_s_sleep(WSLEEP2);
    wpipe_steps<false, POST2, 2>(W2, make_iseq<NS2 - POST2>{});
  } else {
    waitv<0>();
    store8_sc1(v2u{tlo, thi}, vf, srdWS, HF_OFF);
    STAMP(2);
    wg_barrier();
    if (!g2) goto done;
#pragma unroll 1
    for (int i = 0; i < IDLE_SLEEP; ++i) __builtin_amdgcn_s_sleep(16);
    glds_slots<false, 0>(W2, make_iseq<NS2>{});
    setup2();
    wpipe_steps<false, 0, 0>(W2, make_iseq<KW2>{});
    if (w == 0) poll_flags<8>(0, srdWS, EP_OFF, tlo, thi);
    wg_barrier();
  }
  {
    unsigned bv[4];
    {
      const srd_t srdB2 = mk_srd(b2, S_ * 2);
      asm volatile("buffer_load_ushort %0, %1, %2, %3 offen" : "=v"(bv[0]) : "v"(c * 2), "s"(srdB2), "s"(nb2 * 128u) : "memory");
      asm volatile("buffer_load_ushort %0, %1, %2, %3 offen offset:32" : "=v"(bv[1]) : "v"(c * 2), "s"(srdB2), "s"(nb2 * 128u) : "memory");
      asm volatile("buffer_load_ushort %0, %1, %2, %3 offen offset:64" : "=v"(bv[2]) : "v"(c * 2), "s"(srdB2), "s"(nb2 * 128u) : "memory");
      asm volatile("buffer_load_ushort %0, %1, %2, %3 offen offset:96" : "=v"(bv[3]) : "v"(c * 2), "s"(srdB2), "s"(nb2 * 128u) : "memory");
    }
    GAP();
    if (w == 0) {
      const unsigned so = UF_OFF + (nb2 % R2) * UF_REP + mb2 * 1024;
      v4u f[2];
      poll2_issue<1>(f, lane * 16, srdWS, so);
      if (g1) wtail_steps<false, NS2, 5, NA>(W2, make_iseq<KW2 - NS2>{});
      if (!poll2_check<1>(f, c < 14, tlo, thi)) poll_flags2<PSLEEP, 1>(lane * 16, c < 14, srdWS, so, tlo, thi);
    }
    asm volatile("s_barrier" ::: "memory");
    STAMPB(4);
    gemm_prologue<false>(A2, make_iseq<DF2>{});
    if (w != 0 && g1) wtail_steps<false, NS2, 4 + DF2, NA>(W2, make_iseq<KW2 - NS2>{});
    STAMPB(3);
    GAP();
    asm volatile("s_waitcnt vmcnt(%4) lgkmcnt(0)" : "+v"(bv[0]), "+v"(bv[1]), "+v"(bv[2]), "+v"(bv[3]) : "i"(DF2) : "memory");
    f32x4 s[4];
#pragma unroll
    for (int t = 0; t < 4; ++t) { const float bb = bflo(bv[t]); s[t] = f32x4{bb, bb, bb, bb}; }
    GAP();
    gemm_body<false>(s, A2, H2, X);
    STAMPB(5);
    unsigned char* stage = smem + W2_OFF;
#pragma unroll
    for (int t = 0; t < 4; ++t) *(LAS v2u*)((LAS unsigned char*)stage + (16 * t + c) * 144 + 32 * w + 8 * g) = v2u{pack2(s[t][0], s[t][1]), pack2(s[t][2], s[t][3])};
    wg_barrier();
    const srd_t srdO = mk_srd(out, S_ * D_ * 2);
    const unsigned row = 16 * w + (lane >> 3), col = (lane & 7) * 16;
#pragma unroll
    for (int mp = 0; mp < 2; ++mp) {
      const v4u o = *(const LAS v4u*)((LAS unsigned char*)stage + (row + 8 * mp) * 144 + col);
      asm volatile("buffer_store_dwordx4 %0, %1, %2, %3 offen\n\ts_nop 1" ::"v"(o), "v"((row + 8 * mp) * (D_ * 2) + col), "s"(srdO), "s"(nb2 * 64 * (D_ * 2) + mb2 * 128) : "memory");
    }
  }
done:
  STAMP(7);
}

extern "C" void kernel_launch(void* const* d_in, const int* in_sizes, int n_in, void* d_out, int out_size, void* d_ws, size_t ws_size, hipStream_t stream) {
  mixer_kernel<<<256, 256, 0, stream>>>((const u16*)d_in[0], (const u16*)d_in[1], (const u16*)d_in[2], (const u16*)d_in[3], (const u16*)d_in[4],
                                       (u16*)d_out, (unsigned char*)d_ws);
}
```

```cpp
#include <hip/hip_runtime.h>
#include <stdint.h>
#include <utility>

typedef unsigned v4u __attribute__((ext_vector_type(4)));
typedef unsigned v2u __attribute__((ext_vector_type(2)));
typedef float f32x4 __attribute__((ext_vector_type(4)));
typedef unsigned short u16;
typedef __amdgpu_buffer_rsrc_t srd_t;
#define DI __device__ __forceinline__
#define LAS __attribute__((address_space(3)))
#define GAP() __builtin_amdgcn_sched_barrier(0)
template <int... Is> using iseq = std::integer_sequence<int, Is...>;
template <int N> using make_iseq = std::make_integer_sequence<int, N>;
#define ACLOB "a0","a1","a2","a3","a4","a5","a6","a7","a8","a9","a10","a11","a12","a13","a14","a15","a16","a17","a18","a19","a20","a21","a22","a23","a24","a25","a26","a27","a28","a29","a30","a31","a32","a33","a34","a35","a36","a37","a38","a39","a40","a41","a42","a43","a44","a45","a46","a47","a48","a49","a50","a51","a52","a53","a54","a55","a56","a57","a58","a59","a60","a61","a62","a63","a64","a65","a66","a67","a68","a69","a70","a71","a72","a73","a74","a75","a76","a77","a78","a79","a80","a81","a82","a83","a84","a85","a86","a87","a88","a89","a90","a91","a92","a93","a94","a95","a96","a97","a98","a99","a100","a101","a102","a103","a104","a105","a106","a107","a108","a109","a110","a111","a112","a113","a114","a115","a116","a117","a118","a119","a120","a121","a122","a123","a124","a125","a126","a127","a128","a129","a130","a131","a132","a133","a134","a135","a136","a137","a138","a139","a140","a141","a142","a143","a144","a145","a146","a147","a148","a149","a150","a151","a152","a153","a154","a155","a156","a157","a158","a159","a160","a161","a162","a163","a164","a165","a166","a167","a168","a169","a170","a171","a172","a173","a174","a175","a176","a177","a178","a179","a180","a181","a182","a183","a184","a185","a186","a187","a188","a189","a190","a191","a192","a193","a194","a195","a196","a197","a198","a199","a200","a201","a202","a203","a204","a205","a206","a207","a208","a209","a210","a211","a212","a213","a214","a215","a216","a217","a218","a219","a220","a221","a222","a223","a224","a225","a226","a227","a228","a229","a230","a231","a232","a233","a234","a235","a236","a237","a238","a239","a240","a241","a242","a243","a244","a245","a246","a247","a248","a249","a250","a251","a252","a253","a254","a255"

#define ACL0 "a0","a1","a2","a3","a4","a5","a6","a7","a8","a9","a10","a11","a12","a13","a14","a15","a16","a17","a18","a19","a20","a21","a22","a23","a24","a25","a26","a27","a28","a29","a30","a31","a32","a33","a34","a35","a36","a37","a38","a39","a40","a41","a42","a43","a44","a45","a46","a47","a48","a49","a50","a51","a52","a53","a54","a55","a56","a57","a58","a59","a60","a61","a62","a63"
#define ACL1 "a64","a65","a66","a67","a68","a69","a70","a71","a72","a73","a74","a75","a76","a77","a78","a79","a80","a81","a82","a83","a84","a85","a86","a87","a88","a89","a90","a91","a92","a93","a94","a95","a96","a97","a98","a99","a100","a101","a102","a103","a104","a105","a106","a107","a108","a109","a110","a111","a112","a113","a114","a115","a116","a117","a118","a119","a120","a121","a122","a123","a124","a125","a126","a127"
#define ACL2 "a128","a129","a130","a131","a132","a133","a134","a135","a136","a137","a138","a139","a140","a141","a142","a143","a144","a145","a146","a147","a148","a149","a150","a151","a152","a153","a154","a155","a156","a157","a158","a159","a160","a161","a162","a163","a164","a165","a166","a167","a168","a169","a170","a171","a172","a173","a174","a175","a176","a177","a178","a179","a180","a181","a182","a183","a184","a185","a186","a187","a188","a189","a190","a191"
#define ACL3 "a192","a193","a194","a195","a196","a197","a198","a199","a200","a201","a202","a203","a204","a205","a206","a207","a208","a209","a210","a211","a212","a213","a214","a215","a216","a217","a218","a219","a220","a221","a222","a223","a224","a225","a226","a227","a228","a229","a230","a231","a232","a233","a234","a235","a236","a237","a238","a239","a240","a241","a242","a243","a244","a245","a246","a247","a248","a249","a250","a251","a252","a253","a254","a255"

#define TOUCH 5
#define MAPPING 3
#define DF1 16
#define DF2 16
#define CODEPF 1
constexpr unsigned CODE_BYTES = 20480;
#define PSL 3
#define HB 5
#define W2POS 1
#define PRE 3
#define MID 0
#define NA 3
#define NA0 3
#define EARLYPOLL 1
#define POST2 0
#define ROT1 6
#define ROT2 1
#define ROTN1 2
#define ROTN2 2
#define IDLE_SLEEP 0
#define PSLEEP 2
#define QUIET 1
#define WSLEEP1 10
#define WSLEEP2 16
#define R1 4
#define R2 4

constexpr int S_ = 2048, D_ = 448, F_ = 1792;
constexpr int KW1 = 16, KW2 = 14;
constexpr int NS1 = 9, NS2 = 7;
#define TPFMAX 6
constexpr int TPF = DF1 / 4 < TPFMAX ? DF1 / 4 : TPFMAX;
constexpr unsigned KSTRIDE = 1024u;
constexpr unsigned HT = 64u * KSTRIDE, UT = 56u * KSTRIDE;
constexpr unsigned H_OFF = 0, U_OFF = 28u * HT;
constexpr unsigned EP_OFF = 4u << 20, HF_OFF = EP_OFF + 4096u, UF_OFF = HF_OFF + 16u * 8192u;
constexpr unsigned UF_REP = 28u * 256u;
constexpr unsigned WS_USED = UF_OFF + 16u * UF_REP;
constexpr unsigned REG_W = 40960, LN_OFF = 36864, LNP = 960, W2_OFF = 12288, LDS_BYTES = 4 * REG_W;
constexpr unsigned OOB = 0x7ffffff0u;
constexpr int SPIN_MAX = 1 << 15;
constexpr unsigned EPOCH_WG = 225;

DI unsigned rfl(unsigned x) { return __builtin_amdgcn_readfirstlane(x); }
DI float bflo(unsigned w) { return __builtin_bit_cast(float, w << 16); }
DI float bfhi(unsigned w) { return __builtin_bit_cast(float, w & 0xffff0000u); }
DI unsigned pack2(float lo, float hi) {
  typedef __bf16 bf2 __attribute__((ext_vector_type(2)));
  typedef float f2 __attribute__((ext_vector_type(2)));
  f2 v = {lo, hi};
  return __builtin_bit_cast(unsigned, __builtin_convertvector(v, bf2));
}
DI srd_t mk_srd(const void* p, unsigned bytes) { return __builtin_amdgcn_make_buffer_rsrc((void*)p, 0, bytes, 0x00020000); }
template <int C> DI float dpp_add(float v) {
  return v + __builtin_bit_cast(float, __builtin_amdgcn_update_dpp(0, __builtin_bit_cast(int, v), C, 0xf, 0xf, true));
}
DI void wave_sum2(float& a, float& b) {
  a = dpp_add<0xB1>(a); b = dpp_add<0xB1>(b); a = dpp_add<0x4E>(a); b = dpp_add<0x4E>(b);
  a = dpp_add<0x141>(a); b = dpp_add<0x141>(b); a = dpp_add<0x140>(a); b = dpp_add<0x140>(b);
  auto p = __builtin_amdgcn_permlane16_swap(__builtin_bit_cast(unsigned, a), __builtin_bit_cast(unsigned, b), false, false);
  const unsigned p0 = p[0], p1 = p[1];
  float s = __builtin_bit_cast(float, p0) + __builtin_bit_cast(float, p1);
  auto q = __builtin_amdgcn_permlane32_swap(__builtin_bit_cast(unsigned, s), __builtin_bit_cast(unsigned, s), false, false);
  const unsigned q0 = q[0], q1 = q[1];
  s = __builtin_bit_cast(float, q0) + __builtin_bit_cast(float, q1);
  a = __builtin_bit_cast(float, __builtin_amdgcn_readlane(__builtin_bit_cast(int, s), 0));
  b = __builtin_bit_cast(float, __builtin_amdgcn_readlane(__builtin_bit_cast(int, s), 16));
}
template <int N> DI void waitv() { static_assert(N >= 0 && N < 64, "vmcnt"); asm volatile("s_waitcnt vmcnt(%0)" ::"i"(N) : "memory"); }
template <int N> DI void waitv_b() { static_assert(N >= 0 && N < 64, "vmcnt"); __builtin_amdgcn_s_waitcnt((N & 15) | ((N >> 4) << 14) | 0x0F70); }
DI void wait_lds() { asm volatile("s_waitcnt lgkmcnt(0)" ::: "memory"); }
DI void wg_barrier() { asm volatile("s_waitcnt lgkmcnt(0)\n\ts_barrier" ::: "memory"); }

struct WStream {
  srd_t srd;
  unsigned cur, beg, end;
  unsigned vo;
  unsigned ring;
  unsigned ta[4];
};
DI void cyc_add(unsigned& cur, unsigned inc, unsigned beg, unsigned end) {
  asm volatile("s_add_u32 %0, %0, %1\n\ts_cmp_eq_u32 %0, %3\n\ts_cselect_b32 %0, %2, %0" : "+s"(cur) : "s"(inc), "s"(beg), "s"(end) : "scc");
}
template <bool W1> struct WP { static constexpr int LDW = W1 ? F_ : S_, KW = W1 ? KW1 : KW2, NS = W1 ? NS1 : NS2, OFF = W1 ? 0 : (int)W2_OFF; };
template <bool W1, int KSTEP> DI void glds_slot(WStream& s) {
  using P = WP<W1>;
  unsigned t;
  asm volatile("s_add_u32 m0, %2, %3\n\ts_add_u32 %0, %1, 0\n\tbuffer_load_dwordx4 %5, %6, %0 offen lds\n\t"
               "s_add_u32 m0, %2, %3+1024\n\ts_add_u32 %0, %1, %4\n\tbuffer_load_dwordx4 %5, %6, %0 offen lds\n\t"
               "s_add_u32 m0, %2, %3+2048\n\ts_add_u32 %0, %1, 2*%4\n\tbuffer_load_dwordx4 %5, %6, %0 offen lds\n\t"
               "s_add_u32 m0, %2, %3+3072\n\ts_add_u32 %0, %1, 3*%4\n\tbuffer_load_dwordx4 %5, %6, %0 offen lds\n\t"
               "s_add_u32 %1, %1, 4*%4\n\ts_cmp_eq_u32 %1, %8\n\ts_cselect_b32 %1, %7, %1"
               : "=&s"(t), "+s"(s.cur)
               : "s"(s.ring), "i"(P::OFF + (KSTEP % P::NS) * 4096), "i"(8 * P::LDW * 2), "v"(s.vo), "s"(s.srd), "s"(s.beg), "s"(s.end)
               : "scc", "memory");
}
template <bool W1, int KSTEP> DI void tr_slot(const WStream& s) {
  using P = WP<W1>;
  asm volatile("ds_read_b64_tr_b16 a[%c4:%c4+1], %0 offset:%c5\n\tds_read_b64_tr_b16 a[%c4+2:%c4+3], %0 offset:%c5+2048\n\t"
               "ds_read_b64_tr_b16 a[%c4+4:%c4+5], %1 offset:%c5\n\tds_read_b64_tr_b16 a[%c4+6:%c4+7], %1 offset:%c5+2048\n\t"
               "ds_read_b64_tr_b16 a[%c4+8:%c4+9], %2 offset:%c5\n\tds_read_b64_tr_b16 a[%c4+10:%c4+11], %2 offset:%c5+2048\n\t"
               "ds_read_b64_tr_b16 a[%c4+12:%c4+13], %3 offset:%c5\n\tds_read_b64_tr_b16 a[%c4+14:%c4+15], %3 offset:%c5+2048"
               ::"v"(s.ta[0]), "v"(s.ta[1]), "v"(s.ta[2]), "v"(s.ta[3]), "i"(16 * KSTEP), "i"(P::OFF + (KSTEP % P::NS) * 4096) : "memory", ACLOB);
}
constexpr int NB1 = PRE + MID;
static_assert(NB1 + NA <= NS1 && NB1 + NA0 <= NS1 && POST2 <= NS2, "schedule");
template <bool W1, int NAX> constexpr int younger(int j, bool flagged) {
  constexpr int KW = W1 ? KW1 : KW2, NS = W1 ? NS1 : NS2;
  const int before_stores = W1 ? NB1 : (W2POS ? TPF : NS2), before_flag = W1 ? NB1 + NAX : NS2 + POST2;
  const int last = j + NS - 1 < KW - 1 ? j + NS - 1 : KW - 1;
  return 4 * (last - j) + (j < before_stores ? 2 : 0) + (flagged && j < before_flag ? 1 : 0);
}
template <bool W1, int J, int MODE, int EXTRA, int NAX> DI void wpipe_step(WStream& s) {
  using P = WP<W1>;
  constexpr int rem = P::KW - 1 - J;
  GAP();
  waitv_b<(MODE == 0 ? 4 * (rem < P::NS - 1 ? rem : P::NS - 1) : younger<W1, NAX>(J, MODE == 2)) + EXTRA>();
  GAP();
  tr_slot<W1, J>(s);
  if constexpr (J + P::NS < P::KW) { wait_lds(); glds_slot<W1, J + P::NS>(s); }
  GAP();
}
template <bool W1, int J0, int MODE, int NAX, int... Js> DI void wpipe_stepsx(WStream& s, iseq<Js...>) { (wpipe_step<W1, J0 + Js, MODE, 0, NAX>(s), ...); }
template <bool W1, int J0, int MODE, int... Js> DI void wpipe_steps(WStream& s, iseq<Js...>) { (wpipe_step<W1, J0 + Js, MODE, 0, NA>(s), ...); }
template <bool W1, int J0, int EXTRA, int NAX, int... Js> DI void wtail_steps(WStream& s, iseq<Js...>) { (wpipe_step<W1, J0 + Js, 2, EXTRA, NAX>(s), ...); }
template <bool W1, int J0, int... Js> DI void glds_slots(WStream& s, iseq<Js...>) { (glds_slot<W1, J0 + Js>(s), ...); }

constexpr int RB = 160;
static_assert(RB + 4 * DF1 <= 256 && RB + 4 * DF2 <= 256, "ring");
template <int REG> DI void act_load(unsigned voff, srd_t srd, unsigned soff) {
  asm volatile("buffer_load_dwordx4 v[%c0:%c1], %2, %3, %4 offen sc1" ::"i"(REG), "i"(REG + 3), "v"(voff), "s"(srd), "s"(soff) : "memory");
}
struct AStream { srd_t srd; unsigned voff[4], so[16]; };
template <int KW> DI void astream_init(AStream& a, unsigned rot) {
#pragma unroll
  for (int k = 0; k < KW; ++k) {
    const unsigned t = (rot + k) * KSTRIDE;
    if constexpr (KW == 16) a.so[k] = t & (16 * KSTRIDE - 1);
    else { const unsigned t2 = t - KW * KSTRIDE; a.so[k] = t < t2 ? t : t2; }
    asm volatile("" : "+s"(a.so[k]));
  }
}
struct W2Head { unsigned vo[4], cur[8], ring; srd_t srd; };
#define MFMA_ASM(CL)                                                                                                                                       \
  if constexpr (WA) {                                                                                                                                      \
    if constexpr (ZERO) asm volatile("v_mfma_f32_16x16x32_bf16 %0, a[%c1:%c2], v[%c3:%c4], 0" : "=&v"(acc) : "i"(AR), "i"(AR + 3), "i"(VR), "i"(VR + 3) : CL); \
    else asm volatile("v_mfma_f32_16x16x32_bf16 %0, a[%c1:%c2], v[%c3:%c4], %0" : "+v"(acc) : "i"(AR), "i"(AR + 3), "i"(VR), "i"(VR + 3) : CL);               \
  } else {                                                                                                                                                 \
    if constexpr (ZERO) asm volatile("v_mfma_f32_16x16x32_bf16 %0, v[%c3:%c4], a[%c1:%c2], 0" : "=&v"(acc) : "i"(AR), "i"(AR + 3), "i"(VR), "i"(VR + 3) : CL); \
    else asm volatile("v_mfma_f32_16x16x32_bf16 %0, v[%c3:%c4], a[%c1:%c2], %0" : "+v"(acc) : "i"(AR), "i"(AR + 3), "i"(VR), "i"(VR + 3) : CL);               \
  }
template <bool WA, bool ZERO, int AR, int VR, int Q> DI void mfma(f32x4& acc) {
  if constexpr (Q == 0) { MFMA_ASM(ACL0) } else if constexpr (Q == 1) { MFMA_ASM(ACL1) } else if constexpr (Q == 2) { MFMA_ASM(ACL2) } else { MFMA_ASM(ACL3) }
}
template <bool G1> constexpr int vop(int v) { return v < 4 * (G1 ? KW1 : KW2) ? 1 : (G1 && v - 4 * KW1 < 4 * TPF ? 1 : 0); }
template <bool G1> constexpr int vop_range(int a, int b) { int n = 0; for (int v = a; v <= b; ++v) n += vop<G1>(v); return n; }
template <bool G1, int V> DI void act_op(const AStream& a) {
  constexpr int KW = G1 ? KW1 : KW2, DF = G1 ? DF1 : DF2;
  act_load<RB + 4 * (V % DF)>(a.voff[(V / KW + 1) & 3], a.srd, a.so[V % KW]);
}
template <int P> DI void w2_m0(const W2Head& h) {
  asm volatile("s_add_u32 m0, %0, %1" ::"s"(h.ring), "i"((int)W2_OFF + ((P / 4) % NS2) * 4096 + (P % 4) * 1024) : "scc", "memory");
}
template <int P> DI void w2_load(const W2Head& h) {
  asm volatile("buffer_load_dwordx4 %0, %1, %2 offen lds" ::"v"(h.vo[P % 4]), "s"(h.srd), "s"(h.cur[P / 4]) : "memory");
}
struct Exch { LAS unsigned char* mine; const LAS unsigned char* from[3]; };
template <bool G1> constexpr bool has_piece(int i) { constexpr int KW = G1 ? KW1 : KW2, DF = G1 ? DF1 : DF2; return i >= 1 && i - 1 + DF >= 4 * KW && vop<G1>(i - 1 + DF) != 0; }
template <bool G1> constexpr int xcap(int i, int g) {
  constexpr int KW = G1 ? KW1 : KW2;
  const int j = i - 3 * KW;
  if (j < HB || (j == HB && g == 0)) return 0;
  return 2 - (g == 3 ? 1 : 0) - (has_piece<G1>(i) && g < 2 ? 1 : 0);
}
template <bool G1> constexpr int xfirst(int i, int g) {
  constexpr int KW = G1 ? KW1 : KW2;
  int n = 0;
  for (int ii = 3 * KW; ii <= i; ++ii)
    for (int gg = 0; gg < 4 && (ii < i || gg < g); ++gg) n += xcap<G1>(ii, gg);
  return n;
}
constexpr int NX = 12 + 1 + 32;
static_assert(xfirst<true>(4 * KW1, 0) >= NX && xfirst<false>(4 * KW2, 0) >= NX, "exchange does not fit");
struct XRegs { f32x4 r[3][4]; float a[4][4], s[4][4]; };
DI float addf(float a, float b) { float d; asm volatile("v_add_f32 %0, %1, %2" : "=v"(d) : "v"(a), "v"(b)); return d; }
template <int N> DI void xop(XRegs& r, const Exch& x) {
  if constexpr (N < 12) r.r[N / 4][N % 4] = *(const LAS f32x4*)(x.from[N / 4] + (N % 4) * 1024);
  else if constexpr (N == 12) __builtin_amdgcn_s_waitcnt(0xC07F);
  else if constexpr (N < 29) { constexpr int t = (N - 13) / 4, e = (N - 13) % 4; const float p = r.r[0][t][e], q = r.r[1][t][e]; r.a[t][e] = addf(p, q); }
  else if constexpr (N < NX) { constexpr int t = (N - 29) / 4, e = (N - 29) % 4; const float p = r.r[2][t][e]; r.s[t][e] = addf(r.a[t][e], p); }
}
template <bool G1, int I, int G> DI void slot(f32x4 (&acc)[4][4], XRegs& r, const AStream& a, const W2Head& h, const Exch& x) {
  constexpr int KW = G1 ? KW1 : KW2, DF = G1 ? DF1 : DF2, V = I - 1 + DF;
  GAP();
  if constexpr (I >= 1 && V < 4 * KW) { if constexpr (G == 0) act_op<G1, V>(a); }
  else if constexpr (has_piece<G1>(I)) {
    if constexpr (G == 0) w2_m0<V - 4 * KW>(h);
    if constexpr (G == 1) w2_load<V - 4 * KW>(h);
  }
  if constexpr (I % KW == 1 && I > KW) {
    constexpr int m = I / KW;
    asm volatile("" : "+v"(acc[m][G]));
    *(LAS f32x4*)(x.mine + (m - 1) * 4096 + G * 1024) = acc[m][G];
  }
  if constexpr (I == 3 * KW + HB && G == 0) wg_barrier();
  constexpr int n0 = xfirst<G1>(I, G), cap = xcap<G1>(I, G);
  if constexpr (cap >= 1 && n0 < NX) xop<n0>(r, x);
  if constexpr (cap >= 2 && n0 + 1 < NX) xop<n0 + 1>(r, x);
  GAP();
}
template <bool G1, int I> DI void gemm_frag(f32x4 (&acc)[4][4], XRegs& r, const AStream& a, const W2Head& h, const Exch& x) {
  constexpr int KW = G1 ? KW1 : KW2, DF = G1 ? DF1 : DF2, K = I % KW, M = (I / KW + 1) & 3, VR = RB + 4 * (I % DF);
  constexpr bool Z = K == 0 && M != 0;
  waitv_b<I == 0 ? vop_range<G1>(1, DF - 1) : vop_range<G1>(I + 1, I - 2 + DF)>();
  GAP();
  mfma<G1, Z, 16 * K + 0, VR, 0>(acc[M][0]);
  slot<G1, I, 0>(acc, r, a, h, x);
  mfma<G1, Z, 16 * K + 4, VR, 1>(acc[M][1]);
  slot<G1, I, 1>(acc, r, a, h, x);
  mfma<G1, Z, 16 * K + 8, VR, 2>(acc[M][2]);
  slot<G1, I, 2>(acc, r, a, h, x);
  mfma<G1, Z, 16 * K + 12, VR, 3>(acc[M][3]);
  slot<G1, I, 3>(acc, r, a, h, x);
}
template <bool G1, int... Is> DI void gemm_frags(f32x4 (&acc)[4][4], XRegs& r, const AStream& a, const W2Head& h, const Exch& x, iseq<Is...>) {
  (gemm_frag<G1, Is>(acc, r, a, h, x), ...);
}
template <bool G1, int V0, int... Vs> DI void act_ops(const AStream& a, iseq<Vs...>) { (act_op<G1, V0 + Vs>(a), ...); GAP(); }
template <bool G1, bool TAIL, int NAX, int G> DI void start_group(const AStream& a, WStream& ws) {
  constexpr int DF = G1 ? DF1 : DF2, KW = G1 ? KW1 : KW2, J0 = G1 ? 7 : NS2, NG = DF / 4;
  act_ops<G1, 4 * G>(a, make_iseq<4>{});
  if constexpr (TAIL) {
    constexpr int j0 = J0 + 2 * G < KW ? J0 + 2 * G : KW, j1 = G == NG - 1 ? KW : (J0 + 2 * G + 2 < KW ? J0 + 2 * G + 2 : KW);
    wtail_steps<G1, j0, 4 + 4 * (G + 1), NAX>(ws, make_iseq<j1 - j0>{});
  } else if constexpr (G < NG - 1 && PSL > 0) {
    __builtin_amdgcn_s_sleep(PSL);
  }
}
template <bool G1, bool TAIL, int NAX, int... Gs> DI void gemm_start(const AStream& a, WStream& ws, iseq<Gs...>) { (start_group<G1, TAIL, NAX, Gs>(a, ws), ...); GAP(); }
template <bool G1> DI void gemm_body(f32x4 (&pre)[4], const AStream& a, const W2Head& h, const Exch& x) {
  constexpr int KW = G1 ? KW1 : KW2, DF = G1 ? DF1 : DF2, VL = 4 * KW - 1 + DF;
  f32x4 acc[4][4];
  XRegs r;
#pragma unroll
  for (int t = 0; t < 4; ++t) acc[0][t] = pre[t];
  asm volatile("" : "+v"(acc[0][0]), "+v"(acc[0][1]), "+v"(acc[0][2]), "+v"(acc[0][3]));
  GAP();
  gemm_frags<G1>(acc, r, a, h, x, make_iseq<4 * KW>{});
  if constexpr (vop<G1>(VL) != 0) {
    w2_m0<VL - 4 * KW>(h);
    asm volatile("s_nop 0");
    w2_load<VL - 4 * KW>(h);
  }
  asm volatile("s_nop 7" : "+v"(acc[0][0]), "+v"(acc[0][1]), "+v"(acc[0][2]), "+v"(acc[0][3]));
#pragma unroll
  for (int t = 0; t < 4; ++t) pre[t] = acc[0][t] + f32x4{r.s[t][0], r.s[t][1], r.s[t][2], r.s[t][3]};
}
template <int SLEEP> DI void poll_flags(unsigned voff, srd_t srd, unsigned soff, unsigned tlo, unsigned thi) {
#pragma unroll 1
  for (int spin = 0; spin < SPIN_MAX; ++spin) {
    v2u f;
    asm volatile("buffer_load_dwordx2 %0, %1, %2, %3 offen sc1\n\ts_waitcnt vmcnt(0)" : "=v"(f) : "v"(voff), "s"(srd), "s"(soff) : "memory");
    if (__builtin_amdgcn_ballot_w64(f[0] != tlo || f[1] != thi) == 0) break;
    __builtin_amdgcn_s_sleep(SLEEP);
  }
}
template <int N> DI void poll2_issue(v4u (&f)[2], unsigned voff, srd_t srd, unsigned soff) {
  asm volatile("buffer_load_dwordx4 %0, %1, %2, %3 offen sc1" : "=v"(f[0]) : "v"(voff), "s"(srd), "s"(soff) : "memory");
  if constexpr (N == 2) asm volatile("buffer_load_dwordx4 %0, %1, %2, %3 offen offset:1024 sc1" : "=v"(f[1]) : "v"(voff), "s"(srd), "s"(soff) : "memory");
}
template <int N> DI bool poll2_check(v4u (&f)[2], bool use, unsigned tlo, unsigned thi) {
  if constexpr (N == 2) asm volatile("s_waitcnt vmcnt(0)" : "+v"(f[0]), "+v"(f[1])::"memory");
  else asm volatile("s_waitcnt vmcnt(0)" : "+v"(f[0])::"memory");
  const v4u a = f[0];
  bool bad = a[0] != tlo || a[1] != thi || a[2] != tlo || a[3] != thi;
  if constexpr (N == 2) { const v4u c = f[1]; bad = bad || c[0] != tlo || c[1] != thi || c[2] != tlo || c[3] != thi; }
  return __builtin_amdgcn_ballot_w64(bad && use) == 0;
}
template <int SLEEP, int N> DI void poll_flags2(unsigned voff, bool use, srd_t srd, unsigned soff, unsigned tlo, unsigned thi) {
#pragma unroll 1
  for (int spin = 0; spin < SPIN_MAX; ++spin) {
    v4u f[2];
    poll2_issue<N>(f, voff, srd, soff);
    if (poll2_check<N>(f, use, tlo, thi)) break;
    __builtin_amdgcn_s_sleep(SLEEP);
  }
}
DI void store16_sc1(v4u d, unsigned voff, srd_t srd, unsigned soff) {
  asm volatile("buffer_store_dwordx4 %0, %1, %2, %3 offen sc1\n\ts_nop 1" ::"v"(d), "v"(voff), "s"(srd), "s"(soff) : "memory");
}
DI void store8_sc1(v2u d, unsigned voff, srd_t srd, unsigned soff) {
  asm volatile("buffer_store_dwordx2 %0, %1, %2, %3 offen sc1\n\ts_nop 1" ::"v"(d), "v"(voff), "s"(srd), "s"(soff) : "memory");
}
DI float gelu(float u) {
  const float e = __builtin_amdgcn_exp2f(u * (-2.3022082f - 0.1029432f * u * u));
  return u * __builtin_amdgcn_rcpf(1.0f + e);
}
#define PA(i)
#define PB(i)
#define STAMP(i)
#define STAMPA(i)
#define STAMPB(i)
#define STAMPX0(i)
#define STAMPX1(i)

__global__ void __launch_bounds__(256, 1) __attribute__((amdgpu_num_vgpr(RB)))
mixer_kernel(const u16* __restrict__ x, const u16* __restrict__ w1, const u16* __restrict__ b1, const u16* __restrict__ w2, const u16* __restrict__ b2,
             u16* __restrict__ out, unsigned char* __restrict__ ws) {
  __shared__ __attribute__((aligned(1024))) unsigned char smem[LDS_BYTES];
  STAMP(0);
  asm volatile("" ::: ACLOB);
  asm volatile("" ::: "v160", "v255");
  unsigned long long pc0;
  asm volatile("s_getpc_b64 %0" : "=s"(pc0));
  const unsigned tid = threadIdx.x, lane = tid & 63, w = rfl(tid >> 6), b = blockIdx.x;
  const unsigned lds0 = (unsigned)(uintptr_t)(LAS unsigned char*)smem;
  const srd_t srdWS = mk_srd(ws, WS_USED);
  const unsigned g = lane >> 4, c = lane & 15;

  v2u ep; v4u xv[2];
  {
    const srd_t srdX = mk_srd(x, S_ * D_ * 2);
    const unsigned zero = 0, vx = lane < 56 ? lane * 16 : OOB, row = 32 * (b >> 2) + 4 * (b & 3) + 2 * (w & 1) + 16 * (w >> 1);
    asm volatile("buffer_load_dwordx4 %0, %1, %2, %3 offen" : "=v"(xv[0]) : "v"(vx), "s"(srdX), "s"(row * 896u) : "memory");
    asm volatile("buffer_load_dwordx4 %0, %1, %2, %3 offen offset:896" : "=v"(xv[1]) : "v"(vx), "s"(srdX), "s"(row * 896u) : "memory");
    asm volatile("buffer_load_dwordx2 %0, %1, %2, %3 offen sc1" : "=v"(ep) : "v"(zero), "s"(srdWS), "s"(EP_OFF) : "memory");
  }
  GAP();
  const unsigned xcd = b & 7, li = b >> 3;
  bool g1, g2;
  unsigned mb1, nb1, mb2, nb2, nq1;
  {
    const unsigned j = xcd >> 1, odd = xcd & 1, i0 = 7 + odd;
    g1 = li >= i0;
    const unsigned idx = g1 ? li - i0 + (odd ? 25 : 0) : 0;
    nq1 = (idx * 37) >> 8;
    nb1 = 7 * j + nq1; mb1 = idx - 7 * nq1;
    g2 = li < 28; nb2 = 4 * xcd + (li & 3); mb2 = g2 ? li >> 2 : 0;
  }
  const unsigned rot1 = ((nq1 == 3 ? 0xa50c840u : 0xdb96420u) >> (4 * mb1)) & 15;
  const unsigned rot2 = (0x6c4a280u >> (4 * mb2)) & 15;
  WStream W1, W2;
  const unsigned region = lds0 + w * REG_W;
  {
    const unsigned r8 = lane >> 3, pc = (lane & 7) ^ (r8 & 6);
    const unsigned q = (lane >> 2) & 3, p = lane & 3, row = 4 * g + q;
    const unsigned t0 = region + row * 128 + ((row >> 1) & 3) * 32 + p * 8;
    W1.srd = mk_srd(w1, S_ * F_ * 2);
    W2.srd = mk_srd(w2, g2 ? F_ * S_ * 2 : 0);
    W1.beg = w * (KW1 * 32) * F_ * 2 + nb1 * 128; W1.end = W1.beg + KW1 * 32 * F_ * 2; W1.cur = W1.beg + rot1 * (32 * F_ * 2);
    W2.beg = w * (KW2 * 32) * S_ * 2 + nb2 * 128; W2.end = W2.beg + KW2 * 32 * S_ * 2; W2.cur = W2.beg + rot2 * (32 * S_ * 2);
    W1.vo = r8 * F_ * 2 + pc * 16;
    W2.vo = r8 * S_ * 2 + pc * 16;
    W1.ring = W2.ring = region;
#pragma unroll
    for (int t = 0; t < 4; ++t) { W1.ta[t] = W2.ta[t] = t0 ^ (t << 5); }
  }
  constexpr int NT = 1;
  {
    const srd_t srdO = mk_srd(out, S_ * D_ * 2);
    asm volatile("buffer_load_dword v252, %0, %1, %2 offen" ::"v"(lane == 0 ? 0 : OOB), "s"(srdO), "s"((nb2 * 64 + 16 * w) * (D_ * 2) + mb2 * 128) : "memory");
  }
  if (g1) {
    glds_slots<true, 0>(W1, make_iseq<PRE>{});
  } else {
    const srd_t srdC = mk_srd((const void*)(pc0 & ~127ull), CODE_BYTES);
    asm volatile("buffer_load_dword v253, %0, %1, 0 offen" ::"v"((w * 64 + lane) * 128), "s"(srdC) : "memory");
    waitv<1>();
  }
  asm volatile("s_waitcnt vmcnt(%3)" : "+v"(ep), "+v"(xv[0]), "+v"(xv[1]) : "i"(4 * PRE + NT) : "memory");
  GAP();
  STAMP(1);
  if (g1) glds_slots<true, PRE>(W1, make_iseq<MID>{});
  GAP();
  unsigned tlo, thi;
  {
    const unsigned long long e = (((unsigned long long)ep[1] << 32) | ep[0]) + 1ull;
    tlo = rfl((unsigned)e); thi = rfl((unsigned)(e >> 32));
  }
  {
    float f[2][8], m[2], qs[2];
#pragma unroll
    for (int r = 0; r < 2; ++r) {
#pragma unroll
      for (int i = 0; i < 4; ++i) { f[r][2 * i] = bflo(xv[r][i]); f[r][2 * i + 1] = bfhi(xv[r][i]); }
      m[r] = ((f[r][0] + f[r][1]) + (f[r][2] + f[r][3])) + ((f[r][4] + f[r][5]) + (f[r][6] + f[r][7]));
    }
    wave_sum2(m[0], m[1]);
#pragma unroll
    for (int r = 0; r < 2; ++r) {
      const float mu = m[r] * (1.0f / D_);
#pragma unroll
      for (int i = 0; i < 8; ++i) f[r][i] -= mu;
      qs[r] = ((f[r][0] * f[r][0] + f[r][1] * f[r][1]) + (f[r][2] * f[r][2] + f[r][3] * f[r][3])) + ((f[r][4] * f[r][4] + f[r][5] * f[r][5]) + (f[r][6] * f[r][6] + f[r][7] * f[r][7]));
      qs[r] = lane < 56 ? qs[r] : 0.f;
    }
    wave_sum2(qs[0], qs[1]);
    v4u o[2];
#pragma unroll
    for (int r = 0; r < 2; ++r) {
      const float rs = __builtin_amdgcn_rsqf(qs[r] * (1.0f / D_) + 1e-5f);
      o[r] = v4u{pack2(f[r][0] * rs, f[r][1] * rs), pack2(f[r][2] * rs, f[r][3] * rs), pack2(f[r][4] * rs, f[r][5] * rs), pack2(f[r][6] * rs, f[r][7] * rs)};
    }
    if (lane < 56) {
      const unsigned ro = w * REG_W + LN_OFF + (w & 1) * 128 + lane * 16;
      *(LAS v4u*)(smem + ro) = o[0];
      *(LAS v4u*)(smem + ro + LNP) = o[1];
    }
  }
  AStream A1, A2;
  W2Head H2;
  auto setup1 = [&]() {
    GAP();
    A1.srd = srdWS;
    astream_init<KW1>(A1, rot1);
#pragma unroll
    for (int m = 0; m < 4; ++m) A1.voff[m] = lane * 16 + ((m + w) & 3) * HT + (H_OFF + 4 * mb1 * HT + w * KW1 * KSTRIDE);
    H2.srd = W2.srd; H2.ring = W2.ring;
#pragma unroll
    for (int i = 0; i < 4; ++i) H2.vo[i] = W2.vo + i * 8 * S_ * 2;
#pragma unroll
    for (int i = 0; i < TPF; ++i) { H2.cur[i] = W2.cur; cyc_add(W2.cur, 32 * S_ * 2, W2.beg, W2.end); }
    GAP();
  };
  auto setup2 = [&]() {
    GAP();
    A2.srd = srdWS;
    astream_init<KW2>(A2, rot2);
#pragma unroll
    for (int m = 0; m < 4; ++m) A2.voff[m] = lane * 16 + ((m + w) & 3) * UT + (U_OFF + 4 * mb2 * UT + w * KW2 * KSTRIDE);
    GAP();
  };
  wg_barrier();
  {
    v2u lo[2], hi[2];
    const unsigned q = (lane >> 2) & 3;
    const unsigned tlo_a = lds0 + (q >> 1) * (REG_W + 128) + LN_OFF + (q & 1) * LNP + g * 32 + (lane & 3) * 8 + w * 128, thi_a = tlo_a + 2 * REG_W;
    asm volatile("ds_read_b64_tr_b16 %0, %1 offset:0" : "=v"(lo[0]) : "v"(tlo_a) : "memory");
    asm volatile("ds_read_b64_tr_b16 %0, %1 offset:0" : "=v"(hi[0]) : "v"(thi_a) : "memory");
    asm volatile("ds_read_b64_tr_b16 %0, %1 offset:512" : "=v"(lo[1]) : "v"(tlo_a) : "memory");
    asm volatile("ds_read_b64_tr_b16 %0, %1 offset:512" : "=v"(hi[1]) : "v"(thi_a) : "memory");
    asm volatile("s_waitcnt lgkmcnt(0)" : "+v"(lo[0]), "+v"(lo[1]), "+v"(hi[0]), "+v"(hi[1])::"memory");
    const unsigned vo = g * HT + c * 16 + (b & 3) * 256;
    const unsigned so = H_OFF + (b >> 2) * KSTRIDE + w * 4 * HT;
    store16_sc1(v4u{lo[0][0], lo[0][1], hi[0][0], hi[0][1]}, vo, srdWS, so);
    store16_sc1(v4u{lo[1][0], lo[1][1], hi[1][0], hi[1][1]}, w < 3 ? vo : OOB, srdWS, so + 16 * HT);
  }
  Exch X;
  X.mine = (LAS unsigned char*)smem + w * REG_W + lane * 16;
#pragma unroll
  for (int j = 0; j < 3; ++j) X.from[j] = (const LAS unsigned char*)smem + ((w - 1 - j) & 3) * REG_W + j * 4096 + lane * 16;
  const unsigned vf = lane < R1 ? lane * 8192 + w * 2048 + b * 8 : OOB;
  if (g1) {
    static_assert(NA == NA0, "common head");
    glds_slots<true, NB1>(W1, make_iseq<NA>{});
    waitv<4 * NA>();
    store8_sc1(v2u{tlo, thi}, vf, srdWS, HF_OFF);
    STAMP(2);
    if (w != 0) {
      setup1();
      if (WSLEEP1) __builtin_amdgcn_s_sleep(WSLEEP1);
      glds_slots<true, NB1 + NA>(W1, make_iseq<NS1 - NB1 - NA>{});
    } else {
      glds_slots<true, NB1 + NA>(W1, make_iseq<NS1 - NB1 - NA>{});
      setup1();
    }
    wpipe_stepsx<true, 0, 2, NA>(W1, make_iseq<7>{});
    v2u bv[4];
    {
      const srd_t srdB1 = mk_srd(b1, F_ * 2);
      asm volatile("buffer_load_dwordx2 %0, %1, %2, %3 offen" : "=v"(bv[0]) : "v"(g * 8), "s"(srdB1), "s"(nb1 * 128u) : "memory");
      asm volatile("buffer_load_dwordx2 %0, %1, %2, %3 offen offset:32" : "=v"(bv[1]) : "v"(g * 8), "s"(srdB1), "s"(nb1 * 128u) : "memory");
      asm volatile("buffer_load_dwordx2 %0, %1, %2, %3 offen offset:64" : "=v"(bv[2]) : "v"(g * 8), "s"(srdB1), "s"(nb1 * 128u) : "memory");
      asm volatile("buffer_load_dwordx2 %0, %1, %2, %3 offen offset:96" : "=v"(bv[3]) : "v"(g * 8), "s"(srdB1), "s"(nb1 * 128u) : "memory");
    }
    GAP();
    if (w == 0) {
      const unsigned so = HF_OFF + (b % R1) * 8192 + (mb1 & 3) * 2048;
      v4u f[2];
      poll2_issue<2>(f, lane * 16, srdWS, so);
      wtail_steps<true, 7, 6, NA0>(W1, make_iseq<KW1 - 7>{});
      STAMPX0(3);
      if (!poll2_check<2>(f, true, tlo, thi)) poll_flags2<PSLEEP, 2>(lane * 16, true, srdWS, so, tlo, thi);
    }
    asm volatile("s_barrier" ::: "memory");
    STAMPA(4);
    if (w != 0) { gemm_start<true, true, NA>(A1, W1, make_iseq<DF1 / 4>{}); STAMPX1(3); }
    else gemm_start<true, false, NA>(A1, W1, make_iseq<DF1 / 4>{});
    GAP();
    asm volatile("s_waitcnt vmcnt(%4) lgkmcnt(0)" : "+v"(bv[0]), "+v"(bv[1]), "+v"(bv[2]), "+v"(bv[3]) : "i"(DF1) : "memory");
    f32x4 s[4];
#pragma unroll
    for (int t = 0; t < 4; ++t) { const v2u q = bv[t]; const unsigned q0 = q[0], q1 = q[1]; s[t] = f32x4{bflo(q0), bfhi(q0), bflo(q1), bfhi(q1)}; }
    GAP();
    gemm_body<true>(s, A1, H2, X);
    STAMPA(5);
    if (!W2POS) glds_slots<false, TPF>(W2, make_iseq<NS2 - TPF>{});
    GAP();
#pragma unroll
    for (int kk = 0; kk < 2; ++kk) {
      const f32x4 s0 = s[2 * kk], s1 = s[2 * kk + 1];
      const v4u o = {pack2(gelu(s0[0]), gelu(s0[1])), pack2(gelu(s0[2]), gelu(s0[3])), pack2(gelu(s1[0]), gelu(s1[1])), pack2(gelu(s1[2]), gelu(s1[3]))};
      store16_sc1(o, lane * 16, srdWS, U_OFF + (4 * mb1 + w) * UT + (2 * nb1 + kk) * KSTRIDE);
    }
    GAP();
    if (W2POS) glds_slots<false, TPF>(W2, make_iseq<NS2 - TPF>{});
    wpipe_steps<false, 0, 1>(W2, make_iseq<POST2>{});
    setup2();
    waitv<4 * POST2 + (W2POS ? 4 * (NS2 - TPF) : 0)>();
    store8_sc1(v2u{tlo, thi}, lane < R2 ? lane * UF_REP + (mb1 * 4 + (xcd >> 1)) * 256 + (nq1 * 4 + w) * 8 : OOB, srdWS, UF_OFF);
    STAMP(6);
    if (!g2) {
      if (b == EPOCH_WG && w == 1) store8_sc1(v2u{tlo, thi}, lane == 0 ? 0 : OOB, srdWS, EP_OFF);
      waitv<0>();
      goto done;
    }
    if (w != 0) __builtin_amdgcn_s_sleep(WSLEEP2);
    wpipe_steps<false, POST2, 2>(W2, make_iseq<NS2 - POST2>{});
  } else {
    waitv<0>();
    store8_sc1(v2u{tlo, thi}, vf, srdWS, HF_OFF);
    STAMP(2);
    wg_barrier();
    if (!g2) goto done;
#pragma unroll 1
    for (int i = 0; i < IDLE_SLEEP; ++i) __builtin_amdgcn_s_sleep(16);
    glds_slots<false, 0>(W2, make_iseq<NS2>{});
    setup2();
    wpipe_steps<false, 0, 0>(W2, make_iseq<KW2>{});
    if (w == 0) poll_flags<8>(0, srdWS, EP_OFF, tlo, thi);
    wg_barrier();
  }
  {
    unsigned bv[4];
    {
      const srd_t srdB2 = mk_srd(b2, S_ * 2);
      asm volatile("buffer_load_ushort %0, %1, %2, %3 offen" : "=v"(bv[0]) : "v"(c * 2), "s"(srdB2), "s"(nb2 * 128u) : "memory");
      asm volatile("buffer_load_ushort %0, %1, %2, %3 offen offset:32" : "=v"(bv[1]) : "v"(c * 2), "s"(srdB2), "s"(nb2 * 128u) : "memory");
      asm volatile("buffer_load_ushort %0, %1, %2, %3 offen offset:64" : "=v"(bv[2]) : "v"(c * 2), "s"(srdB2), "s"(nb2 * 128u) : "memory");
      asm volatile("buffer_load_ushort %0, %1, %2, %3 offen offset:96" : "=v"(bv[3]) : "v"(c * 2), "s"(srdB2), "s"(nb2 * 128u) : "memory");
    }
    GAP();
    if (w == 0) {
      const unsigned so = UF_OFF + (nb2 % R2) * UF_REP + mb2 * 1024;
      v4u f[2];
      poll2_issue<1>(f, lane * 16, srdWS, so);
      if (g1) wtail_steps<false, NS2, 5, NA>(W2, make_iseq<KW2 - NS2>{});
      if (!poll2_check<1>(f, c < 14, tlo, thi)) poll_flags2<PSLEEP, 1>(lane * 16, c < 14, srdWS, so, tlo, thi);
    }
    asm volatile("s_barrier" ::: "memory");
    STAMPB(4);
    if (w != 0 && g1) gemm_start<false, true, NA>(A2, W2, make_iseq<DF2 / 4>{});
    else gemm_start<false, false, NA>(A2, W2, make_iseq<DF2 / 4>{});
    STAMPB(3);
    GAP();
    asm volatile("s_waitcnt vmcnt(%4) lgkmcnt(0)" : "+v"(bv[0]), "+v"(bv[1]), "+v"(bv[2]), "+v"(bv[3]) : "i"(DF2) : "memory");
    f32x4 s[4];
#pragma unroll
    for (int t = 0; t < 4; ++t) { const float bb = bflo(bv[t]); s[t] = f32x4{bb, bb, bb, bb}; }
    GAP();
    gemm_body<false>(s, A2, H2, X);
    STAMPB(5);
    unsigned char* stage = smem + W2_OFF;
#pragma unroll
    for (int t = 0; t < 4; ++t) *(LAS v2u*)((LAS unsigned char*)stage + (16 * t + c) * 144 + 32 * w + 8 * g) = v2u{pack2(s[t][0], s[t][1]), pack2(s[t][2], s[t][3])};
    wg_barrier();
    const srd_t srdO = mk_srd(out, S_ * D_ * 2);
    const unsigned row = 16 * w + (lane >> 3), col = (lane & 7) * 16;
#pragma unroll
    for (int mp = 0; mp < 2; ++mp) {
      const v4u o = *(const LAS v4u*)((LAS unsigned char*)stage + (row + 8 * mp) * 144 + col);
      asm volatile("buffer_store_dwordx4 %0, %1, %2, %3 offen\n\ts_nop 1" ::"v"(o), "v"((row + 8 * mp) * (D_ * 2) + col), "s"(srdO), "s"(nb2 * 64 * (D_ * 2) + mb2 * 128) : "memory");
    }
  }
done:
  STAMP(7);
}

extern "C" void kernel_launch(void* const* d_in, const int* in_sizes, int n_in, void* d_out, int out_size, void* d_ws, size_t ws_size, hipStream_t stream) {
  mixer_kernel<<<256, 256, 0, stream>>>((const u16*)d_in[0], (const u16*)d_in[1], (const u16*)d_in[2], (const u16*)d_in[3], (const u16*)d_in[4],
                                       (u16*)d_out, (unsigned char*)d_ws);
}
```
